```python
import torch

def _softmax(x, axis=-1):
    return torch.softmax(x, dim=axis)
def _gelu(x):
    return torch.nn.functional.gelu(x, approximate="tanh")
def _silu(x):
    return torch.nn.functional.silu(x)
def _sigmoid(x):
    return torch.sigmoid(x)
def _layernorm(x, axis=-1, eps=1e-5):
    m = x.mean(dim=axis, keepdim=True)
    v = x.var(dim=axis, keepdim=True, unbiased=False)
    return (x - m) / torch.sqrt(v + eps)
def _scatter_add(tgt, idx, src, axis=0):
    return tgt.clone().index_add_(axis, idx, src)
def _segment_sum(src, seg, n):
    out = torch.zeros((n,) + src.shape[1:], dtype=src.dtype)
    return out.index_add_(0, seg, src)
def _pad(x, pw):
    flat = tuple(v for pair in reversed(pw) for v in pair)
    return torch.nn.functional.pad(x, flat)

kernel_name = 'gather_scatter__molecular_angular_gemnet__graph'

def setup_inputs():

    _w = (lambda a: (lambda t: t.to(torch.bfloat16) if t.is_floating_point() else t)(torch.as_tensor(a)))
    N, deg, De, L = 10000, 64, 4, 1
    E = N * deg
    T = E * deg
    NR = 8
    dist = torch.rand(E).to(float)
    angle = (torch.rand(T) * torch.pi).to(float)
    centers = torch.linspace(0.0, 1.0, NR).to(float)
    ang_freq = torch.arange(1, L + 1).to(float)
    return {
        "m": _w((lambda *s: torch.randn(*s, dtype=torch.bfloat16) * 0.05)(E, De)),
        "idx_in": _w((lambda lo, hi, *s: torch.randint(lo, hi, s))(0, E, T).to(torch.int64)),
        "idx_out": _w((lambda lo, hi, *s: torch.randint(lo, hi, s))(0, E, T).to(torch.int64)),
        "dist": _w(dist),
        "angle": _w(angle),
        "centers": _w(centers),
        "ang_freq": _w(ang_freq),
        "w_rbf": _w((lambda *s: torch.randn(*s, dtype=torch.bfloat16) * 0.2)(NR, De)),
        "w_sbf": _w((lambda *s: torch.randn(*s, dtype=torch.bfloat16) * 0.2)(NR * L, De)),
        "w_down": _w((lambda *s: torch.randn(*s, dtype=torch.bfloat16) * 0.2)(De, De)),
        "w_up": _w((lambda *s: torch.randn(*s, dtype=torch.bfloat16) * 0.2)(De, De)),
    }

def reference(m, idx_in, idx_out, dist, angle, centers, ang_freq, w_rbf, w_sbf, w_down, w_up):
    E, De = m.shape[0], m.shape[1]
    NR = centers.shape[0]
    L = ang_freq.shape[0]
    rbf = torch.exp(-10.0 * (dist[:, None] - centers[None, :]) ** 2)
    cbf = torch.cos(ang_freq[None, :] * angle[:, None])
    rbf_t = rbf[idx_out]
    sbf = torch.reshape(rbf_t[:, :, None] * cbf[:, None, :], (-1, NR * L))
    x = (m[idx_in] @ w_down) * (sbf @ w_sbf)
    agg = _segment_sum(x, idx_out, E)
    return m + (agg @ w_up) * (rbf @ w_rbf)
```

```cpp
#include <hip/hip_runtime.h>
#include <hip/hip_bf16.h>
#include <stdint.h>

typedef int v4i __attribute__((ext_vector_type(4)));
typedef unsigned v4u __attribute__((ext_vector_type(4)));
typedef unsigned v2u __attribute__((ext_vector_type(2)));
typedef int v4i_rs __attribute__((ext_vector_type(4)));
typedef __attribute__((address_space(3))) unsigned lds_u32;
typedef __attribute__((address_space(3))) v4u lds_v4u;

static __device__ __forceinline__ float bf2f(unsigned short v) { return __uint_as_float(((unsigned)v) << 16); }
static __device__ __forceinline__ unsigned f2bf_bits(float f) {
    unsigned u = __float_as_uint(f);
    u += 0x7FFF + ((u >> 16) & 1);
    return u >> 16;
}
static __device__ __forceinline__ float lo_bf(unsigned u) { return __uint_as_float(u << 16); }
static __device__ __forceinline__ float hi_bf(unsigned u) { return __uint_as_float(u & 0xFFFF0000u); }
static __device__ __forceinline__ v4i_rs mk_rsrc(const void* p, unsigned bytes) {
    unsigned long long a = (unsigned long long)p;
    v4i_rs r = {(int)(unsigned)a, (int)(unsigned)((a >> 32) & 0xFFFF), (int)bytes, 0x00020000};
    return r;
}
static __device__ __forceinline__ lds_u32* ldsp(unsigned byte) { return (lds_u32*)(size_t)byte; }

#define P1_THREADS 1024
#define PD 1
#define UNR ((PD & 1) ? 2 * PD : PD)
#define P1_GRID 256
#define NWS 256
#define CH (P1_THREADS * 4)
#define NT 1024
#define LMAX 20480
#define P2_THREADS 1024
#define OOB 0x80000000u
#define OBASE 20480
#define IDXB 4096
#define ST_AUX 16
#define LB 4096
#define LS 132
#define TINC 683
#define WL_OFF (LB + NT * LS)
#define MS_OFF (WL_OFF + 16 * 512)
#define BIN_LDS (MS_OFF + 128 + 320)
#define NSTAT 14
#define WM_CTR 1024
#define WM_BOX (WM_CTR + 256)
#define RM 1
#define RXOR 0
#define CALN 3
#define VTHR 5
#define CORD 0
#define PSTR 256
#define NTH "nt"
#define OUT_AUX 16
#define PR_AUX 18
#define TCH 7
#define CZERO ((512u << 1) | (512u << 11) | (512u << 22))
#define FL_SIZE (2 * P1_GRID * 2)

struct Grp { v4i io; v4i ii; v2u an; };
static __device__ __forceinline__ void load_grp(Grp& r, v4i_rs ro, v4i_rs ri, v4i_rs ra, unsigned c, unsigned vo16, unsigned vo8) {
    asm volatile("buffer_load_dwordx4 %0, %1, %2, %3 offen nt" : "=v"(r.io) : "v"(vo16), "s"(ro), "s"(c * 1024));
    asm volatile("buffer_load_dwordx4 %0, %1, %2, %3 offen nt" : "=v"(r.ii) : "v"(vo16), "s"(ri), "s"(c * 1024));
    asm volatile("buffer_load_dwordx2 %0, %1, %2, %3 offen nt" : "=v"(r.an) : "v"(vo8), "s"(ra), "s"(c * 512));
}
#define WAIT_GRP(n, g) asm volatile("s_waitcnt vmcnt(%3)" : "+v"((g).io), "+v"((g).ii), "+v"((g).an) : "n"(n))

static __device__ __forceinline__ unsigned wave_scan_max(unsigned x) {
    x = max(x, (unsigned)__builtin_amdgcn_update_dpp(0, (int)x, 0x111, 0xF, 0xF, false));
    x = max(x, (unsigned)__builtin_amdgcn_update_dpp(0, (int)x, 0x112, 0xF, 0xF, false));
    x = max(x, (unsigned)__builtin_amdgcn_update_dpp(0, (int)x, 0x114, 0xF, 0xF, false));
    x = max(x, (unsigned)__builtin_amdgcn_update_dpp(0, (int)x, 0x118, 0xF, 0xF, false));
    x = max(x, (unsigned)__builtin_amdgcn_update_dpp(0, (int)x, 0x142, 0xA, 0xF, false));
    x = max(x, (unsigned)__builtin_amdgcn_update_dpp(0, (int)x, 0x143, 0xC, 0xF, false));
    return x;
}
static __device__ __forceinline__ unsigned row_scan_add(unsigned x) {
    x += (unsigned)__builtin_amdgcn_update_dpp(0, (int)x, 0x111, 0xF, 0xF, false);
    x += (unsigned)__builtin_amdgcn_update_dpp(0, (int)x, 0x112, 0xF, 0xF, false);
    x += (unsigned)__builtin_amdgcn_update_dpp(0, (int)x, 0x114, 0xF, 0xF, false);
    x += (unsigned)__builtin_amdgcn_update_dpp(0, (int)x, 0x118, 0xF, 0xF, false);
    return x;
}
static __device__ __forceinline__ unsigned wave_scan_add(unsigned x) {
    x = row_scan_add(x);
    x += (unsigned)__builtin_amdgcn_update_dpp(0, (int)x, 0x142, 0xA, 0xF, false);
    x += (unsigned)__builtin_amdgcn_update_dpp(0, (int)x, 0x143, 0xC, 0xF, false);
    return x;
}

static __device__ __forceinline__ float wave_scan_addf(float x) {
    x += __int_as_float(__builtin_amdgcn_update_dpp(0, __float_as_int(x), 0x111, 0xF, 0xF, false));
    x += __int_as_float(__builtin_amdgcn_update_dpp(0, __float_as_int(x), 0x112, 0xF, 0xF, false));
    x += __int_as_float(__builtin_amdgcn_update_dpp(0, __float_as_int(x), 0x114, 0xF, 0xF, false));
    x += __int_as_float(__builtin_amdgcn_update_dpp(0, __float_as_int(x), 0x118, 0xF, 0xF, false));
    x += __int_as_float(__builtin_amdgcn_update_dpp(0, __float_as_int(x), 0x142, 0xA, 0xF, false));
    x += __int_as_float(__builtin_amdgcn_update_dpp(0, __float_as_int(x), 0x143, 0xC, 0xF, false));
    return x;
}

struct Lay { unsigned F0, cap, dumu, fcap; };
#define SHP 2
#define LA 3
#define XORD 0
#define NPOS 10
#define LMUL (8 + 32768)
#define LMASK ((((1 << LA) - 1) << 3) | ((1023 >> LA) << (15 + LA)))
static __device__ __forceinline__ char* rec_align(const char* p) { return (char*)(((size_t)p + 0x1FFFFF) & ~(size_t)0x1FFFFF); }
static __device__ __forceinline__ unsigned pairidx(unsigned w) { return XORD & 2 ? (w & 3) * 32 + (w >> 3) : (w >> 3) * 4 + (w & 3); }
static __device__ __forceinline__ unsigned logbase(unsigned wave, unsigned w) {
    const unsigned x = XORD & 1 ? wave * 128 + pairidx(w) : pairidx(w) * 16 + wave;
    return ((x & ((64 >> LA) - 1)) << (3 + LA)) | ((x >> (6 - LA)) << 10);
}

static __device__ __forceinline__ void conv_rec(unsigned io, unsigned ii, float ang, float frev, unsigned& A, unsigned& t4, unsigned& cq, unsigned& seen) {
    unsigned x, y;
    asm("v_and_b32 %0, 31, %4\n v_and_b32 %1, 0xf80, %3\n v_lshl_or_b32 %2, %0, 2, %1" : "=&v"(x), "=&v"(y), "=v"(t4) : "v"(io), "v"(ii));
    asm("v_lshrrev_b32 %0, 5, %3\n v_lshrrev_b32 %1, 3, %4\n v_bfi_b32 %0, %5, %3, %0\n v_add_u32 %1, %6, %1\n v_lshl_add_u32 %2, %0, 17, %1"
        : "=&v"(x), "=&v"(y), "=v"(A) : "v"(io), "v"(ii), "s"(127u), "n"(IDXB << 17));
    float c = __builtin_amdgcn_cosf(__builtin_amdgcn_fractf(frev * ang));
    const unsigned cb = __float_as_uint(__builtin_fmaf(c, 511.0f, 12583424.0f));
    seen |= cb;
    cq = cb & 0x3FFu;
}
#define PADV 1
static __device__ __forceinline__ unsigned pad_rec(unsigned q) { return PADV == 0 ? 0x7FFFu << 17 : PADV == 1 ? (0x7FFFu - (q & 511)) << 17 : (IDXB + (q & 511)) << 17; }

static __device__ __forceinline__ void prep_md(const v2u* mv, unsigned dst, const unsigned short* w_down, unsigned* mdp, unsigned plane, unsigned* mx, float* sq) {
    float wd[16];
#pragma unroll
    for (int i = 0; i < 8; i++) { unsigned u = ((const __attribute__((address_space(4))) unsigned*)w_down)[i]; wd[2 * i] = lo_bf(u); wd[2 * i + 1] = hi_bf(u); }
    unsigned o[2][4];
#pragma unroll
    for (int j = 0; j < 4; j++) {
        const float a[4] = {lo_bf(mv[j].x), hi_bf(mv[j].x), lo_bf(mv[j].y), hi_bf(mv[j].y)};
        float s[4];
#pragma unroll
        for (int d = 0; d < 4; d++) {
            s[d] = 0.f;
#pragma unroll
            for (int k = 0; k < 4; k++) s[d] += a[k] * wd[k * 4 + d];
            sq[d] += s[d] * s[d];
            mx[d] = max(mx[d], __float_as_uint(s[d]) & 0x7FFFFFFFu);
        }
#pragma unroll
        for (int p = 0; p < 2; p++) {
            const unsigned lo = f2bf_bits(s[2 * p]) & 0xFFFF;
            const unsigned sb = __float_as_uint(s[2 * p + 1]);
            const unsigned h = (unsigned)max((int)((sb & 0x7FFFFFFFu) - lo + 0x8000u), 0) & 0x7FFF0000u;
            o[p][j] = lo | h | (sb & 0x80000000u);
        }
    }
#pragma unroll
    for (int p = 0; p < 2; p++) {
        v4u v = {o[p][0], o[p][1], o[p][2], o[p][3]};
        *(v4u*)(mdp + (size_t)p * plane + dst) = v;
    }
}

static __device__ __forceinline__ unsigned touch_code(int tid) {
    unsigned alo, ahi, blo, r = 0;
    asm volatile("s_getpc_b64 s[96:97]\n s_add_u32 %0, s96, k_bin@rel32@lo+4\n s_addc_u32 %1, s97, k_bin@rel32@hi+12\n"
                 "s_getpc_b64 s[96:97]\n s_add_u32 %2, s96, k_naive@rel32@lo+4\n" : "=&s"(alo), "=&s"(ahi), "=&s"(blo) :: "scc", "s96", "s97");
    const unsigned size = blo - alo < 65536u ? blo - alo : 0u;
    const v4i_rs rc = {(int)alo, (int)(ahi & 0xFFFF), (int)size, 0x00020000};
    if (tid < 512) asm volatile("buffer_load_dword %0, %1, %2, 0 offen" : "=v"(r) : "v"(tid * 128), "s"(rc));
    return r;
}

struct Held { unsigned a[4], t[4], A[4], cd[4]; unsigned long long m[4]; };
struct Conv { unsigned A[4], t4[4], cq[4]; };
#define HB(k) "s_mov_b64 exec, %[m" #k "]\n ds_write_b32 %[a" #k "], %[A" #k "]\n ds_or_b32 %[t" #k "], %[c" #k "]\n"
static __device__ __forceinline__ void held_out(const Held& H) {
    asm volatile(HB(0) HB(1) HB(2) HB(3) "s_mov_b64 exec, -1\n"
                 :: [m0] "s"(H.m[0]), [m1] "s"(H.m[1]), [m2] "s"(H.m[2]), [m3] "s"(H.m[3]), [a0] "v"(H.a[0]), [a1] "v"(H.a[1]), [a2] "v"(H.a[2]), [a3] "v"(H.a[3]),
                   [t0] "v"(H.t[0]), [t1] "v"(H.t[1]), [t2] "v"(H.t[2]), [t3] "v"(H.t[3]),
                   [A0] "v"(H.A[0]), [A1] "v"(H.A[1]), [A2] "v"(H.A[2]), [A3] "v"(H.A[3]), [c0] "v"(H.cd[0]), [c1] "v"(H.cd[1]), [c2] "v"(H.cd[2]), [c3] "v"(H.cd[3])
                 : "memory");
}
static __device__ __forceinline__ void tickets(unsigned* v, const Conv& c, unsigned inc) {
    asm volatile("ds_add_rtn_u32 %0, %4, %8\n ds_add_rtn_u32 %1, %5, %8\n ds_add_rtn_u32 %2, %6, %8\n ds_add_rtn_u32 %3, %7, %8\n"
                 : "=&v"(v[0]), "=&v"(v[1]), "=&v"(v[2]), "=&v"(v[3]) : "v"(c.t4[0]), "v"(c.t4[1]), "v"(c.t4[2]), "v"(c.t4[3]), "v"(inc) : "memory");
}
static __device__ __forceinline__ void tickets_masked(unsigned* v, const Conv& c, unsigned inc, const unsigned long long* vm) {
#pragma unroll
    for (int k = 0; k < 4; k++) {
        v[k] = 0;
        asm volatile("s_mov_b64 exec, %3\n ds_add_rtn_u32 %0, %1, %2\n s_mov_b64 exec, -1" : "+v"(v[k]) : "v"(c.t4[k]), "v"(inc), "s"(vm[k]) : "memory");
    }
}
static __device__ __forceinline__ void place(Held& H, unsigned* v, Conv& c, const unsigned long long* vm, unsigned lb) {
    unsigned bt[4];
#pragma unroll
    for (int k = 0; k < 4; k++) asm("v_mad_u32_u24 %0, %1, %2, %3" : "=v"(bt[k]) : "v"(c.t4[k]), "n"(LS / 4), "v"(lb));
    asm volatile("s_waitcnt lgkmcnt(0)" : "+v"(v[0]), "+v"(v[1]), "+v"(v[2]), "+v"(v[3]));
#pragma unroll
    for (int k = 0; k < 4; k++) {
        H.m[k] = __ballot(v[k] >= 16384u);
        unsigned sh;
        asm("v_lshrrev_b32 %0, 7, %3\n v_lshrrev_b32 %2, 6, %3\n v_and_b32 %0, 0x7c, %0\n v_or_b32 %1, 12, %0\n v_add_u32 %0, %0, %4\n v_add_u32 %1, %1, %4"
            : "=&v"(H.a[k]), "=&v"(H.t[k]), "=&v"(sh) : "v"(v[k]), "v"(bt[k]));
        H.A[k] = c.A[k];
        asm("v_lshlrev_b32 %0, %1, %2" : "=v"(H.cd[k]) : "v"(sh), "v"(c.cq[k]));
        asm volatile("s_andn2_b64 exec, %[w], %[m]\n ds_write_b32 %[a], %[A]\n ds_or_b32 %[t], %[c]\n s_mov_b64 exec, -1\n"
                     :: [w] "s"(vm[k]), [m] "s"(H.m[k]), [a] "v"(H.a[k]), [A] "v"(H.A[k]), [t] "v"(H.t[k]), [c] "v"(H.cd[k]) : "memory", "scc");
    }
}
struct Own { unsigned la, p[4], nl, ovf; };
static __device__ __forceinline__ v4u read_unit(unsigned la) {
    const lds_u32* p = ldsp(la);
    v4u v = {p[0], p[1], p[2], p[3]};
    return v;
}
typedef __attribute__((address_space(3))) v2u lds_v2u;
static __device__ __forceinline__ unsigned long long low_lanes(unsigned groups) { return groups >= 8 ? ~0ull : (1ull << (groups * 8)) - 1; }
static __device__ __forceinline__ void flush_pass(unsigned e, unsigned f, v4u v, unsigned long long act, unsigned base, __amdgpu_buffer_rsrc_t rr) {
    unsigned vo = (__umul24(f, LMUL * 16) & (LMASK * 16u)) | base;
    asm volatile("s_mov_b64 exec, %1\n ds_write_b32 %2, %3 offset:12\n s_mov_b64 exec, -1\n v_cndmask_b32 %0, %4, %0, %1"
                 : "+v"(vo) : "s"(act), "v"(e), "v"(0u), "v"(OOB) : "memory");
    __builtin_amdgcn_raw_buffer_store_b128(v, rr, vo, 0, ST_AUX);
}
static __device__ __forceinline__ void flush(Own& o, unsigned& wc, unsigned cnt, int tid, unsigned base, __amdgpu_buffer_rsrc_t rr) {
    const bool full = cnt >= 16384u;
    const unsigned long long bal = __ballot(full);
    const unsigned nfull = __popcll(bal);
    const unsigned wl = WL_OFF + (tid >> 6) * 512;
    if (full) {
        const unsigned pos = __builtin_amdgcn_mbcnt_hi((unsigned)(bal >> 32), __builtin_amdgcn_mbcnt_lo((unsigned)bal, 0));
        *ldsp(wl + pos * 4) = o.la;
        cnt -= 24 * TINC;
        *ldsp(tid * 4) = cnt;
        o.ovf |= cnt;
        o.nl++;
        o.p[3] = __builtin_amdgcn_alignbit(o.p[3], o.p[2], 22); o.p[2] = __builtin_amdgcn_alignbit(o.p[2], o.p[1], 22); o.p[1] = __builtin_amdgcn_alignbit(o.p[1], o.p[0], 22);
        o.p[0] = (o.p[0] << 10) + pos + wc;
    }
    const unsigned g = (tid >> 3) & 7, k16 = (tid & 7) * 16;
    const unsigned e0 = *ldsp(wl + g * 4) + k16, e1 = *ldsp(wl + g * 4 + 32) + k16;
    const v4u v0 = read_unit(e0), v1 = read_unit(e1);
    asm volatile("" ::: "memory");
    flush_pass(e0, g + wc, v0, low_lanes(nfull), base, rr);
    flush_pass(e1, g + wc + 8, v1, nfull > 8 ? low_lanes(nfull - 8) : 0ull, base, rr);
    for (unsigned p = 16; p < nfull; p += 8) {
        const unsigned e = *ldsp(wl + (p + g) * 4) + k16;
        const v4u v = read_unit(e);
        asm volatile("" ::: "memory");
        flush_pass(e, g + wc + p, v, low_lanes(nfull - p), base, rr);
    }
    wc = min(wc + nfull, 960u);
    asm volatile("s_waitcnt lgkmcnt(0)" ::: "memory");
}

extern "C" __global__ __launch_bounds__(P1_THREADS) void k_bin(const int* __restrict__ idx_in, const int* __restrict__ idx_out,
                                                    const unsigned short* __restrict__ angle, const unsigned short* __restrict__ ang_freq,
                                                    const unsigned short* __restrict__ m, const unsigned short* __restrict__ w_down,
                                                    unsigned* __restrict__ mdp, int E, int T,
                                                    char* __restrict__ rec0, unsigned* __restrict__ tab, unsigned* __restrict__ flags,
                                                    int NR, int per, Lay ly, unsigned* __restrict__ wgmode) {
    __shared__ __attribute__((aligned(16))) unsigned L[BIN_LDS / 4];
    const int tid = threadIdx.x;
    const unsigned run = blockIdx.x;
    char* rec = rec_align(rec0);
    unsigned tc0 = touch_code(tid);
    unsigned fbits;
    asm volatile("buffer_load_ushort %0, off, %1, 0" : "=v"(fbits) : "s"(mk_rsrc(ang_freq, 2)));
    float freq;
    const v4i_rs ro = mk_rsrc(idx_out, (unsigned)T * 4), ri = mk_rsrc(idx_in, (unsigned)T * 4), ra = mk_rsrc(angle, (unsigned)T * 2);
    const __amdgpu_buffer_rsrc_t rr = __builtin_amdgcn_make_buffer_rsrc((void*)rec, (short)0,
        (int)(ly.dumu * 16),
        0x00020000);
    const unsigned vo16 = (tid >> (8 - SHP)) * 32768 + (tid & ((256 >> SHP) - 1)) * 16, vo8 = vo16 >> 1;
    auto chunk = [&](unsigned c) { const unsigned G = c >> 3; return (G >> SHP) * (128 << SHP) + ((c ^ RXOR) & 7) * 4 + (G & ((1 << SHP) - 1)) * (4 >> SHP); };
#define NSH 4
    const unsigned shard = (run >> 3) & 3, member = (run & 7) | ((run >> 5) << 3);
    const bool leader = member == 0;
    const v4i_rs rw = mk_rsrc(wgmode + WM_CTR, (256 + 256) * 4);
    const int wave0 = __builtin_amdgcn_readfirstlane(tid >> 6) == 0;
    const unsigned ctr_o = shard * 128, box_o = (256 + shard * (256 / NSH) + member) * 4;
    if (wave0) asm volatile("buffer_store_dword %0, %1, %2, 0 offen sc1" :: "v"(0u), "v"(tid == 0 ? (leader ? ctr_o : box_o) : OOB), "s"(rw) : "memory");
    unsigned mail = NR < NSTAT ? 24u : 0u;
    if (leader && NR >= NSTAT) {
        const v4i_rs rp = mk_rsrc(rec, ly.dumu * 16);
        const unsigned pbase = __builtin_amdgcn_readfirstlane(logbase(tid >> 6, run) + (((ly.F0 + (tid >> 6) * 24) * LMUL) & LMASK)) * 16;
        unsigned x = 0, xcc;
        asm volatile("buffer_atomic_add %0, %1, %2, %3 offen sc0" : "+v"(x) : "v"((tid & 63) < 2 * CALN ? (tid & 1) * 8192 + ((tid & 63) >> 1) * PSTR : OOB), "s"(rp), "s"(pbase));
        asm volatile("s_getreg_b32 %0, hwreg(HW_REG_XCC_ID)" : "=s"(xcc));
        unsigned mn[2] = {~0u, ~0u};
        unsigned long long ta, tb;
        asm volatile("s_waitcnt vmcnt(0)\n s_memtime %1" : "+v"(x), "=s"(ta));
#pragma unroll
        for (int i = 0; i < 2 * CALN; i++) {
            const int sd = (CORD ? i ^ (i >> 1) : i) & 1;
            asm volatile("v_mov_b32 %0, 0\n buffer_atomic_add %0, %5, %3, %4 offen sc0\n s_waitcnt vmcnt(0)\n s_memtime %1\n s_waitcnt lgkmcnt(0)"
                         : "+v"(x), "=s"(tb), "+s"(ta) : "s"(rp), "s"(pbase + sd * 8192 + (i >> 1) * PSTR), "v"((tid & 63) ? OOB : 0u));
            mn[sd] = min(mn[sd], (unsigned)(tb - ta));
            ta = tb;
        }
        *ldsp(WL_OFF + (tid >> 6) * 4) = (mn[1] < mn[0]) ^ ((pbase >> 21) & 1);
        __syncthreads();
        const unsigned votes = (unsigned)__ballot(*ldsp(WL_OFF + (tid & 15) * 4) != 0) & 0xFFFF, nv = __popc(votes);
        mail = 8 | (xcc & 3) | (nv >= 8 ? 4 : 0) | (nv > VTHR && nv < 16 - VTHR ? 16 : 0);
        if (wave0) asm volatile("buffer_store_dword %0, %1, %2, 0 offen sc1" :: "v"(mail), "v"((256 + shard * 64 + tid) * 4), "s"(rw) : "memory");
        __syncthreads();
    }
    v2u mv[4];
    const unsigned gp = run * 80 + (tid >> 5) * 4;
    {
        const v4i_rs rm = mk_rsrc(m, (unsigned)E * 8);
        const unsigned o = tid < 640 ? (((gp & ~255u) | ((gp >> 2) & 63)) * 32 + (tid & 31)) * 8 : OOB;
#pragma unroll
        for (int j = 0; j < 4; j++) asm volatile("buffer_load_dwordx2 %0, %1, %2, %3 offen " NTH : "=v"(mv[j]) : "v"(o), "s"(rm), "s"(j * 64 * 32 * 8));
    }
    Grp tg; bool tv[4];
    const unsigned tbase = (unsigned)NR * (P1_GRID * CH) + run * per;
    {
        const bool in = tid * 4 < per;
#pragma unroll
        for (int k = 0; k < 4; k++) tv[k] = in && tbase + tid * 4 + k < (unsigned)T;
        const unsigned o16 = in ? tid * 16 : OOB, o8 = in ? tid * 8 : OOB;
        asm volatile("buffer_load_dwordx4 %0, %1, %2, %3 offen nt" : "=v"(tg.io) : "v"(o16), "s"(ro), "s"(tbase * 4));
        asm volatile("buffer_load_dwordx4 %0, %1, %2, %3 offen nt" : "=v"(tg.ii) : "v"(o16), "s"(ri), "s"(tbase * 4));
        asm volatile("buffer_load_dwordx2 %0, %1, %2, %3 offen nt" : "=v"(tg.an) : "v"(o8), "s"(ra), "s"(tbase * 2));
    }
    const unsigned NFULL = (unsigned)NR * P1_GRID;
    Grp g0, cur[1];
    load_grp(g0, ro, ri, ra, NR > 0 ? chunk(run) : 0xFFFFFu, vo16, vo8);
    load_grp(cur[0], ro, ri, ra, NR > 1 ? chunk(run + P1_GRID) : 0xFFFFFu, vo16, vo8);
    Own own;
    own.la = LB + tid * LS; own.nl = 0; own.p[0] = own.p[1] = own.p[2] = own.p[3] = 0;
    unsigned wc = ly.F0;
    unsigned base = OOB;
    auto sided = [&]() {
        const unsigned lb0 = logbase(tid >> 6, run), sw = (((run + mail) >> 2) ^ (mail & 16 ? tid >> 6 : lb0 >> 17)) & 1;
        base = ((lb0 | (sw << 9)) + (tid & 7)) * 16;
    };
    auto await = [&]() {
        for (int i = 0; i < 4096 && !mail; i++) {
            unsigned x = 0;
            asm volatile("buffer_atomic_add %0, %1, %2, 0 offen sc0\n s_waitcnt vmcnt(0)" : "+v"(x) : "v"((tid & 63) ? OOB : box_o), "s"(rw) : "memory");
            mail = __builtin_amdgcn_readfirstlane(x);
        }
        if (mail) sided(); else { mail = 1; own.ovf |= 16384u; }
    };
    if (mail) sided();
    own.ovf = (unsigned)(size_t)L << 14;
    *ldsp(tid * 4) = 64;
#pragma unroll
    for (int i = 0; i < 8; i++) *ldsp(LB + tid * LS + i * 16 + 12) = 0;
    { const v2u z = {0, 0}; *(lds_v2u*)(size_t)(WL_OFF + tid * 8) = z; }
    if (tid < 32) *ldsp(MS_OFF + tid * 4) = 0;
    if (run == 0 && tid < 64) wgmode[(tid >> 1) * 32 + (tid & 1)] = 0;
    if (run == 1 && tid < 64) {
        v4u v = {pad_rec(tid), pad_rec(tid + 64), pad_rec(tid + 128), CZERO};
        *(v4u*)(rec + ((size_t)ly.dumu + tid) * 16) = v;
    }
    Conv cv[2];
    unsigned seen = 0;
    auto convert = [&](Grp& gp, Conv& o) {
#pragma unroll
        for (int k = 0; k < 4; k++) {
            unsigned io = k == 0 ? gp.io.x : k == 1 ? gp.io.y : k == 2 ? gp.io.z : gp.io.w;
            unsigned ii = k == 0 ? gp.ii.x : k == 1 ? gp.ii.y : k == 2 ? gp.ii.z : gp.ii.w;
            unsigned aw = (k & 2) ? gp.an.y : gp.an.x;
            float ang = (k & 1) ? hi_bf(aw) : lo_bf(aw);
            conv_rec(io, ii, ang, freq, o.A[k], o.t4[k], o.cq[k], seen);
        }
#pragma unroll
        for (int k = 0; k < 4; k++) asm volatile("" : "+v"(o.A[k]), "+v"(o.t4[k]), "+v"(o.cq[k]));
    };
    const unsigned tinc = TINC, lb7 = LB;
    Held H;
    {
        asm volatile("s_waitcnt vmcnt(9)" : "+v"(mv[0]), "+v"(mv[1]), "+v"(mv[2]), "+v"(mv[3]));
        if (tid < 640) {
            unsigned mx[4] = {0, 0, 0, 0};
            float sq[4] = {0.f, 0.f, 0.f, 0.f};
            prep_md(mv, (tid & 31) * LMAX + gp, w_down, mdp, 32 * LMAX, mx, sq);
#pragma unroll
            for (int d = 0; d < 4; d++) {
                const unsigned v = wave_scan_max(mx[d]);
                const float s = wave_scan_addf(sq[d]);
                if ((tid & 63) == 63) { *ldsp(MS_OFF + 128 + (tid >> 6) * 32 + d * 4) = v; *ldsp(MS_OFF + 128 + (tid >> 6) * 32 + 16 + d * 4) = __float_as_uint(s); }
            }
        }
        asm volatile("s_waitcnt vmcnt(3)" : "+v"(tg.io), "+v"(tg.ii), "+v"(tg.an), "+v"(fbits),
                     "+v"(g0.io), "+v"(g0.ii), "+v"(g0.an));
        asm volatile("" :: "v"(tc0));
        freq = lo_bf(fbits) * 0.15915494309189535f;
        if (T & 1) {
            const unsigned rel = (unsigned)T - 1 - (tbase + tid * 4);
            if (tv[0] && rel < 4) { const unsigned av = angle[T - 1]; if (rel == 0) tg.an.x = av; else tg.an.y = av; }
        }
        __syncthreads();
        unsigned long long vm[4];
#pragma unroll
        for (int k = 0; k < 4; k++) vm[k] = __ballot(tv[k]);
        if (vm[0]) {
            unsigned v[4];
            convert(tg, cv[1]);
            tickets_masked(v, cv[1], tinc, vm);
            place(H, v, cv[1], vm, lb7);
            if ((H.m[0] & vm[0]) | (H.m[1] & vm[1]) | (H.m[2] & vm[2]) | (H.m[3] & vm[3])) own.ovf |= 16384u;
        }
        convert(g0, cv[0]);
#pragma unroll
        for (int k = 0; k < 4; k++) { H.m[k] = 0; H.a[k] = 0; H.t[k] = 0; H.A[k] = 0; H.cd[k] = 0; }
        asm volatile("s_waitcnt vmcnt(0)" ::: "memory");
    }
    const unsigned long long vm[4] = {~0ull, ~0ull, ~0ull, ~0ull};
    unsigned ca = run + P1_GRID, cb = run + 2 * P1_GRID;
    unsigned got = 0;
    int st = leader ? 1 : 0;
    if (NR == 0) { asm volatile("s_waitcnt lgkmcnt(0)" ::: "memory"); __syncthreads(); }
    for (int rd = 0; NR > 0; rd += 2) {
        bool done = false;
#pragma unroll
        for (int r = 0; r < 2; r++) {
            unsigned v[4];
            tickets(v, cv[r & 1], tinc);
            held_out(H);
            asm volatile("s_waitcnt vmcnt(2)" : "+v"(cur[0].io), "+v"(cur[0].ii), "+v"(cur[0].an), "+v"(got));
            convert(cur[0], cv[(r + 1) & 1]);
            if (wave0) {
                const unsigned g = __builtin_amdgcn_readfirstlane(got);
                unsigned nx = cb + P1_GRID;
                if (rd + r + 3 >= NSTAT) nx = st == 2 ? NSTAT * P1_GRID + g * NSH + shard : NFULL;
                if (st == 0 && g != 0) { st = 1; if (!mail) { mail = g; sided(); } }
                if (st == 0 && rd + r + 4 == NSTAT) {
                    for (int i = 0; i < 128 && st == 0; i++) {
                        unsigned x = 0;
                        asm volatile("buffer_atomic_add %0, %1, %2, 0 offen sc0\n s_waitcnt vmcnt(0)" : "+v"(x) : "v"(tid == 0 ? box_o : OOB), "s"(rw) : "memory");
                        if (__builtin_amdgcn_readfirstlane(x) != 0) st = 1;
                    }
                }
                if (st == 1 && rd + r + 4 >= NSTAT) st = 2;
                nx = cb < NFULL ? min(nx, NFULL) : NFULL;
                if ((tid & 63) == 0) { const v2u w2 = {nx, mail}; *(lds_v2u*)(size_t)(MS_OFF + 104) = w2; }
                got = st == 2 ? 1u : 0u;
                if (st != 1 && nx < NFULL)
                    asm volatile("buffer_atomic_add %0, %1, %2, 0 offen sc0" : "+v"(got) : "v"(tid == 0 ? (st == 2 ? ctr_o : box_o) : OOB), "s"(rw) : "memory");
                if (leader) asm volatile("buffer_store_dword %0, %1, %2, 0 offen sc1" :: "v"(mail), "v"(tid < 256 / NSH ? (256 + shard * (256 / NSH) + tid) * 4 : OOB), "s"(rw) : "memory");
            }
            load_grp(cur[0], ro, ri, ra, cb < NFULL ? chunk(cb) : 0xFFFFFu, vo16, vo8);
            place(H, v, cv[r & 1], vm, lb7);
            asm volatile("s_waitcnt lgkmcnt(0)" ::: "memory");
            __syncthreads();
            const unsigned cnt = *ldsp(tid * 4);
            const unsigned nx = *ldsp(MS_OFF + 104);
            if (!mail) {
                mail = __builtin_amdgcn_readfirstlane(*ldsp(MS_OFF + 108));
                if (mail) sided(); else if (__ballot(cnt >= 16384u)) await();
            }
            flush(own, wc, cnt, tid, base, rr);
            __syncthreads();
            if (ca >= NFULL) { done = true; break; }
            ca = cb;
            cb = ca < NFULL ? __builtin_amdgcn_readfirstlane(nx) : NFULL;
        }
        if (done) break;
    }
    asm volatile("" : "+v"(got));
    unsigned tc1 = touch_code(tid);
    if (!mail) await();
    held_out(H);
    asm volatile("s_waitcnt lgkmcnt(0)" ::: "memory");
    {
        v2u fl;
        if (tid < 64) {
            const unsigned sa = MS_OFF + 128 + (tid & 15) * 32 + (tid >> 4) * 4;
            unsigned mx = (tid & 15) < 10 ? *ldsp(sa) : 0u;
            float sq = (tid & 15) < 10 ? __uint_as_float(*ldsp(sa + 16)) : 0.f;
#pragma unroll
            for (int i = 0; i < 4; i++) {
                mx = max(mx, (unsigned)(i == 0 ? __builtin_amdgcn_update_dpp(0, (int)mx, 0x111, 0xF, 0xF, false) : i == 1 ? __builtin_amdgcn_update_dpp(0, (int)mx, 0x112, 0xF, 0xF, false)
                                        : i == 2 ? __builtin_amdgcn_update_dpp(0, (int)mx, 0x114, 0xF, 0xF, false) : __builtin_amdgcn_update_dpp(0, (int)mx, 0x118, 0xF, 0xF, false)));
                const int si = __float_as_int(sq);
                sq += __int_as_float(i == 0 ? __builtin_amdgcn_update_dpp(0, si, 0x111, 0xF, 0xF, false) : i == 1 ? __builtin_amdgcn_update_dpp(0, si, 0x112, 0xF, 0xF, false)
                                     : i == 2 ? __builtin_amdgcn_update_dpp(0, si, 0x114, 0xF, 0xF, false) : __builtin_amdgcn_update_dpp(0, si, 0x118, 0xF, 0xF, false));
            }
            mx = min(mx ? (mx >> 16) + 2 : 0u, 0x7F80u);
            fl.x = (mx << 16) | (unsigned)__builtin_amdgcn_update_dpp(0, (int)mx, 0x142, 0xA, 0xF, false);
            fl.y = (__float_as_uint(sq) & 0xFFFF0000u) | ((unsigned)__builtin_amdgcn_update_dpp(0, __float_as_int(sq), 0x142, 0xA, 0xF, false) >> 16);
        }
        if (own.ovf >= 16384u || own.nl > NPOS || wc > ly.cap || (seen & 0x30000000u)) *ldsp(MS_OFF + 96) = 1;
        const unsigned tt = ((tid & 31) << 5) | (tid >> 5);
        const unsigned d = *ldsp(tt * 4) - 64;
        unsigned P = (d * 3) >> 11;
        if (P >= 24) { *ldsp(MS_OFF + 96) = 1; P = 0; }
        const unsigned nu = (P + 2) / 3;
        const unsigned inc = wave_scan_add(nu);
        if ((tid & 63) == 63) *ldsp(MS_OFF + (tid >> 6) * 4) = inc;
        __syncthreads();
        unsigned wb = (tid & 63) < (tid >> 6) ? *ldsp(MS_OFF + (tid & 15) * 4) : 0;
        wb = __builtin_amdgcn_readlane(row_scan_add(wb), 15);
        unsigned fo = wb + inc - nu;
        if (fo + nu > ly.fcap) *ldsp(MS_OFF + 96) = 1;
        const unsigned fbase = pairidx(run) * (ly.F0 * 256);
        {
            const unsigned n = (fo * 33223) >> 24;
            fo = fo - n * 505 + n * 1024;
            fo |= ((((run + mail) >> 2) ^ (mail & 16 ? n : (fbase + fo) >> 17)) & 1) << 9;
            fo &= 0xFFFF;
        }
        *ldsp(WL_OFF + tt * 4) = (nu << 8) | (fo << 16);
        *ldsp(tid * 4) = (fo << 8) | P;
        __syncthreads();
        {
            const v4u te = {own.p[0], own.p[1], own.p[2], (own.p[3] & 15) | (min(own.nl, 15u) << 4) | ((base >> 1) & 4096) | *ldsp(WL_OFF + tid * 4)};
            __builtin_amdgcn_raw_buffer_store_b128(te, __builtin_amdgcn_make_buffer_rsrc((void*)(tab + run * NT * 4), (short)0, NT * 16, 0x00020000), tid * 16, 0, 16);
        }
        if (tid < 64) {
            if (*ldsp(MS_OFF + 96)) fl.x = 0x7F807F80u;
            if ((tid & 31) == 31) __hip_atomic_store((unsigned long long*)(flags + ((tid >> 5) * P1_GRID + run) * 2), ((unsigned long long)fl.y << 32) | fl.x, __ATOMIC_RELAXED, __HIP_MEMORY_SCOPE_AGENT);
        }
        const unsigned k = tid & 7;
        const unsigned pa = (run & 15) * 24 + k * 3;
        const __amdgpu_buffer_rsrc_t rf = __builtin_amdgcn_make_buffer_rsrc((void*)(rec + (size_t)fbase * 16), (short)0, (int)(ly.F0 * 4096), 0x00020000);
#pragma unroll 4
        for (int it = 0; it < 8; it++) {
            const unsigned u = (tid & ~63) + it * 8 + ((tid >> 3) & 7), t = ((u & 31) << 5) | (u >> 5);
            const unsigned w = *ldsp(u * 4), Pt = w & 255;
            v4u v = read_unit(LB + t * LS + k * 16);
            if (k * 3 + 1 >= Pt) { v.y = pad_rec(pa + 1); v.w |= 512u << 11; }
            if (k * 3 + 2 >= Pt) { v.z = pad_rec(pa + 2); v.w |= 512u << 22; }
            __builtin_amdgcn_raw_buffer_store_b128(v, rf, k * 3 < Pt ? ((w >> 8) + k) * 16 : OOB, 0, 16);
        }
        asm volatile("s_waitcnt vmcnt(8)" :: "v"(tc1));
    }
#pragma unroll
    for (int r = 0; r < PD; r++) WAIT_GRP(0, cur[r]);
}

struct AccArgs {
    const char* rec; const unsigned* tab; const unsigned* flags; const unsigned* mdp; float* part; unsigned* praw; unsigned* wgmode;
    const int* idx_in; const int* idx_out; const unsigned short* angle; const unsigned short* ang_freq; const unsigned short* m; const unsigned short* w_down;
    const unsigned short* dist; const unsigned short* centers; const unsigned short* w_rbf; const unsigned short* w_sbf; const unsigned short* w_up;
    unsigned short* out;
    int EP; unsigned O; int T; float c4; int E; int unused;
    Lay ly;
};
typedef char AccArgs_has_no_padding[sizeof(AccArgs) == 19 * 8 + 10 * 4 ? 1 : -1];

typedef const __attribute__((address_space(4))) unsigned* cptr;
static __device__ __forceinline__ void edge_gh(float d, const unsigned short* centers, const unsigned short* w_sbf, const unsigned short* w_rbf, float* g, float* h) {
#pragma unroll
    for (int k = 0; k < 4; k++) g[k] = h[k] = 0.f;
#pragma unroll
    for (int r = 0; r < 8; r++) {
        const unsigned cu = ((cptr)centers)[r >> 1];
        const float df = d - ((r & 1) ? hi_bf(cu) : lo_bf(cu));
        const float rb = __expf(-10.f * df * df);
#pragma unroll
        for (int k = 0; k < 2; k++) {
            const unsigned u = ((cptr)w_sbf)[r * 2 + k], v = ((cptr)w_rbf)[r * 2 + k];
            g[2 * k] += rb * lo_bf(u); g[2 * k + 1] += rb * hi_bf(u); h[2 * k] += rb * lo_bf(v); h[2 * k + 1] += rb * hi_bf(v);
        }
    }
}
static __device__ __forceinline__ v2u edge_out(const float* agg, const float* g, const float* h, unsigned m0, unsigned m1, const unsigned* wu) {
    float a[4];
#pragma unroll
    for (int k = 0; k < 4; k++) a[k] = agg[k] * g[k];
    const float mm[4] = {lo_bf(m0), hi_bf(m0), lo_bf(m1), hi_bf(m1)};
    float o[4];
#pragma unroll
    for (int dd = 0; dd < 4; dd++) {
        float s = 0.f;
#pragma unroll
        for (int k = 0; k < 4; k++) { const unsigned u = wu[k * 2 + (dd >> 1)]; s += a[k] * ((dd & 1) ? hi_bf(u) : lo_bf(u)); }
        o[dd] = mm[dd] + s * h[dd];
    }
    v2u r;
    asm("v_cvt_pk_bf16_f32 %0, %1, %2" : "=v"(r.x) : "v"(o[0]), "v"(o[1]));
    asm("v_cvt_pk_bf16_f32 %0, %1, %2" : "=v"(r.y) : "v"(o[2]), "v"(o[3]));
    return r;
}

typedef const __attribute__((address_space(4))) AccArgs* kaptr;
static __device__ __forceinline__ void acc_fallback(unsigned* lds, unsigned mode, const __attribute__((address_space(4))) AccArgs& a_, int tid) {
    const char* rec = rec_align(a_.rec); const unsigned* flags = a_.flags; const unsigned* mdp = a_.mdp;
    const int EP = a_.EP, T = a_.T; const Lay ly = {a_.ly.F0, a_.ly.cap, a_.ly.dumu, a_.ly.fcap};
    const int lane = tid & 63;
    const int xcd = blockIdx.x & 7, jj = blockIdx.x >> 3;
    const int qt = xcd >> 1, cp = jj & 1, b = (xcd & 1) * 16 + (jj >> 1);
    const unsigned O = a_.O;
    float* pout0 = a_.part + (size_t)(qt * 4 + cp * 2) * EP + (size_t)b * O;
    const unsigned G = tid >> 3, k = tid & 7;
    {
        unsigned* praw = a_.praw + (size_t)(qt * 2 + cp) * EP + (size_t)b * O;
        for (unsigned i = tid; i < O; i += P2_THREADS) praw[i] = 0;
    }
    if (mode == 1) {
        unsigned* acc = lds + OBASE;
        const float Xm = 2139095040.0f / (float)(NWS * (NPOS * 24 + 24)) - 2.0f;
#pragma unroll 1
        for (int c = 0; c < 2; c++) {
            __syncthreads();
            if (tid == 0) lds[0] = 0;
            __syncthreads();
            if (tid < NWS) { unsigned v = wave_scan_max((flags[(cp * P1_GRID + tid) * 2] >> (16 * c)) & 0xFFFF); if (lane == 63) atomicMax(&lds[0], v); }
            __syncthreads();
            unsigned mbits = __builtin_amdgcn_readfirstlane(lds[0]);
            if (mbits == 0) mbits = 0x3f80;
            const float M = __uint_as_float(mbits << 16);
            const float S = Xm / (511.0f * M), inv = M / Xm;
            float racc[20];
#pragma unroll
            for (int r = 0; r < 20; r++) racc[r] = 0.f;
#pragma unroll 1
            for (int s = 0; s < 8; s++) {
                __syncthreads();
                const unsigned* src = mdp + (size_t)cp * (32 * LMAX) + (size_t)(qt * 8 + s) * LMAX;
                for (unsigned i = tid; i < LMAX; i += P2_THREADS) {
                    unsigned w = src[i];
                    lds[(i & ~255u) | ((i & 3) << 6) | ((i >> 2) & 63)] = __float_as_uint((c ? __uint_as_float(w) : lo_bf(w)) * S);
                }
                for (int i = tid; i < OBASE; i += P2_THREADS) acc[i] = 0;
                __syncthreads();
                const unsigned t = b * 32 + qt * 8 + s;
                for (unsigned r = G; r < NWS; r += 128) {
                    v4u te = ((const v4u*)a_.tab)[r * NT + t];
                    const unsigned tw = te.w, nl = (tw >> 4) & 15, nu = (tw >> 8) & 15;
                    for (unsigned l = 0; l <= nl; l++) {
                        unsigned u;
                        if (l < nl) {
                            u = (((te.x & 1023) * LMUL) & LMASK) + logbase(t >> 6, r) + ((tw >> 3) & 512) + k;
                            te.x = __builtin_amdgcn_alignbit(te.y, te.x, 10); te.y = __builtin_amdgcn_alignbit(te.z, te.y, 10); te.z = __builtin_amdgcn_alignbit(te.w, te.z, 10);
                        }
                        else { if (k >= nu) break; u = pairidx(r) * (ly.F0 * 256) + (tw >> 16) + k; }
                        v4u q = *(const v4u*)(rec + (size_t)u * 16);
                        unsigned a[3] = {q.x, q.y, q.z};
#pragma unroll
                        for (int j = 0; j < 3; j++) {
                            const int cq = (int)((q.w >> (j == 0 ? 1 : j == 1 ? 11 : 22)) & 1023) - 512;
                            const int x = __float2int_rn(__uint_as_float(lds[(a[j] >> 2) & 0x7FFF]) * (float)cq);
                            if ((a[j] >> 17) - IDXB < OBASE) atomicAdd(&acc[(a[j] >> 17) - IDXB], (unsigned)x);
                        }
                    }
                }
                __syncthreads();
#pragma unroll
                for (int r = 0; r < 20; r++) racc[r] += (float)(int)acc[r * P2_THREADS + tid] * inv;
            }
#pragma unroll
            for (int r = 0; r < 20; r++) if ((unsigned)(r * P2_THREADS + tid) < O) pout0[(size_t)c * EP + r * P2_THREADS + tid] = racc[r];
        }
    }
    if (mode == 2) {
        float* facc = (float*)lds;
        const float freq = bf2f(a_.ang_freq[0]);
        float wd[2][4];
#pragma unroll
        for (int kk = 0; kk < 4; kk++) { wd[0][kk] = bf2f(a_.w_down[kk * 4 + cp * 2]); wd[1][kk] = bf2f(a_.w_down[kk * 4 + cp * 2 + 1]); }
        __syncthreads();
        for (int i = tid; i < 2 * OBASE; i += P2_THREADS) facc[i] = 0.f;
        __syncthreads();
        for (int t = tid; t < T; t += P2_THREADS) {
            const unsigned io = a_.idx_out[t], ii = a_.idx_in[t];
            if (((io >> 7) & 31) == (unsigned)b && ((ii >> 3) & 3) == (unsigned)qt) {
                const v2u mv = ((const v2u*)a_.m)[ii];
                const float a[4] = {lo_bf(mv.x), hi_bf(mv.x), lo_bf(mv.y), hi_bf(mv.y)};
                const float cs = __cosf(freq * bf2f(a_.angle[t]));
                const unsigned ol = (io & 127u) | ((io >> 5) & ~127u);
#pragma unroll
                for (int c = 0; c < 2; c++)
                    atomicAdd(&facc[c * OBASE + ol], (a[0] * wd[c][0] + a[1] * wd[c][1] + a[2] * wd[c][2] + a[3] * wd[c][3]) * cs);
            }
        }
        __syncthreads();
        for (unsigned idx = tid; idx < O; idx += P2_THREADS) { pout0[idx] = facc[idx]; pout0[(size_t)EP + idx] = facc[OBASE + idx]; }
    }
}

#define SWAPX 2
#define RAUX ""
#define J0 (wave >> 1)
#define IDXMASK "0x8fffffff"
#define STR2(x) #x
#define STR(x) STR2(x)
#define PLAN_IT 160

extern "C" __global__ __launch_bounds__(P2_THREADS) void k_acc(AccArgs a_) {
    const Lay ly = a_.ly;
    __shared__ __attribute__((aligned(16))) unsigned lds[2 * OBASE];
    const int tid = threadIdx.x;
    const int wave = __builtin_amdgcn_readfirstlane(tid >> 6), lane = tid & 63;
    const int xcd = blockIdx.x & 7, jj = blockIdx.x >> 3;
    const int qt = xcd >> 1, cp = jj & 1, b = (xcd & 1) * 16 + (jj >> 1);
    const unsigned O = a_.O;
    unsigned stv = 0;
    unsigned tc2 = touch_code(tid);
    unsigned* praw = a_.praw + (size_t)(qt * 2 + cp) * a_.EP + (size_t)b * O;
    const unsigned* mds = a_.mdp + (size_t)cp * (32 * LMAX) + (size_t)(qt * 8) * LMAX;
    unsigned mode = (unsigned)(size_t)lds ? 2u : 0u;
    unsigned exs = 0, bad = 0;
    v4u mm[2], dd;
    const int q = qt * 2 + cp;
    unsigned t16 = tid * 16;
    if (mode == 0) {
        const unsigned w = wave * 16 + ((lane & 15) ^ (cp ? SWAPX : 0)), t0 = b * 32 + qt * 8 + (lane >> 4) * 2;
        v4i_rs rs = mk_rsrc(rec_align(a_.rec), ly.dumu + 64);
        rs.y |= 16 << 16;
        const v4i_rs rm = mk_rsrc(mds, 8 * LMAX * 4);
        const v4i_rs rp0 = mk_rsrc(praw, O * 4);
        const v4i_rs rt = mk_rsrc(a_.tab, (NT * NWS * 4 + FL_SIZE) * 4);
        const v4i_rs rmm = mk_rsrc(a_.m, a_.E * 8), rd = mk_rsrc(a_.dist, a_.E * 2);
        asm volatile(
            "s_setprio 3\n"
            "buffer_load_dwordx4 v[32:35], %[tof], %[rt], 0 offen\n"
            "buffer_load_dwordx4 v[36:39], %[tof], %[rt], 0 offen offset:16\n"
            "v_and_b32 v10, 1023, %[tid16]\n"
            "v_bfrev_b32 v12, 1\n"
            "v_add_u32 v11, %[pb], v10\n"
            "v_bfrev_b32 v13, 1\n"
            "v_bfrev_b32 v14, 1\n"
            "v_bfrev_b32 v15, 1\n"
            "ds_write_b128 v11, v[12:15] offset:0\n"
            "ds_write_b128 v11, v[12:15] offset:1024\n"
            "ds_write_b128 v11, v[12:15] offset:2048\n"
            "ds_write_b128 v11, v[12:15] offset:3072\n"
            "ds_write_b128 v11, v[12:15] offset:4096\n"
            "v_bfrev_b32 v80, 1\n"
            "v_mov_b32 v84, 0xb9000000\n"
            "v_mov_b32 v88, 0\n"
            "v_add_u32 v89, 81920, %[tid16]\n"
            "v_add_u32 v90, 65536, v89\n"
            "v_lshrrev_b32 v16, 4, v10\n"
            "v_add_u32 v83, %[dumu], v16\n"
            "v_and_b32 v17, 7, v16\n"
            "v_lshl_or_b32 v82, v17, 28, v17\n"
            "v_lshrrev_b32 v18, 3, v16\n"
            "v_lshl_add_u32 v17, v17, 3, v18\n"
            "v_lshlrev_b32 v17, 2, v17\n"
            "v_add_u32 v17, %[pb], v17\n"
            "s_getpc_b64 s[70:71]\n"
            "Lpc%=:\n"
            "s_add_u32 s72, s70, Lchg2%=-Lpc%=\n s_addc_u32 s73, s71, 0\n"
            "s_add_u32 s66, s70, Lchg3%=-Lpc%=\n s_addc_u32 s67, s71, 0\n"
            "s_add_u32 s70, s70, Lchg1%=-Lpc%=\n s_addc_u32 s71, s71, 0\n"
            "s_waitcnt vmcnt(0)\n"
            "buffer_load_dwordx4 v[92:95], v10, %[rt], %[fof] offen\n"
            "buffer_load_dwordx4 v[96:99], v10, %[rt], %[fof] offen offset:1024\n"
            "s_mov_b32 s94, 0\n"
            "buffer_load_dwordx4 v[40:43], %[tid16], %[rm], s94 offen\n"
            "s_add_u32 s96, s94, 16384\n"
            "buffer_load_dwordx4 v[44:47], %[tid16], %[rm], s96 offen\n"
            "s_add_u32 s96, s94, 32768\n"
            "buffer_load_dwordx4 v[48:51], %[tid16], %[rm], s96 offen\n"
            "s_add_u32 s96, s94, 49152\n"
            "buffer_load_dwordx4 v[52:55], %[tid16], %[rm], s96 offen\n"
            "s_add_u32 s96, s94, 65536\n"
            "buffer_load_dwordx4 v[56:59], %[tid16], %[rm], s96 offen\n"
            "v_bfe_u32 v20, v35, 4, 4\n v_bfe_u32 v21, v39, 4, 4\n v_bfe_u32 v22, v35, 8, 4\n v_bfe_u32 v23, v39, 8, 4\n"
            "v_min_u32 v24, 1, v22\n v_min_u32 v25, 1, v23\n v_add_u32 v24, v24, v20\n v_add_u32 v25, v25, v21\n"
            "v_lshl_or_b32 v24, v25, 16, v24\n"
            "v_lshrrev_b32 v8, 16, v35\n v_lshrrev_b32 v9, 16, v39\n"
            "v_add_u32_dpp v25, v24, v24 row_shr:1 row_mask:0xf bank_mask:0xf bound_ctrl:1\n"
            "v_add_u32 v8, %[fb], v8\n v_add_u32 v9, %[fb], v9\n"
            "v_add_u32_dpp v25, v25, v25 row_shr:2 row_mask:0xf bank_mask:0xf bound_ctrl:1\n"
            "v_sub_u32 v26, 8, v22\n v_sub_u32 v27, 8, v23\n"
            "v_add_u32_dpp v25, v25, v25 row_shr:4 row_mask:0xf bank_mask:0xf bound_ctrl:1\n"
            "v_lshl_or_b32 v8, v26, 28, v8\n v_lshl_or_b32 v9, v27, 28, v9\n"
            "v_add_u32_dpp v25, v25, v25 row_shr:8 row_mask:0xf bank_mask:0xf bound_ctrl:1\n"
            "s_nop 1\n"
            "v_readlane_b32 s84, v25, 15\n"
            "v_readlane_b32 s85, v25, 31\n"
            "v_readlane_b32 s86, v25, 47\n"
            "v_readlane_b32 s87, v25, 63\n"
            "v_sub_u32 v25, v25, v24\n"
            "s_mov_b32 s96, 0\n s_mov_b64 s[76:77], 0\n"
            "s_add_u32 s84, s84, 0x70007\n"
            "s_bfe_u32 s88, s84, 0xd0003\n s_lshr_b32 s89, s84, 19\n"
            "s_max_u32 s88, s88, 8\n s_max_u32 s89, s89, 8\n"
            "s_add_u32 s90, s96, s88\n"
            "s_pack_ll_b32_b16 s91, s96, s90\n"
            "s_add_u32 s96, s90, s89\n"
            "s_lshl_b32 s89, s89, 8\n s_or_b32 s88, s88, s89\n s_and_b32 s88, s88, 0xffff\n"
            "s_or_b32 s76, s76, s88\n"
            "s_lshl_b32 s91, s91, 3\n"
            "v_mov_b32 v26, s91\n"
            "s_add_u32 s85, s85, 0x70007\n"
            "s_bfe_u32 s88, s85, 0xd0003\n s_lshr_b32 s89, s85, 19\n"
            "s_max_u32 s88, s88, 8\n s_max_u32 s89, s89, 8\n"
            "s_add_u32 s90, s96, s88\n"
            "s_pack_ll_b32_b16 s91, s96, s90\n"
            "s_add_u32 s96, s90, s89\n"
            "s_lshl_b32 s89, s89, 8\n s_or_b32 s88, s88, s89\n s_and_b32 s88, s88, 0xffff\n"
            "s_lshl_b32 s88, s88, 16\n"
            "s_or_b32 s76, s76, s88\n"
            "s_lshl_b32 s91, s91, 3\n"
            "s_mov_b32 exec_lo, 0xffff0000\n s_mov_b32 exec_hi, 0xffffffff\n"
            "v_mov_b32 v26, s91\n"
            "s_add_u32 s86, s86, 0x70007\n"
            "s_bfe_u32 s88, s86, 0xd0003\n s_lshr_b32 s89, s86, 19\n"
            "s_max_u32 s88, s88, 8\n s_max_u32 s89, s89, 8\n"
            "s_add_u32 s90, s96, s88\n"
            "s_pack_ll_b32_b16 s91, s96, s90\n"
            "s_add_u32 s96, s90, s89\n"
            "s_lshl_b32 s89, s89, 8\n s_or_b32 s88, s88, s89\n s_and_b32 s88, s88, 0xffff\n"
            "s_or_b32 s77, s77, s88\n"
            "s_lshl_b32 s91, s91, 3\n"
            "s_mov_b32 exec_lo, 0x0\n s_mov_b32 exec_hi, 0xffffffff\n"
            "v_mov_b32 v26, s91\n"
            "s_add_u32 s87, s87, 0x70007\n"
            "s_bfe_u32 s88, s87, 0xd0003\n s_lshr_b32 s89, s87, 19\n"
            "s_max_u32 s88, s88, 8\n s_max_u32 s89, s89, 8\n"
            "s_add_u32 s90, s96, s88\n"
            "s_pack_ll_b32_b16 s91, s96, s90\n"
            "s_add_u32 s96, s90, s89\n"
            "s_lshl_b32 s89, s89, 8\n s_or_b32 s88, s88, s89\n s_and_b32 s88, s88, 0xffff\n"
            "s_lshl_b32 s88, s88, 16\n"
            "s_or_b32 s77, s77, s88\n"
            "s_lshl_b32 s91, s91, 3\n"
            "s_mov_b32 exec_lo, 0x0\n s_mov_b32 exec_hi, 0xffff0000\n"
            "v_mov_b32 v26, s91\n"
            "s_mov_b64 exec, -1\n"
            "s_mov_b32 %[bad], 0\n"
            "s_cmp_gt_u32 s96, " STR(PLAN_IT) "\n"
            "s_cbranch_scc0 1f\n"
            "s_mov_b32 %[bad], 1\n s_mov_b32 s76, 0x08080808\n s_mov_b32 s77, 0x08080808\n"
            "s_branch Lnoplan%=\n"
            "1:\n"
            "v_add_u32 v25, v25, v26\n"
            "v_and_b32 v26, 0xffff, v25\n v_lshrrev_b32 v27, 16, v25\n"
            "v_lshlrev_b32 v26, 2, v26\n v_lshlrev_b32 v27, 2, v27\n"
            "v_add_u32 v26, %[pb], v26\n v_add_u32 v27, %[pb], v27\n"
            "v_lshl_add_u32 v28, v20, 2, v26\n v_lshl_add_u32 v29, v21, 2, v27\n"
            "v_cmp_ne_u32 vcc, 0, v22\n v_cmp_ne_u32 s[88:89], 0, v23\n"
            "v_lshrrev_b32 v30, 3, v35\n v_and_b32 v30, 0x200, v30\n v_or_b32 v30, %[xb], v30\n"
            "s_mov_b64 exec, vcc\n ds_write_b32 v28, v8\n s_mov_b64 exec, s[88:89]\n ds_write_b32 v29, v9\n s_mov_b64 exec, -1\n"
            "v_min_u32 v24, v20, v21\n v_mov_b32 v31, %[lmask]\n s_movk_i32 s91, 1023\n"
            "v_cmp_gt_u32 vcc, 4, v24\n"
            "s_cmp_eq_u64 vcc, 0\n s_cbranch_scc1 4f\n"
            "v_cmp_lt_u32 vcc, 0, v20\n v_cmp_lt_u32 s[88:89], 0, v21\n"
            "v_bfe_u32 v28, v32, 0, 10\n"
            "v_bfe_u32 v29, v36, 0, 10\n"
            "s_or_b64 s[84:85], vcc, s[88:89]\n"
            "s_cbranch_scc0 Lnoplan%=\n"
            "v_mul_u32_u24 v28, %[lmul], v28\n"
            "v_mul_u32_u24 v29, %[lmul], v29\n"
            "v_and_or_b32 v28, v28, v31, v30\n"
            "v_and_or_b32 v29, v29, v31, v30\n"
            "s_mov_b64 exec, vcc\n ds_write_b32 v26, v28 offset:0\n s_mov_b64 exec, s[88:89]\n ds_write_b32 v27, v29 offset:0\n s_mov_b64 exec, -1\n"
            "v_cmp_lt_u32 vcc, 1, v20\n v_cmp_lt_u32 s[88:89], 1, v21\n"
            "v_bfe_u32 v28, v32, 10, 10\n"
            "v_bfe_u32 v29, v36, 10, 10\n"
            "s_or_b64 s[84:85], vcc, s[88:89]\n"
            "s_cbranch_scc0 Lnoplan%=\n"
            "v_mul_u32_u24 v28, %[lmul], v28\n"
            "v_mul_u32_u24 v29, %[lmul], v29\n"
            "v_and_or_b32 v28, v28, v31, v30\n"
            "v_and_or_b32 v29, v29, v31, v30\n"
            "s_mov_b64 exec, vcc\n ds_write_b32 v26, v28 offset:4\n s_mov_b64 exec, s[88:89]\n ds_write_b32 v27, v29 offset:4\n s_mov_b64 exec, -1\n"
            "v_cmp_lt_u32 vcc, 2, v20\n v_cmp_lt_u32 s[88:89], 2, v21\n"
            "v_bfe_u32 v28, v32, 20, 10\n"
            "v_bfe_u32 v29, v36, 20, 10\n"
            "s_or_b64 s[84:85], vcc, s[88:89]\n"
            "s_cbranch_scc0 Lnoplan%=\n"
            "v_mul_u32_u24 v28, %[lmul], v28\n"
            "v_mul_u32_u24 v29, %[lmul], v29\n"
            "v_and_or_b32 v28, v28, v31, v30\n"
            "v_and_or_b32 v29, v29, v31, v30\n"
            "s_mov_b64 exec, vcc\n ds_write_b32 v26, v28 offset:8\n s_mov_b64 exec, s[88:89]\n ds_write_b32 v27, v29 offset:8\n s_mov_b64 exec, -1\n"
            "v_cmp_lt_u32 vcc, 3, v20\n v_cmp_lt_u32 s[88:89], 3, v21\n"
            "v_alignbit_b32 v28, v33, v32, 30\n"
            "v_and_b32 v28, s91, v28\n"
            "v_alignbit_b32 v29, v37, v36, 30\n"
            "v_and_b32 v29, s91, v29\n"
            "s_or_b64 s[84:85], vcc, s[88:89]\n"
            "s_cbranch_scc0 Lnoplan%=\n"
            "v_mul_u32_u24 v28, %[lmul], v28\n"
            "v_mul_u32_u24 v29, %[lmul], v29\n"
            "v_and_or_b32 v28, v28, v31, v30\n"
            "v_and_or_b32 v29, v29, v31, v30\n"
            "s_mov_b64 exec, vcc\n ds_write_b32 v26, v28 offset:12\n s_mov_b64 exec, s[88:89]\n ds_write_b32 v27, v29 offset:12\n s_mov_b64 exec, -1\n"
            "s_branch 5f\n"
            "4:\n"
            "v_bfe_u32 v12, v32, 0, 10\n"
            "v_bfe_u32 v13, v32, 10, 10\n"
            "v_bfe_u32 v14, v36, 0, 10\n"
            "v_bfe_u32 v15, v36, 10, 10\n"
            "v_mul_u32_u24 v12, %[lmul], v12\n"
            "v_mul_u32_u24 v13, %[lmul], v13\n"
            "v_mul_u32_u24 v14, %[lmul], v14\n"
            "v_mul_u32_u24 v15, %[lmul], v15\n"
            "v_and_or_b32 v12, v12, v31, v30\n"
            "v_and_or_b32 v13, v13, v31, v30\n"
            "v_and_or_b32 v14, v14, v31, v30\n"
            "v_and_or_b32 v15, v15, v31, v30\n"
            "ds_write2_b32 v26, v12, v13 offset0:0 offset1:1\n ds_write2_b32 v27, v14, v15 offset0:0 offset1:1\n"
            "v_bfe_u32 v12, v32, 20, 10\n"
            "v_alignbit_b32 v13, v33, v32, 30\n"
            "v_and_b32 v13, s91, v13\n"
            "v_bfe_u32 v14, v36, 20, 10\n"
            "v_alignbit_b32 v15, v37, v36, 30\n"
            "v_and_b32 v15, s91, v15\n"
            "v_mul_u32_u24 v12, %[lmul], v12\n"
            "v_mul_u32_u24 v13, %[lmul], v13\n"
            "v_mul_u32_u24 v14, %[lmul], v14\n"
            "v_mul_u32_u24 v15, %[lmul], v15\n"
            "v_and_or_b32 v12, v12, v31, v30\n"
            "v_and_or_b32 v13, v13, v31, v30\n"
            "v_and_or_b32 v14, v14, v31, v30\n"
            "v_and_or_b32 v15, v15, v31, v30\n"
            "ds_write2_b32 v26, v12, v13 offset0:2 offset1:3\n ds_write2_b32 v27, v14, v15 offset0:2 offset1:3\n"
            "5:\n"
            "v_cmp_lt_u32 vcc, 4, v20\n v_cmp_lt_u32 s[88:89], 4, v21\n"
            "v_bfe_u32 v28, v33, 8, 10\n"
            "v_bfe_u32 v29, v37, 8, 10\n"
            "s_or_b64 s[84:85], vcc, s[88:89]\n"
            "s_cbranch_scc0 Lnoplan%=\n"
            "v_mul_u32_u24 v28, %[lmul], v28\n"
            "v_mul_u32_u24 v29, %[lmul], v29\n"
            "v_and_or_b32 v28, v28, v31, v30\n"
            "v_and_or_b32 v29, v29, v31, v30\n"
            "s_mov_b64 exec, vcc\n ds_write_b32 v26, v28 offset:16\n s_mov_b64 exec, s[88:89]\n ds_write_b32 v27, v29 offset:16\n s_mov_b64 exec, -1\n"
            "v_cmp_lt_u32 vcc, 5, v20\n v_cmp_lt_u32 s[88:89], 5, v21\n"
            "v_bfe_u32 v28, v33, 18, 10\n"
            "v_bfe_u32 v29, v37, 18, 10\n"
            "s_or_b64 s[84:85], vcc, s[88:89]\n"
            "s_cbranch_scc0 Lnoplan%=\n"
            "v_mul_u32_u24 v28, %[lmul], v28\n"
            "v_mul_u32_u24 v29, %[lmul], v29\n"
            "v_and_or_b32 v28, v28, v31, v30\n"
            "v_and_or_b32 v29, v29, v31, v30\n"
            "s_mov_b64 exec, vcc\n ds_write_b32 v26, v28 offset:20\n s_mov_b64 exec, s[88:89]\n ds_write_b32 v27, v29 offset:20\n s_mov_b64 exec, -1\n"
            "v_cmp_lt_u32 vcc, 6, v20\n v_cmp_lt_u32 s[88:89], 6, v21\n"
            "v_alignbit_b32 v28, v34, v33, 28\n"
            "v_and_b32 v28, s91, v28\n"
            "v_alignbit_b32 v29, v38, v37, 28\n"
            "v_and_b32 v29, s91, v29\n"
            "s_or_b64 s[84:85], vcc, s[88:89]\n"
            "s_cbranch_scc0 Lnoplan%=\n"
            "v_mul_u32_u24 v28, %[lmul], v28\n"
            "v_mul_u32_u24 v29, %[lmul], v29\n"
            "v_and_or_b32 v28, v28, v31, v30\n"
            "v_and_or_b32 v29, v29, v31, v30\n"
            "s_mov_b64 exec, vcc\n ds_write_b32 v26, v28 offset:24\n s_mov_b64 exec, s[88:89]\n ds_write_b32 v27, v29 offset:24\n s_mov_b64 exec, -1\n"
            "v_cmp_lt_u32 vcc, 7, v20\n v_cmp_lt_u32 s[88:89], 7, v21\n"
            "v_bfe_u32 v28, v34, 6, 10\n"
            "v_bfe_u32 v29, v38, 6, 10\n"
            "s_or_b64 s[84:85], vcc, s[88:89]\n"
            "s_cbranch_scc0 Lnoplan%=\n"
            "v_mul_u32_u24 v28, %[lmul], v28\n"
            "v_mul_u32_u24 v29, %[lmul], v29\n"
            "v_and_or_b32 v28, v28, v31, v30\n"
            "v_and_or_b32 v29, v29, v31, v30\n"
            "s_mov_b64 exec, vcc\n ds_write_b32 v26, v28 offset:28\n s_mov_b64 exec, s[88:89]\n ds_write_b32 v27, v29 offset:28\n s_mov_b64 exec, -1\n"
            "v_cmp_lt_u32 vcc, 8, v20\n v_cmp_lt_u32 s[88:89], 8, v21\n"
            "v_bfe_u32 v28, v34, 16, 10\n"
            "v_bfe_u32 v29, v38, 16, 10\n"
            "s_or_b64 s[84:85], vcc, s[88:89]\n"
            "s_cbranch_scc0 Lnoplan%=\n"
            "v_mul_u32_u24 v28, %[lmul], v28\n"
            "v_mul_u32_u24 v29, %[lmul], v29\n"
            "v_and_or_b32 v28, v28, v31, v30\n"
            "v_and_or_b32 v29, v29, v31, v30\n"
            "s_mov_b64 exec, vcc\n ds_write_b32 v26, v28 offset:32\n s_mov_b64 exec, s[88:89]\n ds_write_b32 v27, v29 offset:32\n s_mov_b64 exec, -1\n"
            "v_cmp_lt_u32 vcc, 9, v20\n v_cmp_lt_u32 s[88:89], 9, v21\n"
            "v_alignbit_b32 v28, v35, v34, 26\n"
            "v_and_b32 v28, s91, v28\n"
            "v_alignbit_b32 v29, v39, v38, 26\n"
            "v_and_b32 v29, s91, v29\n"
            "s_or_b64 s[84:85], vcc, s[88:89]\n"
            "s_cbranch_scc0 Lnoplan%=\n"
            "v_mul_u32_u24 v28, %[lmul], v28\n"
            "v_mul_u32_u24 v29, %[lmul], v29\n"
            "v_and_or_b32 v28, v28, v31, v30\n"
            "v_and_or_b32 v29, v29, v31, v30\n"
            "s_mov_b64 exec, vcc\n ds_write_b32 v26, v28 offset:36\n s_mov_b64 exec, s[88:89]\n ds_write_b32 v27, v29 offset:36\n s_mov_b64 exec, -1\n"
            "Lnoplan%=:\n"
            "ds_read_b32 v60, v17 offset:0\n"
            "ds_read_b32 v61, v17 offset:256\n"
            "ds_read_b32 v62, v17 offset:512\n"
            "ds_read_b32 v63, v17 offset:768\n"
            "ds_read_b32 v64, v17 offset:1024\n"
            "ds_read_b32 v65, v17 offset:1280\n"
            "ds_read_b32 v66, v17 offset:1536\n"
            "ds_read_b32 v67, v17 offset:1792\n"
            "ds_read_b32 v68, v17 offset:2048\n"
            "ds_read_b32 v69, v17 offset:2304\n"
            "ds_read_b32 v70, v17 offset:2560\n"
            "ds_read_b32 v71, v17 offset:2816\n"
            "ds_read_b32 v72, v17 offset:3072\n"
            "ds_read_b32 v73, v17 offset:3328\n"
            "ds_read_b32 v74, v17 offset:3584\n"
            "ds_read_b32 v75, v17 offset:3840\n"
            "ds_read_b32 v76, v17 offset:4096\n"
            "ds_read_b32 v77, v17 offset:4352\n"
            "ds_read_b32 v78, v17 offset:4608\n"
            "ds_read_b32 v79, v17 offset:4864\n"
            "v_mov_b32 v100, 0\n"
            "v_mov_b32 v101, 0\n"
            "v_mov_b32 v102, 0\n"
            "v_mov_b32 v103, 0\n"
            "v_mov_b32 v107, v11\n"
            "s_mov_b32 s82, 0\n"
            "s_waitcnt lgkmcnt(15)\n"
            "v_mov_b32 v81, v60\n"
            "s_nop 1\n"
            "v_add_u32_dpp v111, v81, v82 row_newbcast:0 row_mask:0xf bank_mask:0x3\n"
            "v_add_u32_dpp v111, v81, v82 row_newbcast:8 row_mask:0xf bank_mask:0xc\n"
            "v_and_b32 v111, " IDXMASK ", v111\n"
            "v_min_u32 v111, v111, v83\n"
            "buffer_load_dwordx4 v[8:11], v111, %[rs], 0 idxen " RAUX "\n"
            "v_add_u32_dpp v111, v81, v82 row_newbcast:1 row_mask:0xf bank_mask:0x3\n"
            "v_add_u32_dpp v111, v81, v82 row_newbcast:9 row_mask:0xf bank_mask:0xc\n"
            "v_and_b32 v111, " IDXMASK ", v111\n"
            "v_min_u32 v111, v111, v83\n"
            "buffer_load_dwordx4 v[12:15], v111, %[rs], 0 idxen " RAUX "\n"
            "v_add_u32_dpp v111, v81, v82 row_newbcast:2 row_mask:0xf bank_mask:0x3\n"
            "v_add_u32_dpp v111, v81, v82 row_newbcast:10 row_mask:0xf bank_mask:0xc\n"
            "v_and_b32 v111, " IDXMASK ", v111\n"
            "v_min_u32 v111, v111, v83\n"
            "buffer_load_dwordx4 v[16:19], v111, %[rs], 0 idxen " RAUX "\n"
            "v_add_u32_dpp v111, v81, v82 row_newbcast:3 row_mask:0xf bank_mask:0x3\n"
            "v_add_u32_dpp v111, v81, v82 row_newbcast:11 row_mask:0xf bank_mask:0xc\n"
            "v_and_b32 v111, " IDXMASK ", v111\n"
            "v_min_u32 v111, v111, v83\n"
            "buffer_load_dwordx4 v[20:23], v111, %[rs], 0 idxen " RAUX "\n"
            "v_add_u32_dpp v111, v81, v82 row_newbcast:4 row_mask:0xf bank_mask:0x3\n"
            "v_add_u32_dpp v111, v81, v82 row_newbcast:12 row_mask:0xf bank_mask:0xc\n"
            "v_and_b32 v111, " IDXMASK ", v111\n"
            "v_min_u32 v111, v111, v83\n"
            "buffer_load_dwordx4 v[24:27], v111, %[rs], 0 idxen " RAUX "\n"
            "v_add_u32_dpp v111, v81, v82 row_newbcast:5 row_mask:0xf bank_mask:0x3\n"
            "v_add_u32_dpp v111, v81, v82 row_newbcast:13 row_mask:0xf bank_mask:0xc\n"
            "v_and_b32 v111, " IDXMASK ", v111\n"
            "v_min_u32 v111, v111, v83\n"
            "buffer_load_dwordx4 v[28:31], v111, %[rs], 0 idxen " RAUX "\n"
            "v_add_u32_dpp v111, v81, v82 row_newbcast:6 row_mask:0xf bank_mask:0x3\n"
            "v_add_u32_dpp v111, v81, v82 row_newbcast:14 row_mask:0xf bank_mask:0xc\n"
            "v_and_b32 v111, " IDXMASK ", v111\n"
            "v_min_u32 v111, v111, v83\n"
            "buffer_load_dwordx4 v[32:35], v111, %[rs], 0 idxen " RAUX "\n"
            "v_add_u32_dpp v111, v81, v82 row_newbcast:7 row_mask:0xf bank_mask:0x3\n"
            "v_add_u32_dpp v111, v81, v82 row_newbcast:15 row_mask:0xf bank_mask:0xc\n"
            "v_and_b32 v111, " IDXMASK ", v111\n"
            "v_min_u32 v111, v111, v83\n"
            "buffer_load_dwordx4 v[36:39], v111, %[rs], 0 idxen " RAUX "\n"
            "s_min_u32 s82, s82, 19\n"
            "s_add_u32 s82, s82, 1\n"
            "s_set_gpr_idx_on s82, gpr_idx(SRC0)\n"
            "s_nop 0\n"
            "v_mov_b32 v81, v60\n"
            "s_set_gpr_idx_off\n"
            "s_setprio 0\n"
            "ds_write_b128 v107, v[100:103] offset:0\n"
            "ds_write_b128 v107, v[100:103] offset:1024\n"
            "ds_write_b128 v107, v[100:103] offset:2048\n"
            "ds_write_b128 v107, v[100:103] offset:3072\n"
            "ds_write_b128 v107, v[100:103] offset:4096\n"
            "s_waitcnt vmcnt(13)\n"
            "v_pk_max_u16 v92, v92, v94\n v_pk_max_u16 v96, v96, v98\n"
            "v_lshlrev_b32 v104, 16, v93\n v_lshlrev_b32 v105, 16, v95\n v_lshlrev_b32 v106, 16, v97\n v_lshlrev_b32 v108, 16, v99\n"
            "v_pk_max_u16 v92, v92, v96\n"
            "v_add_f32 v104, v104, v105\n v_add_f32 v106, v106, v108\n v_add_f32 v93, v93, v95\n v_add_f32 v97, v97, v99\n"
            "v_and_b32 v94, 0xffff, v92\n v_lshrrev_b32 v92, 16, v92\n"
            "v_add_f32 v104, v104, v106\n v_add_f32 v93, v93, v97\n"
            "s_nop 0\n"
            "v_max_u32_dpp v94, v94, v94 row_shr:1 row_mask:0xf bank_mask:0xf bound_ctrl:1\n"
            "v_max_u32_dpp v92, v92, v92 row_shr:1 row_mask:0xf bank_mask:0xf bound_ctrl:1\n"
            "v_add_f32_dpp v104, v104, v104 row_shr:1 row_mask:0xf bank_mask:0xf bound_ctrl:1\n"
            "v_add_f32_dpp v93, v93, v93 row_shr:1 row_mask:0xf bank_mask:0xf bound_ctrl:1\n"
            "v_max_u32_dpp v94, v94, v94 row_shr:2 row_mask:0xf bank_mask:0xf bound_ctrl:1\n"
            "v_max_u32_dpp v92, v92, v92 row_shr:2 row_mask:0xf bank_mask:0xf bound_ctrl:1\n"
            "v_add_f32_dpp v104, v104, v104 row_shr:2 row_mask:0xf bank_mask:0xf bound_ctrl:1\n"
            "v_add_f32_dpp v93, v93, v93 row_shr:2 row_mask:0xf bank_mask:0xf bound_ctrl:1\n"
            "v_max_u32_dpp v94, v94, v94 row_shr:4 row_mask:0xf bank_mask:0xf bound_ctrl:1\n"
            "v_max_u32_dpp v92, v92, v92 row_shr:4 row_mask:0xf bank_mask:0xf bound_ctrl:1\n"
            "v_add_f32_dpp v104, v104, v104 row_shr:4 row_mask:0xf bank_mask:0xf bound_ctrl:1\n"
            "v_add_f32_dpp v93, v93, v93 row_shr:4 row_mask:0xf bank_mask:0xf bound_ctrl:1\n"
            "v_max_u32_dpp v94, v94, v94 row_shr:8 row_mask:0xf bank_mask:0xf bound_ctrl:1\n"
            "v_max_u32_dpp v92, v92, v92 row_shr:8 row_mask:0xf bank_mask:0xf bound_ctrl:1\n"
            "v_add_f32_dpp v104, v104, v104 row_shr:8 row_mask:0xf bank_mask:0xf bound_ctrl:1\n"
            "v_add_f32_dpp v93, v93, v93 row_shr:8 row_mask:0xf bank_mask:0xf bound_ctrl:1\n"
            "v_max_u32_dpp v94, v94, v94 row_bcast:15 row_mask:0xa bank_mask:0xf\n"
            "v_max_u32_dpp v92, v92, v92 row_bcast:15 row_mask:0xa bank_mask:0xf\n"
            "v_add_f32_dpp v104, v104, v104 row_bcast:15 row_mask:0xa bank_mask:0xf\n"
            "v_add_f32_dpp v93, v93, v93 row_bcast:15 row_mask:0xa bank_mask:0xf\n"
            "v_max_u32_dpp v94, v94, v94 row_bcast:31 row_mask:0xc bank_mask:0xf\n"
            "v_max_u32_dpp v92, v92, v92 row_bcast:31 row_mask:0xc bank_mask:0xf\n"
            "v_add_f32_dpp v104, v104, v104 row_bcast:31 row_mask:0xc bank_mask:0xf\n"
            "v_add_f32_dpp v93, v93, v93 row_bcast:31 row_mask:0xc bank_mask:0xf\n"
            "s_nop 1\n"
            "v_readlane_b32 s84, v94, 63\n v_readlane_b32 s85, v92, 63\n v_readlane_b32 s86, v104, 63\n v_readlane_b32 s87, v93, 63\n"
            "s_mov_b32 %[mode], 0\n"
            "s_max_u32 s96, s84, s85\n s_cmp_ge_u32 s96, 0x7f00\n s_cselect_b32 %[mode], 2, 0\n"
            "s_cmp_eq_u32 s84, 0\n s_cselect_b32 s84, 0x3f80, s84\n s_cselect_b32 s86, 0x7f000000, s86\n"
            "s_cmp_eq_u32 s85, 0\n s_cselect_b32 s85, 0x3f80, s85\n s_cselect_b32 s87, 0x7f000000, s87\n"
            "s_min_u32 s96, s84, s85\n s_cmp_lt_u32 s96, 0x800\n s_cselect_b32 %[mode], 2, %[mode]\n"
            "s_cmp_lg_u32 %[mode], 0\n"
            "s_cbranch_scc1 Lend%=\n"
            "s_lshl_b32 s84, s84, 16\n s_lshl_b32 s85, s85, 16\n"
            "v_mov_b32 v104, s84\n v_mov_b32 v105, s85\n"
            "v_mul_f32 v106, 0x3e82d0e5, v104\n v_mul_f32 v108, 0x3e82d0e5, v105\n"
            "v_mul_f32 v104, v104, v104\n v_mul_f32 v105, v105, v105\n"
            "v_add_u32 v106, 0x7fffff, v106\n v_add_u32 v108, 0x7fffff, v108\n"
            "v_mul_f32 v104, %[c4], v104\n v_mul_f32 v105, %[c4], v105\n"
            "v_and_b32 v106, 0x7f800000, v106\n v_and_b32 v108, 0x7f800000, v108\n"
            "v_cmp_lt_f32 vcc, s86, v104\n v_cmp_lt_f32 s[88:89], s87, v105\n"
            "v_add_u32 v86, 0xc00000, v106\n v_add_u32 v87, 0xc00000, v108\n"
            "v_lshrrev_b32 v106, 23, v106\n v_lshrrev_b32 v108, 15, v108\n"
            "v_or_b32 %[exs], v106, v108\n"
            "s_or_b64 vcc, vcc, s[88:89]\n"
            "s_cselect_b32 %[mode], 1, 0\n"
            "s_cbranch_scc1 Lend%=\n"
            "s_waitcnt vmcnt(8)\n"
            "s_mov_b32 m0, %[wb]\n"
            "s_nop 0\n"
            "ds_write_addtid_b32 v40 offset:0\n"
            "ds_write_addtid_b32 v41 offset:256\n"
            "ds_write_addtid_b32 v42 offset:512\n"
            "ds_write_addtid_b32 v43 offset:768\n"
            "ds_write_addtid_b32 v44 offset:16384\n"
            "ds_write_addtid_b32 v45 offset:16640\n"
            "ds_write_addtid_b32 v46 offset:16896\n"
            "ds_write_addtid_b32 v47 offset:17152\n"
            "ds_write_addtid_b32 v48 offset:32768\n"
            "ds_write_addtid_b32 v49 offset:33024\n"
            "ds_write_addtid_b32 v50 offset:33280\n"
            "ds_write_addtid_b32 v51 offset:33536\n"
            "ds_write_addtid_b32 v52 offset:49152\n"
            "ds_write_addtid_b32 v53 offset:49408\n"
            "ds_write_addtid_b32 v54 offset:49664\n"
            "ds_write_addtid_b32 v55 offset:49920\n"
            "s_add_u32 m0, %[wb], 16384\n"
            "s_nop 0\n"
            "ds_write_addtid_b32 v56 offset:49152\n"
            "ds_write_addtid_b32 v57 offset:49408\n"
            "ds_write_addtid_b32 v58 offset:49664\n"
            "ds_write_addtid_b32 v59 offset:49920\n"
            "s_mov_b32 s92, 0\n"
            "s_and_b32 s75, s76, 255\n"
            "s_movk_i32 s74, 1000\n"
            "s_sub_i32 s79, s75, %[j0]\n"
            "s_max_i32 s79, s79, 8\n"
            "s_cmp_eq_u32 s92, 7\n s_cselect_b32 s79, 8, s79\n"
            "s_setprio 3\n"
            "s_waitcnt lgkmcnt(0)\n"
            "s_barrier\n"
            "s_waitcnt vmcnt(7)\n"
            "v_and_b32 v104, 0x1fffc, v8\n"
            "v_and_b32 v105, 0x1fffc, v9\n"
            "v_and_b32 v106, 0x1fffc, v10\n"
            "ds_read_b32 v93, v104\n"
            "ds_read_b32 v95, v105\n"
            "ds_read_b32 v97, v106\n"
            "s_waitcnt lgkmcnt(0)\n"
            "Ls0_%=:\n"
            "s_cmp_gt_i32 s75, s74\n"
            "s_cbranch_scc1 1f\n"
            "s_waitcnt vmcnt(6)\n"
            "1:\n"
            "s_waitcnt vmcnt(11)\n"
            "s_cmp_eq_u32 s75, s79\n"
            "s_cbranch_scc1 Le0_%=\n"
            "Lc0_%=:\n"
            "v_and_b32 v104, 0x1fffc, v12\n"
            "v_and_b32 v105, 0x1fffc, v13\n"
            "v_and_b32 v106, 0x1fffc, v14\n"
            "ds_read_b32 v99, v104\n"
            "ds_read_b32 v101, v105\n"
            "ds_read_b32 v103, v106\n"
            "v_add_u32_dpp v111, v81, v82 row_newbcast:0 row_mask:0xf bank_mask:0x3\n"
            "v_add_u32_dpp v111, v81, v82 row_newbcast:8 row_mask:0xf bank_mask:0xc\n"
            "v_and_b32 v108, 0x7fe, v11\n"
            "v_and_b32 v109, 0x1ff800, v11\n"
            "v_lshrrev_b32 v110, 22, v11\n"
            "v_lshrrev_b32 v112, 15, v8\n"
            "v_lshrrev_b32 v113, 15, v9\n"
            "v_lshrrev_b32 v114, 15, v10\n"
            "v_and_b32 v111, " IDXMASK ", v111\n"
            "v_min_u32 v111, v111, v83\n"
            "buffer_load_dwordx4 v[8:11], v111, %[rs], 0 idxen " RAUX "\n"
            "v_fmamk_f32 v108, v108, 0x7e800000, v84\n"
            "v_fmamk_f32 v109, v109, 0x79800000, v84\n"
            "v_fmamk_f32 v110, v110, 0x7f000000, v84\n"
            "v_or_b32 v112, 3, v112\n"
            "v_or_b32 v113, 3, v113\n"
            "v_or_b32 v114, 3, v114\n"
            "s_sub_u32 s75, s75, 1\n"
            "s_waitcnt lgkmcnt(6)\n"
            "v_lshlrev_b32 v92, 16, v93\n"
            "v_lshlrev_b32 v94, 16, v95\n"
            "v_lshlrev_b32 v96, 16, v97\n"
            "v_pk_fma_f32 v[92:93], v[92:93], v[108:109], v[86:87] op_sel:[0,0,0] op_sel_hi:[1,0,1]\n"
            "v_pk_fma_f32 v[94:95], v[94:95], v[108:109], v[86:87] op_sel:[0,1,0] op_sel_hi:[1,1,1]\n"
            "v_pk_fma_f32 v[96:97], v[96:97], v[110:111], v[86:87] op_sel:[0,0,0] op_sel_hi:[1,0,1]\n"
            "v_lshl_add_u32 v92, v93, 16, v92\n"
            "v_lshl_add_u32 v94, v95, 16, v94\n"
            "v_lshl_add_u32 v96, v97, 16, v96\n"
            "v_sub_u32 v92, v92, v86\n"
            "v_mad_i32_i16 v88, v93, v112, v88\n"
            "ds_add_u32 v112, v92 offset:65533\n"
            "v_sub_u32 v94, v94, v86\n"
            "v_mad_i32_i16 v88, v95, v113, v88\n"
            "ds_add_u32 v113, v94 offset:65533\n"
            "v_sub_u32 v96, v96, v86\n"
            "v_mad_i32_i16 v88, v97, v114, v88\n"
            "ds_add_u32 v114, v96 offset:65533\n"
            "Ls1_%=:\n"
            "s_cmp_gt_i32 s75, s74\n"
            "s_cbranch_scc1 1f\n"
            "s_waitcnt vmcnt(6)\n"
            "1:\n"
            "s_waitcnt vmcnt(11)\n"
            "s_cmp_eq_u32 s75, s79\n"
            "s_cbranch_scc1 Le1_%=\n"
            "Lc1_%=:\n"
            "v_and_b32 v104, 0x1fffc, v16\n"
            "v_and_b32 v105, 0x1fffc, v17\n"
            "v_and_b32 v106, 0x1fffc, v18\n"
            "ds_read_b32 v93, v104\n"
            "ds_read_b32 v95, v105\n"
            "ds_read_b32 v97, v106\n"
            "v_add_u32_dpp v111, v81, v82 row_newbcast:1 row_mask:0xf bank_mask:0x3\n"
            "v_add_u32_dpp v111, v81, v82 row_newbcast:9 row_mask:0xf bank_mask:0xc\n"
            "v_and_b32 v108, 0x7fe, v15\n"
            "v_and_b32 v109, 0x1ff800, v15\n"
            "v_lshrrev_b32 v110, 22, v15\n"
            "v_lshrrev_b32 v112, 15, v12\n"
            "v_lshrrev_b32 v113, 15, v13\n"
            "v_lshrrev_b32 v114, 15, v14\n"
            "v_and_b32 v111, " IDXMASK ", v111\n"
            "v_min_u32 v111, v111, v83\n"
            "buffer_load_dwordx4 v[12:15], v111, %[rs], 0 idxen " RAUX "\n"
            "v_fmamk_f32 v108, v108, 0x7e800000, v84\n"
            "v_fmamk_f32 v109, v109, 0x79800000, v84\n"
            "v_fmamk_f32 v110, v110, 0x7f000000, v84\n"
            "v_or_b32 v112, 3, v112\n"
            "v_or_b32 v113, 3, v113\n"
            "v_or_b32 v114, 3, v114\n"
            "s_sub_u32 s75, s75, 1\n"
            "s_waitcnt lgkmcnt(6)\n"
            "v_lshlrev_b32 v98, 16, v99\n"
            "v_lshlrev_b32 v100, 16, v101\n"
            "v_lshlrev_b32 v102, 16, v103\n"
            "v_pk_fma_f32 v[98:99], v[98:99], v[108:109], v[86:87] op_sel:[0,0,0] op_sel_hi:[1,0,1]\n"
            "v_pk_fma_f32 v[100:101], v[100:101], v[108:109], v[86:87] op_sel:[0,1,0] op_sel_hi:[1,1,1]\n"
            "v_pk_fma_f32 v[102:103], v[102:103], v[110:111], v[86:87] op_sel:[0,0,0] op_sel_hi:[1,0,1]\n"
            "v_lshl_add_u32 v98, v99, 16, v98\n"
            "v_lshl_add_u32 v100, v101, 16, v100\n"
            "v_lshl_add_u32 v102, v103, 16, v102\n"
            "v_sub_u32 v98, v98, v86\n"
            "v_mad_i32_i16 v88, v99, v112, v88\n"
            "ds_add_u32 v112, v98 offset:65533\n"
            "v_sub_u32 v100, v100, v86\n"
            "v_mad_i32_i16 v88, v101, v113, v88\n"
            "ds_add_u32 v113, v100 offset:65533\n"
            "v_sub_u32 v102, v102, v86\n"
            "v_mad_i32_i16 v88, v103, v114, v88\n"
            "ds_add_u32 v114, v102 offset:65533\n"
            "Ls2_%=:\n"
            "s_cmp_gt_i32 s75, s74\n"
            "s_cbranch_scc1 1f\n"
            "s_waitcnt vmcnt(6)\n"
            "1:\n"
            "s_waitcnt vmcnt(11)\n"
            "s_cmp_eq_u32 s75, s79\n"
            "s_cbranch_scc1 Le2_%=\n"
            "Lc2_%=:\n"
            "v_and_b32 v104, 0x1fffc, v20\n"
            "v_and_b32 v105, 0x1fffc, v21\n"
            "v_and_b32 v106, 0x1fffc, v22\n"
            "ds_read_b32 v99, v104\n"
            "ds_read_b32 v101, v105\n"
            "ds_read_b32 v103, v106\n"
            "v_add_u32_dpp v111, v81, v82 row_newbcast:2 row_mask:0xf bank_mask:0x3\n"
            "v_add_u32_dpp v111, v81, v82 row_newbcast:10 row_mask:0xf bank_mask:0xc\n"
            "v_and_b32 v108, 0x7fe, v19\n"
            "v_and_b32 v109, 0x1ff800, v19\n"
            "v_lshrrev_b32 v110, 22, v19\n"
            "v_lshrrev_b32 v112, 15, v16\n"
            "v_lshrrev_b32 v113, 15, v17\n"
            "v_lshrrev_b32 v114, 15, v18\n"
            "v_and_b32 v111, " IDXMASK ", v111\n"
            "v_min_u32 v111, v111, v83\n"
            "buffer_load_dwordx4 v[16:19], v111, %[rs], 0 idxen " RAUX "\n"
            "v_fmamk_f32 v108, v108, 0x7e800000, v84\n"
            "v_fmamk_f32 v109, v109, 0x79800000, v84\n"
            "v_fmamk_f32 v110, v110, 0x7f000000, v84\n"
            "v_or_b32 v112, 3, v112\n"
            "v_or_b32 v113, 3, v113\n"
            "v_or_b32 v114, 3, v114\n"
            "s_sub_u32 s75, s75, 1\n"
            "s_waitcnt lgkmcnt(6)\n"
            "v_lshlrev_b32 v92, 16, v93\n"
            "v_lshlrev_b32 v94, 16, v95\n"
            "v_lshlrev_b32 v96, 16, v97\n"
            "v_pk_fma_f32 v[92:93], v[92:93], v[108:109], v[86:87] op_sel:[0,0,0] op_sel_hi:[1,0,1]\n"
            "v_pk_fma_f32 v[94:95], v[94:95], v[108:109], v[86:87] op_sel:[0,1,0] op_sel_hi:[1,1,1]\n"
            "v_pk_fma_f32 v[96:97], v[96:97], v[110:111], v[86:87] op_sel:[0,0,0] op_sel_hi:[1,0,1]\n"
            "v_lshl_add_u32 v92, v93, 16, v92\n"
            "v_lshl_add_u32 v94, v95, 16, v94\n"
            "v_lshl_add_u32 v96, v97, 16, v96\n"
            "v_sub_u32 v92, v92, v86\n"
            "v_mad_i32_i16 v88, v93, v112, v88\n"
            "ds_add_u32 v112, v92 offset:65533\n"
            "v_sub_u32 v94, v94, v86\n"
            "v_mad_i32_i16 v88, v95, v113, v88\n"
            "ds_add_u32 v113, v94 offset:65533\n"
            "v_sub_u32 v96, v96, v86\n"
            "v_mad_i32_i16 v88, v97, v114, v88\n"
            "ds_add_u32 v114, v96 offset:65533\n"
            "Ls3_%=:\n"
            "s_cmp_gt_i32 s75, s74\n"
            "s_cbranch_scc1 1f\n"
            "s_waitcnt vmcnt(6)\n"
            "1:\n"
            "s_waitcnt vmcnt(11)\n"
            "s_cmp_eq_u32 s75, s79\n"
            "s_cbranch_scc1 Le3_%=\n"
            "Lc3_%=:\n"
            "v_and_b32 v104, 0x1fffc, v24\n"
            "v_and_b32 v105, 0x1fffc, v25\n"
            "v_and_b32 v106, 0x1fffc, v26\n"
            "ds_read_b32 v93, v104\n"
            "ds_read_b32 v95, v105\n"
            "ds_read_b32 v97, v106\n"
            "v_add_u32_dpp v111, v81, v82 row_newbcast:3 row_mask:0xf bank_mask:0x3\n"
            "v_add_u32_dpp v111, v81, v82 row_newbcast:11 row_mask:0xf bank_mask:0xc\n"
            "v_and_b32 v108, 0x7fe, v23\n"
            "v_and_b32 v109, 0x1ff800, v23\n"
            "v_lshrrev_b32 v110, 22, v23\n"
            "v_lshrrev_b32 v112, 15, v20\n"
            "v_lshrrev_b32 v113, 15, v21\n"
            "v_lshrrev_b32 v114, 15, v22\n"
            "v_and_b32 v111, " IDXMASK ", v111\n"
            "v_min_u32 v111, v111, v83\n"
            "buffer_load_dwordx4 v[20:23], v111, %[rs], 0 idxen " RAUX "\n"
            "v_fmamk_f32 v108, v108, 0x7e800000, v84\n"
            "v_fmamk_f32 v109, v109, 0x79800000, v84\n"
            "v_fmamk_f32 v110, v110, 0x7f000000, v84\n"
            "v_or_b32 v112, 3, v112\n"
            "v_or_b32 v113, 3, v113\n"
            "v_or_b32 v114, 3, v114\n"
            "s_sub_u32 s75, s75, 1\n"
            "s_waitcnt lgkmcnt(6)\n"
            "v_lshlrev_b32 v98, 16, v99\n"
            "v_lshlrev_b32 v100, 16, v101\n"
            "v_lshlrev_b32 v102, 16, v103\n"
            "v_pk_fma_f32 v[98:99], v[98:99], v[108:109], v[86:87] op_sel:[0,0,0] op_sel_hi:[1,0,1]\n"
            "v_pk_fma_f32 v[100:101], v[100:101], v[108:109], v[86:87] op_sel:[0,1,0] op_sel_hi:[1,1,1]\n"
            "v_pk_fma_f32 v[102:103], v[102:103], v[110:111], v[86:87] op_sel:[0,0,0] op_sel_hi:[1,0,1]\n"
            "v_lshl_add_u32 v98, v99, 16, v98\n"
            "v_lshl_add_u32 v100, v101, 16, v100\n"
            "v_lshl_add_u32 v102, v103, 16, v102\n"
            "v_sub_u32 v98, v98, v86\n"
            "v_mad_i32_i16 v88, v99, v112, v88\n"
            "ds_add_u32 v112, v98 offset:65533\n"
            "v_sub_u32 v100, v100, v86\n"
            "v_mad_i32_i16 v88, v101, v113, v88\n"
            "ds_add_u32 v113, v100 offset:65533\n"
            "v_sub_u32 v102, v102, v86\n"
            "v_mad_i32_i16 v88, v103, v114, v88\n"
            "ds_add_u32 v114, v102 offset:65533\n"
            "Ls4_%=:\n"
            "s_cmp_gt_i32 s75, s74\n"
            "s_cbranch_scc1 1f\n"
            "s_waitcnt vmcnt(6)\n"
            "1:\n"
            "s_waitcnt vmcnt(11)\n"
            "s_cmp_eq_u32 s75, s79\n"
            "s_cbranch_scc1 Le4_%=\n"
            "Lc4_%=:\n"
            "v_and_b32 v104, 0x1fffc, v28\n"
            "v_and_b32 v105, 0x1fffc, v29\n"
            "v_and_b32 v106, 0x1fffc, v30\n"
            "ds_read_b32 v99, v104\n"
            "ds_read_b32 v101, v105\n"
            "ds_read_b32 v103, v106\n"
            "v_add_u32_dpp v111, v81, v82 row_newbcast:4 row_mask:0xf bank_mask:0x3\n"
            "v_add_u32_dpp v111, v81, v82 row_newbcast:12 row_mask:0xf bank_mask:0xc\n"
            "v_and_b32 v108, 0x7fe, v27\n"
            "v_and_b32 v109, 0x1ff800, v27\n"
            "v_lshrrev_b32 v110, 22, v27\n"
            "v_lshrrev_b32 v112, 15, v24\n"
            "v_lshrrev_b32 v113, 15, v25\n"
            "v_lshrrev_b32 v114, 15, v26\n"
            "v_and_b32 v111, " IDXMASK ", v111\n"
            "v_min_u32 v111, v111, v83\n"
            "buffer_load_dwordx4 v[24:27], v111, %[rs], 0 idxen " RAUX "\n"
            "v_fmamk_f32 v108, v108, 0x7e800000, v84\n"
            "v_fmamk_f32 v109, v109, 0x79800000, v84\n"
            "v_fmamk_f32 v110, v110, 0x7f000000, v84\n"
            "v_or_b32 v112, 3, v112\n"
            "v_or_b32 v113, 3, v113\n"
            "v_or_b32 v114, 3, v114\n"
            "s_sub_u32 s75, s75, 1\n"
            "s_waitcnt lgkmcnt(6)\n"
            "v_lshlrev_b32 v92, 16, v93\n"
            "v_lshlrev_b32 v94, 16, v95\n"
            "v_lshlrev_b32 v96, 16, v97\n"
            "v_pk_fma_f32 v[92:93], v[92:93], v[108:109], v[86:87] op_sel:[0,0,0] op_sel_hi:[1,0,1]\n"
            "v_pk_fma_f32 v[94:95], v[94:95], v[108:109], v[86:87] op_sel:[0,1,0] op_sel_hi:[1,1,1]\n"
            "v_pk_fma_f32 v[96:97], v[96:97], v[110:111], v[86:87] op_sel:[0,0,0] op_sel_hi:[1,0,1]\n"
            "v_lshl_add_u32 v92, v93, 16, v92\n"
            "v_lshl_add_u32 v94, v95, 16, v94\n"
            "v_lshl_add_u32 v96, v97, 16, v96\n"
            "v_sub_u32 v92, v92, v86\n"
            "v_mad_i32_i16 v88, v93, v112, v88\n"
            "ds_add_u32 v112, v92 offset:65533\n"
            "v_sub_u32 v94, v94, v86\n"
            "v_mad_i32_i16 v88, v95, v113, v88\n"
            "ds_add_u32 v113, v94 offset:65533\n"
            "v_sub_u32 v96, v96, v86\n"
            "v_mad_i32_i16 v88, v97, v114, v88\n"
            "ds_add_u32 v114, v96 offset:65533\n"
            "Ls5_%=:\n"
            "s_cmp_gt_i32 s75, s74\n"
            "s_cbranch_scc1 1f\n"
            "s_waitcnt vmcnt(6)\n"
            "1:\n"
            "s_waitcnt vmcnt(11)\n"
            "s_cmp_eq_u32 s75, s79\n"
            "s_cbranch_scc1 Le5_%=\n"
            "Lc5_%=:\n"
            "v_and_b32 v104, 0x1fffc, v32\n"
            "v_and_b32 v105, 0x1fffc, v33\n"
            "v_and_b32 v106, 0x1fffc, v34\n"
            "ds_read_b32 v93, v104\n"
            "ds_read_b32 v95, v105\n"
            "ds_read_b32 v97, v106\n"
            "v_add_u32_dpp v111, v81, v82 row_newbcast:5 row_mask:0xf bank_mask:0x3\n"
            "v_add_u32_dpp v111, v81, v82 row_newbcast:13 row_mask:0xf bank_mask:0xc\n"
            "v_and_b32 v108, 0x7fe, v31\n"
            "v_and_b32 v109, 0x1ff800, v31\n"
            "v_lshrrev_b32 v110, 22, v31\n"
            "v_lshrrev_b32 v112, 15, v28\n"
            "v_lshrrev_b32 v113, 15, v29\n"
            "v_lshrrev_b32 v114, 15, v30\n"
            "v_and_b32 v111, " IDXMASK ", v111\n"
            "v_min_u32 v111, v111, v83\n"
            "buffer_load_dwordx4 v[28:31], v111, %[rs], 0 idxen " RAUX "\n"
            "v_fmamk_f32 v108, v108, 0x7e800000, v84\n"
            "v_fmamk_f32 v109, v109, 0x79800000, v84\n"
            "v_fmamk_f32 v110, v110, 0x7f000000, v84\n"
            "v_or_b32 v112, 3, v112\n"
            "v_or_b32 v113, 3, v113\n"
            "v_or_b32 v114, 3, v114\n"
            "s_sub_u32 s75, s75, 1\n"
            "s_waitcnt lgkmcnt(6)\n"
            "v_lshlrev_b32 v98, 16, v99\n"
            "v_lshlrev_b32 v100, 16, v101\n"
            "v_lshlrev_b32 v102, 16, v103\n"
            "v_pk_fma_f32 v[98:99], v[98:99], v[108:109], v[86:87] op_sel:[0,0,0] op_sel_hi:[1,0,1]\n"
            "v_pk_fma_f32 v[100:101], v[100:101], v[108:109], v[86:87] op_sel:[0,1,0] op_sel_hi:[1,1,1]\n"
            "v_pk_fma_f32 v[102:103], v[102:103], v[110:111], v[86:87] op_sel:[0,0,0] op_sel_hi:[1,0,1]\n"
            "v_lshl_add_u32 v98, v99, 16, v98\n"
            "v_lshl_add_u32 v100, v101, 16, v100\n"
            "v_lshl_add_u32 v102, v103, 16, v102\n"
            "v_sub_u32 v98, v98, v86\n"
            "v_mad_i32_i16 v88, v99, v112, v88\n"
            "ds_add_u32 v112, v98 offset:65533\n"
            "v_sub_u32 v100, v100, v86\n"
            "v_mad_i32_i16 v88, v101, v113, v88\n"
            "ds_add_u32 v113, v100 offset:65533\n"
            "v_sub_u32 v102, v102, v86\n"
            "v_mad_i32_i16 v88, v103, v114, v88\n"
            "ds_add_u32 v114, v102 offset:65533\n"
            "Ls6_%=:\n"
            "s_cmp_gt_i32 s75, s74\n"
            "s_cbranch_scc1 1f\n"
            "s_waitcnt vmcnt(6)\n"
            "1:\n"
            "s_waitcnt vmcnt(11)\n"
            "s_cmp_eq_u32 s75, s79\n"
            "s_cbranch_scc1 Le6_%=\n"
            "Lc6_%=:\n"
            "v_and_b32 v104, 0x1fffc, v36\n"
            "v_and_b32 v105, 0x1fffc, v37\n"
            "v_and_b32 v106, 0x1fffc, v38\n"
            "ds_read_b32 v99, v104\n"
            "ds_read_b32 v101, v105\n"
            "ds_read_b32 v103, v106\n"
            "v_add_u32_dpp v111, v81, v82 row_newbcast:6 row_mask:0xf bank_mask:0x3\n"
            "v_add_u32_dpp v111, v81, v82 row_newbcast:14 row_mask:0xf bank_mask:0xc\n"
            "v_and_b32 v108, 0x7fe, v35\n"
            "v_and_b32 v109, 0x1ff800, v35\n"
            "v_lshrrev_b32 v110, 22, v35\n"
            "v_lshrrev_b32 v112, 15, v32\n"
            "v_lshrrev_b32 v113, 15, v33\n"
            "v_lshrrev_b32 v114, 15, v34\n"
            "v_and_b32 v111, " IDXMASK ", v111\n"
            "v_min_u32 v111, v111, v83\n"
            "buffer_load_dwordx4 v[32:35], v111, %[rs], 0 idxen " RAUX "\n"
            "v_fmamk_f32 v108, v108, 0x7e800000, v84\n"
            "v_fmamk_f32 v109, v109, 0x79800000, v84\n"
            "v_fmamk_f32 v110, v110, 0x7f000000, v84\n"
            "v_or_b32 v112, 3, v112\n"
            "v_or_b32 v113, 3, v113\n"
            "v_or_b32 v114, 3, v114\n"
            "s_sub_u32 s75, s75, 1\n"
            "s_waitcnt lgkmcnt(6)\n"
            "v_lshlrev_b32 v92, 16, v93\n"
            "v_lshlrev_b32 v94, 16, v95\n"
            "v_lshlrev_b32 v96, 16, v97\n"
            "v_pk_fma_f32 v[92:93], v[92:93], v[108:109], v[86:87] op_sel:[0,0,0] op_sel_hi:[1,0,1]\n"
            "v_pk_fma_f32 v[94:95], v[94:95], v[108:109], v[86:87] op_sel:[0,1,0] op_sel_hi:[1,1,1]\n"
            "v_pk_fma_f32 v[96:97], v[96:97], v[110:111], v[86:87] op_sel:[0,0,0] op_sel_hi:[1,0,1]\n"
            "v_lshl_add_u32 v92, v93, 16, v92\n"
            "v_lshl_add_u32 v94, v95, 16, v94\n"
            "v_lshl_add_u32 v96, v97, 16, v96\n"
            "v_sub_u32 v92, v92, v86\n"
            "v_mad_i32_i16 v88, v93, v112, v88\n"
            "ds_add_u32 v112, v92 offset:65533\n"
            "v_sub_u32 v94, v94, v86\n"
            "v_mad_i32_i16 v88, v95, v113, v88\n"
            "ds_add_u32 v113, v94 offset:65533\n"
            "v_sub_u32 v96, v96, v86\n"
            "v_mad_i32_i16 v88, v97, v114, v88\n"
            "ds_add_u32 v114, v96 offset:65533\n"
            "Ls7_%=:\n"
            "s_cmp_gt_i32 s75, s74\n"
            "s_cbranch_scc1 1f\n"
            "s_waitcnt vmcnt(6)\n"
            "1:\n"
            "s_waitcnt vmcnt(11)\n"
            "s_cmp_eq_u32 s75, s79\n"
            "s_cbranch_scc1 Le7_%=\n"
            "Lc7_%=:\n"
            "v_and_b32 v104, 0x1fffc, v8\n"
            "v_and_b32 v105, 0x1fffc, v9\n"
            "v_and_b32 v106, 0x1fffc, v10\n"
            "ds_read_b32 v93, v104\n"
            "ds_read_b32 v95, v105\n"
            "ds_read_b32 v97, v106\n"
            "v_add_u32_dpp v111, v81, v82 row_newbcast:7 row_mask:0xf bank_mask:0x3\n"
            "v_add_u32_dpp v111, v81, v82 row_newbcast:15 row_mask:0xf bank_mask:0xc\n"
            "v_and_b32 v108, 0x7fe, v39\n"
            "v_and_b32 v109, 0x1ff800, v39\n"
            "v_lshrrev_b32 v110, 22, v39\n"
            "v_lshrrev_b32 v112, 15, v36\n"
            "v_lshrrev_b32 v113, 15, v37\n"
            "v_lshrrev_b32 v114, 15, v38\n"
            "v_and_b32 v111, " IDXMASK ", v111\n"
            "v_min_u32 v111, v111, v83\n"
            "buffer_load_dwordx4 v[36:39], v111, %[rs], 0 idxen " RAUX "\n"
            "s_min_u32 s82, s82, 19\n"
            "s_add_u32 s82, s82, 1\n"
            "s_set_gpr_idx_on s82, gpr_idx(SRC0)\n"
            "s_nop 0\n"
            "v_mov_b32 v81, v60\n"
            "s_set_gpr_idx_off\n"
            "v_fmamk_f32 v108, v108, 0x7e800000, v84\n"
            "v_fmamk_f32 v109, v109, 0x79800000, v84\n"
            "v_fmamk_f32 v110, v110, 0x7f000000, v84\n"
            "v_or_b32 v112, 3, v112\n"
            "v_or_b32 v113, 3, v113\n"
            "v_or_b32 v114, 3, v114\n"
            "s_sub_u32 s75, s75, 1\n"
            "s_waitcnt lgkmcnt(6)\n"
            "v_lshlrev_b32 v98, 16, v99\n"
            "v_lshlrev_b32 v100, 16, v101\n"
            "v_lshlrev_b32 v102, 16, v103\n"
            "v_pk_fma_f32 v[98:99], v[98:99], v[108:109], v[86:87] op_sel:[0,0,0] op_sel_hi:[1,0,1]\n"
            "v_pk_fma_f32 v[100:101], v[100:101], v[108:109], v[86:87] op_sel:[0,1,0] op_sel_hi:[1,1,1]\n"
            "v_pk_fma_f32 v[102:103], v[102:103], v[110:111], v[86:87] op_sel:[0,0,0] op_sel_hi:[1,0,1]\n"
            "v_lshl_add_u32 v98, v99, 16, v98\n"
            "v_lshl_add_u32 v100, v101, 16, v100\n"
            "v_lshl_add_u32 v102, v103, 16, v102\n"
            "v_sub_u32 v98, v98, v86\n"
            "v_mad_i32_i16 v88, v99, v112, v88\n"
            "ds_add_u32 v112, v98 offset:65533\n"
            "v_sub_u32 v100, v100, v86\n"
            "v_mad_i32_i16 v88, v101, v113, v88\n"
            "ds_add_u32 v113, v100 offset:65533\n"
            "v_sub_u32 v102, v102, v86\n"
            "v_mad_i32_i16 v88, v103, v114, v88\n"
            "ds_add_u32 v114, v102 offset:65533\n"
            "s_branch Ls0_%=\n"
            "Le0_%=:\n"
            "s_cmp_lt_u32 s75, 8\n"
            "s_cbranch_scc1 1f\n"
            "s_swappc_b64 s[80:81], s[66:67]\n"
            "s_branch Lc0_%=\n"
            "1:\n"
            "s_cmp_lg_u32 s75, 7\n"
            "s_cbranch_scc1 1f\n"
            "s_setprio 2\n"
            "s_mov_b32 s79, 3\n"
            "s_branch Lc0_%=\n"
            "1:\n"
            "s_cmp_lg_u32 s75, 3\n"
            "s_cbranch_scc1 1f\n"
            "s_setprio 1\n"
            "s_mov_b32 s79, 1\n"
            "s_branch Lc0_%=\n"
            "1:\n"
            "s_setprio 0\n"
            "v_add_u32_dpp v111, v81, v82 row_newbcast:0 row_mask:0xf bank_mask:0x3\n"
            "v_add_u32_dpp v111, v81, v82 row_newbcast:8 row_mask:0xf bank_mask:0xc\n"
            "v_and_b32 v108, 0x7fe, v11\n"
            "v_and_b32 v109, 0x1ff800, v11\n"
            "v_lshrrev_b32 v110, 22, v11\n"
            "v_lshrrev_b32 v112, 15, v8\n"
            "v_lshrrev_b32 v113, 15, v9\n"
            "v_lshrrev_b32 v114, 15, v10\n"
            "v_and_b32 v111, " IDXMASK ", v111\n"
            "v_min_u32 v111, v111, v83\n"
            "buffer_load_dwordx4 v[8:11], v111, %[rs], 0 idxen " RAUX "\n"
            "v_fmamk_f32 v108, v108, 0x7e800000, v84\n"
            "v_fmamk_f32 v109, v109, 0x79800000, v84\n"
            "v_fmamk_f32 v110, v110, 0x7f000000, v84\n"
            "v_or_b32 v112, 3, v112\n"
            "v_or_b32 v113, 3, v113\n"
            "v_or_b32 v114, 3, v114\n"
            "s_waitcnt lgkmcnt(3)\n"
            "s_swappc_b64 s[80:81], s[70:71]\n"
            "v_lshlrev_b32 v92, 16, v93\n"
            "v_lshlrev_b32 v94, 16, v95\n"
            "v_lshlrev_b32 v96, 16, v97\n"
            "v_pk_fma_f32 v[92:93], v[92:93], v[108:109], v[86:87] op_sel:[0,0,0] op_sel_hi:[1,0,1]\n"
            "v_pk_fma_f32 v[94:95], v[94:95], v[108:109], v[86:87] op_sel:[0,1,0] op_sel_hi:[1,1,1]\n"
            "v_pk_fma_f32 v[96:97], v[96:97], v[110:111], v[86:87] op_sel:[0,0,0] op_sel_hi:[1,0,1]\n"
            "v_lshl_add_u32 v92, v93, 16, v92\n"
            "v_lshl_add_u32 v94, v95, 16, v94\n"
            "v_lshl_add_u32 v96, v97, 16, v96\n"
            "v_sub_u32 v92, v92, v86\n"
            "v_mad_i32_i16 v88, v93, v112, v88\n"
            "ds_add_u32 v112, v92 offset:65533\n"
            "v_sub_u32 v94, v94, v86\n"
            "v_mad_i32_i16 v88, v95, v113, v88\n"
            "ds_add_u32 v113, v94 offset:65533\n"
            "v_sub_u32 v96, v96, v86\n"
            "v_mad_i32_i16 v88, v97, v114, v88\n"
            "ds_add_u32 v114, v96 offset:65533\n"
            "v_and_b32 v104, 0x1fffc, v12\n"
            "v_and_b32 v105, 0x1fffc, v13\n"
            "v_and_b32 v106, 0x1fffc, v14\n"
            "s_swappc_b64 s[80:81], s[72:73]\n"
            "ds_read_b32 v99, v104\n"
            "ds_read_b32 v101, v105\n"
            "ds_read_b32 v103, v106\n"
            "s_waitcnt lgkmcnt(0)\n"
            "s_branch Ls1_%=\n"
            "Le1_%=:\n"
            "s_cmp_lt_u32 s75, 8\n"
            "s_cbranch_scc1 1f\n"
            "s_swappc_b64 s[80:81], s[66:67]\n"
            "s_branch Lc1_%=\n"
            "1:\n"
            "s_cmp_lg_u32 s75, 7\n"
            "s_cbranch_scc1 1f\n"
            "s_setprio 2\n"
            "s_mov_b32 s79, 3\n"
            "s_branch Lc1_%=\n"
            "1:\n"
            "s_cmp_lg_u32 s75, 3\n"
            "s_cbranch_scc1 1f\n"
            "s_setprio 1\n"
            "s_mov_b32 s79, 1\n"
            "s_branch Lc1_%=\n"
            "1:\n"
            "s_setprio 0\n"
            "v_add_u32_dpp v111, v81, v82 row_newbcast:1 row_mask:0xf bank_mask:0x3\n"
            "v_add_u32_dpp v111, v81, v82 row_newbcast:9 row_mask:0xf bank_mask:0xc\n"
            "v_and_b32 v108, 0x7fe, v15\n"
            "v_and_b32 v109, 0x1ff800, v15\n"
            "v_lshrrev_b32 v110, 22, v15\n"
            "v_lshrrev_b32 v112, 15, v12\n"
            "v_lshrrev_b32 v113, 15, v13\n"
            "v_lshrrev_b32 v114, 15, v14\n"
            "v_and_b32 v111, " IDXMASK ", v111\n"
            "v_min_u32 v111, v111, v83\n"
            "buffer_load_dwordx4 v[12:15], v111, %[rs], 0 idxen " RAUX "\n"
            "v_fmamk_f32 v108, v108, 0x7e800000, v84\n"
            "v_fmamk_f32 v109, v109, 0x79800000, v84\n"
            "v_fmamk_f32 v110, v110, 0x7f000000, v84\n"
            "v_or_b32 v112, 3, v112\n"
            "v_or_b32 v113, 3, v113\n"
            "v_or_b32 v114, 3, v114\n"
            "s_waitcnt lgkmcnt(3)\n"
            "s_swappc_b64 s[80:81], s[70:71]\n"
            "v_lshlrev_b32 v98, 16, v99\n"
            "v_lshlrev_b32 v100, 16, v101\n"
            "v_lshlrev_b32 v102, 16, v103\n"
            "v_pk_fma_f32 v[98:99], v[98:99], v[108:109], v[86:87] op_sel:[0,0,0] op_sel_hi:[1,0,1]\n"
            "v_pk_fma_f32 v[100:101], v[100:101], v[108:109], v[86:87] op_sel:[0,1,0] op_sel_hi:[1,1,1]\n"
            "v_pk_fma_f32 v[102:103], v[102:103], v[110:111], v[86:87] op_sel:[0,0,0] op_sel_hi:[1,0,1]\n"
            "v_lshl_add_u32 v98, v99, 16, v98\n"
            "v_lshl_add_u32 v100, v101, 16, v100\n"
            "v_lshl_add_u32 v102, v103, 16, v102\n"
            "v_sub_u32 v98, v98, v86\n"
            "v_mad_i32_i16 v88, v99, v112, v88\n"
            "ds_add_u32 v112, v98 offset:65533\n"
            "v_sub_u32 v100, v100, v86\n"
            "v_mad_i32_i16 v88, v101, v113, v88\n"
            "ds_add_u32 v113, v100 offset:65533\n"
            "v_sub_u32 v102, v102, v86\n"
            "v_mad_i32_i16 v88, v103, v114, v88\n"
            "ds_add_u32 v114, v102 offset:65533\n"
            "v_and_b32 v104, 0x1fffc, v16\n"
            "v_and_b32 v105, 0x1fffc, v17\n"
            "v_and_b32 v106, 0x1fffc, v18\n"
            "s_swappc_b64 s[80:81], s[72:73]\n"
            "ds_read_b32 v93, v104\n"
            "ds_read_b32 v95, v105\n"
            "ds_read_b32 v97, v106\n"
            "s_waitcnt lgkmcnt(0)\n"
            "s_branch Ls2_%=\n"
            "Le2_%=:\n"
            "s_cmp_lt_u32 s75, 8\n"
            "s_cbranch_scc1 1f\n"
            "s_swappc_b64 s[80:81], s[66:67]\n"
            "s_branch Lc2_%=\n"
            "1:\n"
            "s_cmp_lg_u32 s75, 7\n"
            "s_cbranch_scc1 1f\n"
            "s_setprio 2\n"
            "s_mov_b32 s79, 3\n"
            "s_branch Lc2_%=\n"
            "1:\n"
            "s_cmp_lg_u32 s75, 3\n"
            "s_cbranch_scc1 1f\n"
            "s_setprio 1\n"
            "s_mov_b32 s79, 1\n"
            "s_branch Lc2_%=\n"
            "1:\n"
            "s_setprio 0\n"
            "v_add_u32_dpp v111, v81, v82 row_newbcast:2 row_mask:0xf bank_mask:0x3\n"
            "v_add_u32_dpp v111, v81, v82 row_newbcast:10 row_mask:0xf bank_mask:0xc\n"
            "v_and_b32 v108, 0x7fe, v19\n"
            "v_and_b32 v109, 0x1ff800, v19\n"
            "v_lshrrev_b32 v110, 22, v19\n"
            "v_lshrrev_b32 v112, 15, v16\n"
            "v_lshrrev_b32 v113, 15, v17\n"
            "v_lshrrev_b32 v114, 15, v18\n"
            "v_and_b32 v111, " IDXMASK ", v111\n"
            "v_min_u32 v111, v111, v83\n"
            "buffer_load_dwordx4 v[16:19], v111, %[rs], 0 idxen " RAUX "\n"
            "v_fmamk_f32 v108, v108, 0x7e800000, v84\n"
            "v_fmamk_f32 v109, v109, 0x79800000, v84\n"
            "v_fmamk_f32 v110, v110, 0x7f000000, v84\n"
            "v_or_b32 v112, 3, v112\n"
            "v_or_b32 v113, 3, v113\n"
            "v_or_b32 v114, 3, v114\n"
            "s_waitcnt lgkmcnt(3)\n"
            "s_swappc_b64 s[80:81], s[70:71]\n"
            "v_lshlrev_b32 v92, 16, v93\n"
            "v_lshlrev_b32 v94, 16, v95\n"
            "v_lshlrev_b32 v96, 16, v97\n"
            "v_pk_fma_f32 v[92:93], v[92:93], v[108:109], v[86:87] op_sel:[0,0,0] op_sel_hi:[1,0,1]\n"
            "v_pk_fma_f32 v[94:95], v[94:95], v[108:109], v[86:87] op_sel:[0,1,0] op_sel_hi:[1,1,1]\n"
            "v_pk_fma_f32 v[96:97], v[96:97], v[110:111], v[86:87] op_sel:[0,0,0] op_sel_hi:[1,0,1]\n"
            "v_lshl_add_u32 v92, v93, 16, v92\n"
            "v_lshl_add_u32 v94, v95, 16, v94\n"
            "v_lshl_add_u32 v96, v97, 16, v96\n"
            "v_sub_u32 v92, v92, v86\n"
            "v_mad_i32_i16 v88, v93, v112, v88\n"
            "ds_add_u32 v112, v92 offset:65533\n"
            "v_sub_u32 v94, v94, v86\n"
            "v_mad_i32_i16 v88, v95, v113, v88\n"
            "ds_add_u32 v113, v94 offset:65533\n"
            "v_sub_u32 v96, v96, v86\n"
            "v_mad_i32_i16 v88, v97, v114, v88\n"
            "ds_add_u32 v114, v96 offset:65533\n"
            "v_and_b32 v104, 0x1fffc, v20\n"
            "v_and_b32 v105, 0x1fffc, v21\n"
            "v_and_b32 v106, 0x1fffc, v22\n"
            "s_swappc_b64 s[80:81], s[72:73]\n"
            "ds_read_b32 v99, v104\n"
            "ds_read_b32 v101, v105\n"
            "ds_read_b32 v103, v106\n"
            "s_waitcnt lgkmcnt(0)\n"
            "s_branch Ls3_%=\n"
            "Le3_%=:\n"
            "s_cmp_lt_u32 s75, 8\n"
            "s_cbranch_scc1 1f\n"
            "s_swappc_b64 s[80:81], s[66:67]\n"
            "s_branch Lc3_%=\n"
            "1:\n"
            "s_cmp_lg_u32 s75, 7\n"
            "s_cbranch_scc1 1f\n"
            "s_setprio 2\n"
            "s_mov_b32 s79, 3\n"
            "s_branch Lc3_%=\n"
            "1:\n"
            "s_cmp_lg_u32 s75, 3\n"
            "s_cbranch_scc1 1f\n"
            "s_setprio 1\n"
            "s_mov_b32 s79, 1\n"
            "s_branch Lc3_%=\n"
            "1:\n"
            "s_setprio 0\n"
            "v_add_u32_dpp v111, v81, v82 row_newbcast:3 row_mask:0xf bank_mask:0x3\n"
            "v_add_u32_dpp v111, v81, v82 row_newbcast:11 row_mask:0xf bank_mask:0xc\n"
            "v_and_b32 v108, 0x7fe, v23\n"
            "v_and_b32 v109, 0x1ff800, v23\n"
            "v_lshrrev_b32 v110, 22, v23\n"
            "v_lshrrev_b32 v112, 15, v20\n"
            "v_lshrrev_b32 v113, 15, v21\n"
            "v_lshrrev_b32 v114, 15, v22\n"
            "v_and_b32 v111, " IDXMASK ", v111\n"
            "v_min_u32 v111, v111, v83\n"
            "buffer_load_dwordx4 v[20:23], v111, %[rs], 0 idxen " RAUX "\n"
            "v_fmamk_f32 v108, v108, 0x7e800000, v84\n"
            "v_fmamk_f32 v109, v109, 0x79800000, v84\n"
            "v_fmamk_f32 v110, v110, 0x7f000000, v84\n"
            "v_or_b32 v112, 3, v112\n"
            "v_or_b32 v113, 3, v113\n"
            "v_or_b32 v114, 3, v114\n"
            "s_waitcnt lgkmcnt(3)\n"
            "s_swappc_b64 s[80:81], s[70:71]\n"
            "v_lshlrev_b32 v98, 16, v99\n"
            "v_lshlrev_b32 v100, 16, v101\n"
            "v_lshlrev_b32 v102, 16, v103\n"
            "v_pk_fma_f32 v[98:99], v[98:99], v[108:109], v[86:87] op_sel:[0,0,0] op_sel_hi:[1,0,1]\n"
            "v_pk_fma_f32 v[100:101], v[100:101], v[108:109], v[86:87] op_sel:[0,1,0] op_sel_hi:[1,1,1]\n"
            "v_pk_fma_f32 v[102:103], v[102:103], v[110:111], v[86:87] op_sel:[0,0,0] op_sel_hi:[1,0,1]\n"
            "v_lshl_add_u32 v98, v99, 16, v98\n"
            "v_lshl_add_u32 v100, v101, 16, v100\n"
            "v_lshl_add_u32 v102, v103, 16, v102\n"
            "v_sub_u32 v98, v98, v86\n"
            "v_mad_i32_i16 v88, v99, v112, v88\n"
            "ds_add_u32 v112, v98 offset:65533\n"
            "v_sub_u32 v100, v100, v86\n"
            "v_mad_i32_i16 v88, v101, v113, v88\n"
            "ds_add_u32 v113, v100 offset:65533\n"
            "v_sub_u32 v102, v102, v86\n"
            "v_mad_i32_i16 v88, v103, v114, v88\n"
            "ds_add_u32 v114, v102 offset:65533\n"
            "v_and_b32 v104, 0x1fffc, v24\n"
            "v_and_b32 v105, 0x1fffc, v25\n"
            "v_and_b32 v106, 0x1fffc, v26\n"
            "s_swappc_b64 s[80:81], s[72:73]\n"
            "ds_read_b32 v93, v104\n"
            "ds_read_b32 v95, v105\n"
            "ds_read_b32 v97, v106\n"
            "s_waitcnt lgkmcnt(0)\n"
            "s_branch Ls4_%=\n"
            "Le4_%=:\n"
            "s_cmp_lt_u32 s75, 8\n"
            "s_cbranch_scc1 1f\n"
            "s_swappc_b64 s[80:81], s[66:67]\n"
            "s_branch Lc4_%=\n"
            "1:\n"
            "s_cmp_lg_u32 s75, 7\n"
            "s_cbranch_scc1 1f\n"
            "s_setprio 2\n"
            "s_mov_b32 s79, 3\n"
            "s_branch Lc4_%=\n"
            "1:\n"
            "s_cmp_lg_u32 s75, 3\n"
            "s_cbranch_scc1 1f\n"
            "s_setprio 1\n"
            "s_mov_b32 s79, 1\n"
            "s_branch Lc4_%=\n"
            "1:\n"
            "s_setprio 0\n"
            "v_add_u32_dpp v111, v81, v82 row_newbcast:4 row_mask:0xf bank_mask:0x3\n"
            "v_add_u32_dpp v111, v81, v82 row_newbcast:12 row_mask:0xf bank_mask:0xc\n"
            "v_and_b32 v108, 0x7fe, v27\n"
            "v_and_b32 v109, 0x1ff800, v27\n"
            "v_lshrrev_b32 v110, 22, v27\n"
            "v_lshrrev_b32 v112, 15, v24\n"
            "v_lshrrev_b32 v113, 15, v25\n"
            "v_lshrrev_b32 v114, 15, v26\n"
            "v_and_b32 v111, " IDXMASK ", v111\n"
            "v_min_u32 v111, v111, v83\n"
            "buffer_load_dwordx4 v[24:27], v111, %[rs], 0 idxen " RAUX "\n"
            "v_fmamk_f32 v108, v108, 0x7e800000, v84\n"
            "v_fmamk_f32 v109, v109, 0x79800000, v84\n"
            "v_fmamk_f32 v110, v110, 0x7f000000, v84\n"
            "v_or_b32 v112, 3, v112\n"
            "v_or_b32 v113, 3, v113\n"
            "v_or_b32 v114, 3, v114\n"
            "s_waitcnt lgkmcnt(3)\n"
            "s_swappc_b64 s[80:81], s[70:71]\n"
            "v_lshlrev_b32 v92, 16, v93\n"
            "v_lshlrev_b32 v94, 16, v95\n"
            "v_lshlrev_b32 v96, 16, v97\n"
            "v_pk_fma_f32 v[92:93], v[92:93], v[108:109], v[86:87] op_sel:[0,0,0] op_sel_hi:[1,0,1]\n"
            "v_pk_fma_f32 v[94:95], v[94:95], v[108:109], v[86:87] op_sel:[0,1,0] op_sel_hi:[1,1,1]\n"
            "v_pk_fma_f32 v[96:97], v[96:97], v[110:111], v[86:87] op_sel:[0,0,0] op_sel_hi:[1,0,1]\n"
            "v_lshl_add_u32 v92, v93, 16, v92\n"
            "v_lshl_add_u32 v94, v95, 16, v94\n"
            "v_lshl_add_u32 v96, v97, 16, v96\n"
            "v_sub_u32 v92, v92, v86\n"
            "v_mad_i32_i16 v88, v93, v112, v88\n"
            "ds_add_u32 v112, v92 offset:65533\n"
            "v_sub_u32 v94, v94, v86\n"
            "v_mad_i32_i16 v88, v95, v113, v88\n"
            "ds_add_u32 v113, v94 offset:65533\n"
            "v_sub_u32 v96, v96, v86\n"
            "v_mad_i32_i16 v88, v97, v114, v88\n"
            "ds_add_u32 v114, v96 offset:65533\n"
            "v_and_b32 v104, 0x1fffc, v28\n"
            "v_and_b32 v105, 0x1fffc, v29\n"
            "v_and_b32 v106, 0x1fffc, v30\n"
            "s_swappc_b64 s[80:81], s[72:73]\n"
            "ds_read_b32 v99, v104\n"
            "ds_read_b32 v101, v105\n"
            "ds_read_b32 v103, v106\n"
            "s_waitcnt lgkmcnt(0)\n"
            "s_branch Ls5_%=\n"
            "Le5_%=:\n"
            "s_cmp_lt_u32 s75, 8\n"
            "s_cbranch_scc1 1f\n"
            "s_swappc_b64 s[80:81], s[66:67]\n"
            "s_branch Lc5_%=\n"
            "1:\n"
            "s_cmp_lg_u32 s75, 7\n"
            "s_cbranch_scc1 1f\n"
            "s_setprio 2\n"
            "s_mov_b32 s79, 3\n"
            "s_branch Lc5_%=\n"
            "1:\n"
            "s_cmp_lg_u32 s75, 3\n"
            "s_cbranch_scc1 1f\n"
            "s_setprio 1\n"
            "s_mov_b32 s79, 1\n"
            "s_branch Lc5_%=\n"
            "1:\n"
            "s_setprio 0\n"
            "v_add_u32_dpp v111, v81, v82 row_newbcast:5 row_mask:0xf bank_mask:0x3\n"
            "v_add_u32_dpp v111, v81, v82 row_newbcast:13 row_mask:0xf bank_mask:0xc\n"
            "v_and_b32 v108, 0x7fe, v31\n"
            "v_and_b32 v109, 0x1ff800, v31\n"
            "v_lshrrev_b32 v110, 22, v31\n"
            "v_lshrrev_b32 v112, 15, v28\n"
            "v_lshrrev_b32 v113, 15, v29\n"
            "v_lshrrev_b32 v114, 15, v30\n"
            "v_and_b32 v111, " IDXMASK ", v111\n"
            "v_min_u32 v111, v111, v83\n"
            "buffer_load_dwordx4 v[28:31], v111, %[rs], 0 idxen " RAUX "\n"
            "v_fmamk_f32 v108, v108, 0x7e800000, v84\n"
            "v_fmamk_f32 v109, v109, 0x79800000, v84\n"
            "v_fmamk_f32 v110, v110, 0x7f000000, v84\n"
            "v_or_b32 v112, 3, v112\n"
            "v_or_b32 v113, 3, v113\n"
            "v_or_b32 v114, 3, v114\n"
            "s_waitcnt lgkmcnt(3)\n"
            "s_swappc_b64 s[80:81], s[70:71]\n"
            "v_lshlrev_b32 v98, 16, v99\n"
            "v_lshlrev_b32 v100, 16, v101\n"
            "v_lshlrev_b32 v102, 16, v103\n"
            "v_pk_fma_f32 v[98:99], v[98:99], v[108:109], v[86:87] op_sel:[0,0,0] op_sel_hi:[1,0,1]\n"
            "v_pk_fma_f32 v[100:101], v[100:101], v[108:109], v[86:87] op_sel:[0,1,0] op_sel_hi:[1,1,1]\n"
            "v_pk_fma_f32 v[102:103], v[102:103], v[110:111], v[86:87] op_sel:[0,0,0] op_sel_hi:[1,0,1]\n"
            "v_lshl_add_u32 v98, v99, 16, v98\n"
            "v_lshl_add_u32 v100, v101, 16, v100\n"
            "v_lshl_add_u32 v102, v103, 16, v102\n"
            "v_sub_u32 v98, v98, v86\n"
            "v_mad_i32_i16 v88, v99, v112, v88\n"
            "ds_add_u32 v112, v98 offset:65533\n"
            "v_sub_u32 v100, v100, v86\n"
            "v_mad_i32_i16 v88, v101, v113, v88\n"
            "ds_add_u32 v113, v100 offset:65533\n"
            "v_sub_u32 v102, v102, v86\n"
            "v_mad_i32_i16 v88, v103, v114, v88\n"
            "ds_add_u32 v114, v102 offset:65533\n"
            "v_and_b32 v104, 0x1fffc, v32\n"
            "v_and_b32 v105, 0x1fffc, v33\n"
            "v_and_b32 v106, 0x1fffc, v34\n"
            "s_swappc_b64 s[80:81], s[72:73]\n"
            "ds_read_b32 v93, v104\n"
            "ds_read_b32 v95, v105\n"
            "ds_read_b32 v97, v106\n"
            "s_waitcnt lgkmcnt(0)\n"
            "s_branch Ls6_%=\n"
            "Le6_%=:\n"
            "s_cmp_lt_u32 s75, 8\n"
            "s_cbranch_scc1 1f\n"
            "s_swappc_b64 s[80:81], s[66:67]\n"
            "s_branch Lc6_%=\n"
            "1:\n"
            "s_cmp_lg_u32 s75, 7\n"
            "s_cbranch_scc1 1f\n"
            "s_setprio 2\n"
            "s_mov_b32 s79, 3\n"
            "s_branch Lc6_%=\n"
            "1:\n"
            "s_cmp_lg_u32 s75, 3\n"
            "s_cbranch_scc1 1f\n"
            "s_setprio 1\n"
            "s_mov_b32 s79, 1\n"
            "s_branch Lc6_%=\n"
            "1:\n"
            "s_setprio 0\n"
            "v_add_u32_dpp v111, v81, v82 row_newbcast:6 row_mask:0xf bank_mask:0x3\n"
            "v_add_u32_dpp v111, v81, v82 row_newbcast:14 row_mask:0xf bank_mask:0xc\n"
            "v_and_b32 v108, 0x7fe, v35\n"
            "v_and_b32 v109, 0x1ff800, v35\n"
            "v_lshrrev_b32 v110, 22, v35\n"
            "v_lshrrev_b32 v112, 15, v32\n"
            "v_lshrrev_b32 v113, 15, v33\n"
            "v_lshrrev_b32 v114, 15, v34\n"
            "v_and_b32 v111, " IDXMASK ", v111\n"
            "v_min_u32 v111, v111, v83\n"
            "buffer_load_dwordx4 v[32:35], v111, %[rs], 0 idxen " RAUX "\n"
            "v_fmamk_f32 v108, v108, 0x7e800000, v84\n"
            "v_fmamk_f32 v109, v109, 0x79800000, v84\n"
            "v_fmamk_f32 v110, v110, 0x7f000000, v84\n"
            "v_or_b32 v112, 3, v112\n"
            "v_or_b32 v113, 3, v113\n"
            "v_or_b32 v114, 3, v114\n"
            "s_waitcnt lgkmcnt(3)\n"
            "s_swappc_b64 s[80:81], s[70:71]\n"
            "v_lshlrev_b32 v92, 16, v93\n"
            "v_lshlrev_b32 v94, 16, v95\n"
            "v_lshlrev_b32 v96, 16, v97\n"
            "v_pk_fma_f32 v[92:93], v[92:93], v[108:109], v[86:87] op_sel:[0,0,0] op_sel_hi:[1,0,1]\n"
            "v_pk_fma_f32 v[94:95], v[94:95], v[108:109], v[86:87] op_sel:[0,1,0] op_sel_hi:[1,1,1]\n"
            "v_pk_fma_f32 v[96:97], v[96:97], v[110:111], v[86:87] op_sel:[0,0,0] op_sel_hi:[1,0,1]\n"
            "v_lshl_add_u32 v92, v93, 16, v92\n"
            "v_lshl_add_u32 v94, v95, 16, v94\n"
            "v_lshl_add_u32 v96, v97, 16, v96\n"
            "v_sub_u32 v92, v92, v86\n"
            "v_mad_i32_i16 v88, v93, v112, v88\n"
            "ds_add_u32 v112, v92 offset:65533\n"
            "v_sub_u32 v94, v94, v86\n"
            "v_mad_i32_i16 v88, v95, v113, v88\n"
            "ds_add_u32 v113, v94 offset:65533\n"
            "v_sub_u32 v96, v96, v86\n"
            "v_mad_i32_i16 v88, v97, v114, v88\n"
            "ds_add_u32 v114, v96 offset:65533\n"
            "v_and_b32 v104, 0x1fffc, v36\n"
            "v_and_b32 v105, 0x1fffc, v37\n"
            "v_and_b32 v106, 0x1fffc, v38\n"
            "s_swappc_b64 s[80:81], s[72:73]\n"
            "ds_read_b32 v99, v104\n"
            "ds_read_b32 v101, v105\n"
            "ds_read_b32 v103, v106\n"
            "s_waitcnt lgkmcnt(0)\n"
            "s_branch Ls7_%=\n"
            "Le7_%=:\n"
            "s_cmp_lt_u32 s75, 8\n"
            "s_cbranch_scc1 1f\n"
            "s_swappc_b64 s[80:81], s[66:67]\n"
            "s_branch Lc7_%=\n"
            "1:\n"
            "s_cmp_lg_u32 s75, 7\n"
            "s_cbranch_scc1 1f\n"
            "s_setprio 2\n"
            "s_mov_b32 s79, 3\n"
            "s_branch Lc7_%=\n"
            "1:\n"
            "s_cmp_lg_u32 s75, 3\n"
            "s_cbranch_scc1 1f\n"
            "s_setprio 1\n"
            "s_mov_b32 s79, 1\n"
            "s_branch Lc7_%=\n"
            "1:\n"
            "s_setprio 0\n"
            "v_add_u32_dpp v111, v81, v82 row_newbcast:7 row_mask:0xf bank_mask:0x3\n"
            "v_add_u32_dpp v111, v81, v82 row_newbcast:15 row_mask:0xf bank_mask:0xc\n"
            "v_and_b32 v108, 0x7fe, v39\n"
            "v_and_b32 v109, 0x1ff800, v39\n"
            "v_lshrrev_b32 v110, 22, v39\n"
            "v_lshrrev_b32 v112, 15, v36\n"
            "v_lshrrev_b32 v113, 15, v37\n"
            "v_lshrrev_b32 v114, 15, v38\n"
            "v_and_b32 v111, " IDXMASK ", v111\n"
            "v_min_u32 v111, v111, v83\n"
            "buffer_load_dwordx4 v[36:39], v111, %[rs], 0 idxen " RAUX "\n"
            "s_min_u32 s82, s82, 19\n"
            "s_add_u32 s82, s82, 1\n"
            "s_set_gpr_idx_on s82, gpr_idx(SRC0)\n"
            "s_nop 0\n"
            "v_mov_b32 v81, v60\n"
            "s_set_gpr_idx_off\n"
            "v_fmamk_f32 v108, v108, 0x7e800000, v84\n"
            "v_fmamk_f32 v109, v109, 0x79800000, v84\n"
            "v_fmamk_f32 v110, v110, 0x7f000000, v84\n"
            "v_or_b32 v112, 3, v112\n"
            "v_or_b32 v113, 3, v113\n"
            "v_or_b32 v114, 3, v114\n"
            "s_waitcnt lgkmcnt(3)\n"
            "s_swappc_b64 s[80:81], s[70:71]\n"
            "v_lshlrev_b32 v98, 16, v99\n"
            "v_lshlrev_b32 v100, 16, v101\n"
            "v_lshlrev_b32 v102, 16, v103\n"
            "v_pk_fma_f32 v[98:99], v[98:99], v[108:109], v[86:87] op_sel:[0,0,0] op_sel_hi:[1,0,1]\n"
            "v_pk_fma_f32 v[100:101], v[100:101], v[108:109], v[86:87] op_sel:[0,1,0] op_sel_hi:[1,1,1]\n"
            "v_pk_fma_f32 v[102:103], v[102:103], v[110:111], v[86:87] op_sel:[0,0,0] op_sel_hi:[1,0,1]\n"
            "v_lshl_add_u32 v98, v99, 16, v98\n"
            "v_lshl_add_u32 v100, v101, 16, v100\n"
            "v_lshl_add_u32 v102, v103, 16, v102\n"
            "v_sub_u32 v98, v98, v86\n"
            "v_mad_i32_i16 v88, v99, v112, v88\n"
            "ds_add_u32 v112, v98 offset:65533\n"
            "v_sub_u32 v100, v100, v86\n"
            "v_mad_i32_i16 v88, v101, v113, v88\n"
            "ds_add_u32 v113, v100 offset:65533\n"
            "v_sub_u32 v102, v102, v86\n"
            "v_mad_i32_i16 v88, v103, v114, v88\n"
            "ds_add_u32 v114, v102 offset:65533\n"
            "v_and_b32 v104, 0x1fffc, v8\n"
            "v_and_b32 v105, 0x1fffc, v9\n"
            "v_and_b32 v106, 0x1fffc, v10\n"
            "s_swappc_b64 s[80:81], s[72:73]\n"
            "ds_read_b32 v93, v104\n"
            "ds_read_b32 v95, v105\n"
            "ds_read_b32 v97, v106\n"
            "s_waitcnt lgkmcnt(0)\n"
            "s_branch Ls0_%=\n"
            "Lchg1%=:\n"
            "s_add_u32 s92, s92, 1\n"
            "s_cmp_eq_u32 s92, 8\n"
            "s_cbranch_scc1 1f\n"
            "s_barrier\n"
            "s_mov_b32 m0, %[wb]\n"
            "s_nop 0\n"
            "ds_write_addtid_b32 v40 offset:0\n"
            "ds_write_addtid_b32 v41 offset:256\n"
            "ds_write_addtid_b32 v42 offset:512\n"
            "ds_write_addtid_b32 v43 offset:768\n"
            "ds_write_addtid_b32 v44 offset:16384\n"
            "ds_write_addtid_b32 v45 offset:16640\n"
            "ds_write_addtid_b32 v46 offset:16896\n"
            "ds_write_addtid_b32 v47 offset:17152\n"
            "ds_write_addtid_b32 v48 offset:32768\n"
            "ds_write_addtid_b32 v49 offset:33024\n"
            "ds_write_addtid_b32 v50 offset:33280\n"
            "ds_write_addtid_b32 v51 offset:33536\n"
            "ds_write_addtid_b32 v52 offset:49152\n"
            "ds_write_addtid_b32 v53 offset:49408\n"
            "ds_write_addtid_b32 v54 offset:49664\n"
            "ds_write_addtid_b32 v55 offset:49920\n"
            "s_add_u32 m0, %[wb], 16384\n"
            "s_nop 0\n"
            "ds_write_addtid_b32 v56 offset:49152\n"
            "ds_write_addtid_b32 v57 offset:49408\n"
            "ds_write_addtid_b32 v58 offset:49664\n"
            "ds_write_addtid_b32 v59 offset:49920\n"
            "1:\n"
            "s_setpc_b64 s[80:81]\n"
            "Lchg2%=:\n"
            "s_cmp_eq_u32 s92, 8\n"
            "s_cbranch_scc1 Lfin%=\n"
            "s_lshr_b64 s[76:77], s[76:77], 8\n"
            "s_and_b32 s75, s76, 255\n"
            "s_movk_i32 s74, 1000\n"
            "s_sub_i32 s79, s75, %[j0]\n"
            "s_max_i32 s79, s79, 8\n"
            "s_cmp_eq_u32 s92, 7\n s_cselect_b32 s79, 8, s79\n"
            "s_setprio 3\n"
            "s_waitcnt lgkmcnt(3)\n"
            "s_barrier\n"
            "s_setpc_b64 s[80:81]\n"
            "Lchg3%=:\n"
            "s_sub_u32 s74, s75, 7\n"
            "s_mov_b32 s79, 7\n"
            "s_cmp_eq_u32 s92, 7\n"
            "s_cbranch_scc1 1f\n"
            "s_add_u32 s94, s94, %[i4]\n"
            "buffer_load_dwordx4 v[40:43], %[tid16], %[rm], s94 offen\n"
            "s_add_u32 s96, s94, 16384\n"
            "buffer_load_dwordx4 v[44:47], %[tid16], %[rm], s96 offen\n"
            "s_add_u32 s96, s94, 32768\n"
            "buffer_load_dwordx4 v[48:51], %[tid16], %[rm], s96 offen\n"
            "s_add_u32 s96, s94, 49152\n"
            "buffer_load_dwordx4 v[52:55], %[tid16], %[rm], s96 offen\n"
            "s_add_u32 s96, s94, 65536\n"
            "buffer_load_dwordx4 v[56:59], %[tid16], %[rm], s96 offen\n"
            "s_branch 2f\n"
            "1:\n"
            "buffer_load_ushort v48, %[ed], %[rd], 0 offen " NTH "\n"
            "buffer_load_ushort v49, %[ed], %[rd], 0 offen offset:2 " NTH "\n"
            "buffer_load_ushort v50, %[ed], %[rd], %[e1] offen " NTH "\n"
            "buffer_load_ushort v51, %[ed], %[rd], %[e1] offen offset:2 " NTH "\n"
            "v_lshlrev_b32 v52, 2, %[ed]\n"
            "s_lshl_b32 s96, %[e1], 2\n"
            "buffer_load_dwordx4 v[40:43], v52, %[rmm], 0 offen " NTH "\n"
            "buffer_load_dwordx4 v[44:47], v52, %[rmm], s96 offen " NTH "\n"
            "2:\n"
            "s_setpc_b64 s[80:81]\n"
            "Lfin%=:\n"
            "s_waitcnt vmcnt(0) lgkmcnt(0)\n"
            "s_barrier\n"
            "ds_read_b128 v[8:11], v89 offset:0\n"
            "ds_read_b128 v[12:15], v89 offset:16384\n"
            "ds_read_b128 v[16:19], v89 offset:32768\n"
            "ds_read_b128 v[20:23], v89 offset:49152\n"
            "ds_read_b128 v[24:27], v90\n"
            "v_subrev_u32 v117, 65533, v89\n"
            "v_mov_b32 v118, 0\n"
            "v_bfe_u32 v116, %[tid16], 9, 3\n"
            "v_cmp_eq_u32 vcc, %[q], v116\n"
            "v_bfrev_b32 v115, 1\n"
            "s_nop 0\n"
            "v_cndmask_b32 v115, %[tid16], v115, vcc\n"
            "s_mov_b32 s96, 16384\n s_mov_b32 s97, 32768\n s_mov_b32 s92, 49152\n s_mov_b32 s93, 65536\n"
            "s_waitcnt lgkmcnt(4)\n"
            "buffer_store_dwordx4 v[8:11], v115, %[rp0], 0 offen sc1\n"
            "s_waitcnt lgkmcnt(3)\n"
            "buffer_store_dwordx4 v[12:15], v115, %[rp0], s96 offen sc1\n"
            "s_waitcnt lgkmcnt(2)\n"
            "buffer_store_dwordx4 v[16:19], v115, %[rp0], s97 offen sc1\n"
            "s_waitcnt lgkmcnt(1)\n"
            "buffer_store_dwordx4 v[20:23], v115, %[rp0], s92 offen sc1\n"
            "s_waitcnt lgkmcnt(0)\n"
            "buffer_store_dwordx4 v[24:27], v115, %[rp0], s93 offen sc1\n"
            "v_add_u32 v112, 0x8000, v8\n"
            "v_add_u32 v116, 0, v117\n"
            "v_mad_i32_i16 v118, v112, v116, v118 op_sel:[1,0,0,0]\n"
            "v_add_u32 v112, 0x8000, v9\n"
            "v_add_u32 v116, 4, v117\n"
            "v_mad_i32_i16 v118, v112, v116, v118 op_sel:[1,0,0,0]\n"
            "v_add_u32 v112, 0x8000, v10\n"
            "v_add_u32 v116, 8, v117\n"
            "v_mad_i32_i16 v118, v112, v116, v118 op_sel:[1,0,0,0]\n"
            "v_add_u32 v112, 0x8000, v11\n"
            "v_add_u32 v116, 12, v117\n"
            "v_mad_i32_i16 v118, v112, v116, v118 op_sel:[1,0,0,0]\n"
            "v_add_u32 v117, 16384, v117\n"
            "v_add_u32 v112, 0x8000, v12\n"
            "v_add_u32 v116, 0, v117\n"
            "v_mad_i32_i16 v118, v112, v116, v118 op_sel:[1,0,0,0]\n"
            "v_add_u32 v112, 0x8000, v13\n"
            "v_add_u32 v116, 4, v117\n"
            "v_mad_i32_i16 v118, v112, v116, v118 op_sel:[1,0,0,0]\n"
            "v_add_u32 v112, 0x8000, v14\n"
            "v_add_u32 v116, 8, v117\n"
            "v_mad_i32_i16 v118, v112, v116, v118 op_sel:[1,0,0,0]\n"
            "v_add_u32 v112, 0x8000, v15\n"
            "v_add_u32 v116, 12, v117\n"
            "v_mad_i32_i16 v118, v112, v116, v118 op_sel:[1,0,0,0]\n"
            "v_add_u32 v117, 16384, v117\n"
            "v_add_u32 v112, 0x8000, v16\n"
            "v_add_u32 v116, 0, v117\n"
            "v_mad_i32_i16 v118, v112, v116, v118 op_sel:[1,0,0,0]\n"
            "v_add_u32 v112, 0x8000, v17\n"
            "v_add_u32 v116, 4, v117\n"
            "v_mad_i32_i16 v118, v112, v116, v118 op_sel:[1,0,0,0]\n"
            "v_add_u32 v112, 0x8000, v18\n"
            "v_add_u32 v116, 8, v117\n"
            "v_mad_i32_i16 v118, v112, v116, v118 op_sel:[1,0,0,0]\n"
            "v_add_u32 v112, 0x8000, v19\n"
            "v_add_u32 v116, 12, v117\n"
            "v_mad_i32_i16 v118, v112, v116, v118 op_sel:[1,0,0,0]\n"
            "v_add_u32 v117, 16384, v117\n"
            "v_add_u32 v112, 0x8000, v20\n"
            "v_add_u32 v116, 0, v117\n"
            "v_mad_i32_i16 v118, v112, v116, v118 op_sel:[1,0,0,0]\n"
            "v_add_u32 v112, 0x8000, v21\n"
            "v_add_u32 v116, 4, v117\n"
            "v_mad_i32_i16 v118, v112, v116, v118 op_sel:[1,0,0,0]\n"
            "v_add_u32 v112, 0x8000, v22\n"
            "v_add_u32 v116, 8, v117\n"
            "v_mad_i32_i16 v118, v112, v116, v118 op_sel:[1,0,0,0]\n"
            "v_add_u32 v112, 0x8000, v23\n"
            "v_add_u32 v116, 12, v117\n"
            "v_mad_i32_i16 v118, v112, v116, v118 op_sel:[1,0,0,0]\n"
            "v_add_u32 v117, 16384, v117\n"
            "v_add_u32 v112, 0x8000, v24\n"
            "v_add_u32 v116, 0, v117\n"
            "v_mad_i32_i16 v118, v112, v116, v118 op_sel:[1,0,0,0]\n"
            "v_add_u32 v112, 0x8000, v25\n"
            "v_add_u32 v116, 4, v117\n"
            "v_mad_i32_i16 v118, v112, v116, v118 op_sel:[1,0,0,0]\n"
            "v_add_u32 v112, 0x8000, v26\n"
            "v_add_u32 v116, 8, v117\n"
            "v_mad_i32_i16 v118, v112, v116, v118 op_sel:[1,0,0,0]\n"
            "v_add_u32 v112, 0x8000, v27\n"
            "v_add_u32 v116, 12, v117\n"
            "v_mad_i32_i16 v118, v112, v116, v118 op_sel:[1,0,0,0]\n"
            "v_add_u32 v117, 16384, v117\n"
            "v_sub_u32 v118, v88, v118\n"
            "v_mov_b32 v116, %[wb]\n"
            "v_mov_b32 v117, %[bad]\n"
            "v_add_u32_dpp v118, v118, v118 row_shr:1 row_mask:0xf bank_mask:0xf bound_ctrl:1\n"
            "s_nop 1\n"
            "v_add_u32_dpp v118, v118, v118 row_shr:2 row_mask:0xf bank_mask:0xf bound_ctrl:1\n"
            "s_nop 1\n"
            "v_add_u32_dpp v118, v118, v118 row_shr:4 row_mask:0xf bank_mask:0xf bound_ctrl:1\n"
            "s_nop 1\n"
            "v_add_u32_dpp v118, v118, v118 row_shr:8 row_mask:0xf bank_mask:0xf bound_ctrl:1\n"
            "s_nop 1\n"
            "v_add_u32_dpp v118, v118, v118 row_bcast:15 row_mask:0xa bank_mask:0xf\n"
            "s_nop 1\n"
            "v_add_u32_dpp v118, v118, v118 row_bcast:31 row_mask:0xc bank_mask:0xf\n"
            "s_nop 1\n"
            "v_lshrrev_b32 v116, 7, v116\n"
            "s_mov_b32 exec_lo, 0\n s_mov_b32 exec_hi, 0x80000000\n"
            "ds_write2_b32 v116, v118, v117 offset1:1\n"
            "s_mov_b64 exec, -1\n"
            "s_branch Lend2%=\n"
            "Lend%=:\n"
            "s_waitcnt vmcnt(0)\n"
            "Lend2%=:\n"
            "s_setprio 0\n"
            "s_waitcnt lgkmcnt(0)\n"

            : [mode] "=&s"(mode), [bad] "=&s"(bad), [exs] "=&v"(exs), [tid16] "+v"(t16), "+v"(tc2), "=&{v[40:43]}"(mm[0]), "=&{v[44:47]}"(mm[1]), "=&{v[48:51]}"(dd)
            : [rs] "s"(rs), [rm] "s"(rm), [rp0] "s"(rp0), [rt] "s"(rt), [i4] "s"(LMAX * 4), [c4] "s"(a_.c4), [pb] "s"(81920 + wave * 5120), [wb] "s"(wave * 1024), [q] "s"(q), [j0] "s"(J0),
              [rmm] "s"(rmm), [rd] "s"(rd), [e1] "s"(128 * 32 * 128 * 2), [ed] "v"((((wave * 8 + q) * 32 + b) * 128 + lane * 2) * 2),
              [fof] "s"((NT * NWS * 4 + cp * (P1_GRID * 2)) * 4), [dumu] "s"(ly.dumu), [lmul] "n"(LMUL), [lmask] "n"(LMASK),
              [tof] "v"((w * NT + t0) * 16), [fb] "v"(pairidx(w) * (ly.F0 * 256)), [xb] "v"(logbase(b >> 1, w))
            : "memory", "vcc", "scc", "m0", "s66", "s67", "s70", "s71", "s72", "s73", "s74", "s75", "s76", "s77", "s79", "s80", "s81", "s82",
              "s84", "s85", "s86", "s87", "s88", "s89", "s90", "s91", "s92", "s93", "s94", "s96", "s97",
              "v8", "v9", "v10", "v11", "v12", "v13", "v14", "v15", "v16", "v17", "v18", "v19", "v20", "v21", "v22", "v23", "v24", "v25", "v26", "v27", "v28", "v29", "v30", "v31", "v32", "v33", "v34", "v35", "v36", "v37", "v38", "v39", "v52", "v53", "v54", "v55", "v56", "v57", "v58", "v59", "v60", "v61", "v62", "v63", "v64", "v65", "v66", "v67", "v68", "v69", "v70", "v71", "v72", "v73", "v74", "v75", "v76", "v77", "v78", "v79", "v80", "v81", "v82", "v83", "v84", "v85", "v86", "v87", "v88", "v89", "v90", "v91", "v92", "v93", "v94", "v95", "v96", "v97", "v98", "v99", "v100", "v101", "v102", "v103", "v104", "v105", "v106", "v107", "v108", "v109", "v110", "v111", "v112", "v113", "v114", "v115", "v116", "v117", "v118");
    }
    const int tid2 = (int)(t16 >> 4);
    kaptr ka = (kaptr)__builtin_amdgcn_kernarg_segment_ptr();
    asm volatile("" : "+s"(ka));
    unsigned long long* wm = (unsigned long long*)(ka->wgmode + b * 32);
    const int E = ka->E;
    const unsigned nblk = ka->O >> 7;
    const int ln = tid2 & 63, wv = __builtin_amdgcn_readfirstlane(tid2 >> 6);
    const bool two = (16 + wv) * 8 + q < nblk;
    float g[2][2][4], h[2][2][4];
    auto gh = [&]() {
#pragma unroll
        for (int p = 0; p < 2; p++) {
            if (p == 0 || two) {
                edge_gh(lo_bf(p ? dd.z : dd.x), ka->centers, ka->w_sbf, ka->w_rbf, g[p][0], h[p][0]);
                edge_gh(lo_bf(p ? dd.w : dd.y), ka->centers, ka->w_sbf, ka->w_rbf, g[p][1], h[p][1]);
            }
        }
    };
    v2u own[2] = {{0, 0}, {0, 0}};
    if (mode == 0) {
        gh();
        own[0] = *(const v2u*)(lds + OBASE + (wv * 8 + q) * 128 + ln * 2);
        if (two) own[1] = *(const v2u*)(lds + OBASE + ((16 + wv) * 8 + q) * 128 + ln * 2);
        asm volatile("s_waitcnt vmcnt(0)" : "+v"(g[0][0][0]), "+v"(h[0][1][3]) :: "memory");
        __syncthreads();
        const v2u cs = *(const v2u*)(lds + (ln & 15) * 2);
        if (__builtin_amdgcn_readlane(row_scan_add(cs.x), 15) != 0 || __ballot(cs.y != 0)) mode = 1;
    }
    if (mode != 0) {
        acc_fallback(lds, mode, *ka, tid2);
        asm volatile("s_waitcnt vmcnt(0)" ::: "memory");
        __syncthreads();
        if (tid2 == P2_THREADS - 1) { __builtin_amdgcn_fence(__ATOMIC_RELEASE, "agent"); asm volatile("s_waitcnt vmcnt(0)" ::: "memory"); }
        const __amdgpu_buffer_rsrc_t rmm = __builtin_amdgcn_make_buffer_rsrc((void*)ka->m, (short)0, E * 8, 0x00020000);
        const __amdgpu_buffer_rsrc_t rdd = __builtin_amdgcn_make_buffer_rsrc((void*)ka->dist, (short)0, E * 2, 0x00020000);
        const unsigned e = ((wv * 8 + q) * 32 + b) * 128 + ln * 2, e1 = e + 128 * 32 * 128;
        mm[0] = __builtin_amdgcn_raw_buffer_load_b128(rmm, e * 8, 0, 0); mm[1] = __builtin_amdgcn_raw_buffer_load_b128(rmm, e1 * 8, 0, 0);
        dd.x = __builtin_amdgcn_raw_buffer_load_b16(rdd, e * 2, 0, 0); dd.y = __builtin_amdgcn_raw_buffer_load_b16(rdd, e * 2 + 2, 0, 0);
        dd.z = __builtin_amdgcn_raw_buffer_load_b16(rdd, e1 * 2, 0, 0); dd.w = __builtin_amdgcn_raw_buffer_load_b16(rdd, e1 * 2 + 2, 0, 0);
        gh();
        own[0].x = own[0].y = own[1].x = own[1].y = 0;
    }
    const unsigned* prb = ka->praw + (size_t)b * ka->O; unsigned short* outp = ka->out;
    int EP = ka->EP;
    unsigned wu[8];
#pragma unroll
    for (int i = 0; i < 8; i++) wu[i] = ((cptr)ka->w_up)[i];
    if (tid2 == P2_THREADS - 1) {
        const unsigned long long add = (1u | (mode ? 256u << q : 0u)) | (qt == 0 ? (unsigned long long)(__builtin_amdgcn_readfirstlane(exs) & 0xFFFF) << (32 + 16 * cp) : 0ull);
        unsigned long long v = __hip_atomic_fetch_add(wm, add, __ATOMIC_RELAXED, __HIP_MEMORY_SCOPE_AGENT) + add;
        for (int spin = 0; spin < (1 << 22) && (v & 255) != 8; spin++) {
            v = __hip_atomic_load(wm, __ATOMIC_RELAXED, __HIP_MEMORY_SCOPE_AGENT);
            if ((v & 255) != 8) __builtin_amdgcn_s_sleep(2);
        }
        if (v & 0xFF00) { __builtin_amdgcn_fence(__ATOMIC_ACQUIRE, "agent"); asm volatile("s_waitcnt vmcnt(0)" ::: "memory"); }
        lds[64] = ((unsigned)v >> 8) & 255; lds[65] = (unsigned)(v >> 32);
    }
    asm volatile("" : "+s"(prb), "+s"(outp), "+s"(EP), "+s"(wu[0]), "+s"(wu[1]), "+s"(wu[2]), "+s"(wu[3]), "+s"(wu[4]), "+s"(wu[5]), "+s"(wu[6]), "+s"(wu[7]));
    const __amdgpu_buffer_rsrc_t rpr = __builtin_amdgcn_make_buffer_rsrc((void*)prb, (short)0, (7 * EP + (int)(nblk << 7)) * 4, 0x00020000);
    const __amdgpu_buffer_rsrc_t rou = __builtin_amdgcn_make_buffer_rsrc((void*)outp, (short)0, E * 8, 0x00020000);
    __syncthreads();
    v2u w[2][8];
#pragma unroll
    for (int p = 0; p < 2; p++) {
        if (p == 0 || two) {
#pragma unroll
            for (int j = 0; j < 8; j++)
                w[p][j] = __builtin_amdgcn_raw_buffer_load_b64(rpr, (((p * 16 + wv) * 8 + q) * 128 + ln * 2) * 4, j == q ? (int)OOB : j * EP * 4, PR_AUX);
        }
    }
    const unsigned fbm = __builtin_amdgcn_readfirstlane(lds[64]), exw = __builtin_amdgcn_readfirstlane(lds[65]);
    float inv[4];
#pragma unroll
    for (int k = 0; k < 4; k++) inv[k] = __uint_as_float(((exw >> (8 * k)) & 255) << 23) * (1.0f / 511.0f);
#pragma unroll
    for (int p = 0; p < 2; p++) {
        const unsigned blk = (p * 16 + wv) * 8 + q;
        if (p == 0 || two) {
            int s[2][4];
            {
                const int l0 = (int)(short)(own[p].x & 0xFFFF), h0 = (int)(own[p].x + 0x8000u) >> 16, l1 = (int)(short)(own[p].y & 0xFFFF), h1 = (int)(own[p].y + 0x8000u) >> 16;
                s[0][0] = cp ? 0 : l0; s[0][1] = cp ? 0 : h0; s[0][2] = cp ? l0 : 0; s[0][3] = cp ? h0 : 0;
                s[1][0] = cp ? 0 : l1; s[1][1] = cp ? 0 : h1; s[1][2] = cp ? l1 : 0; s[1][3] = cp ? h1 : 0;
            }
#pragma unroll
            for (int j = 0; j < 8; j++) {
                s[0][(j & 1) * 2] += (int)(short)(w[p][j].x & 0xFFFF); s[0][(j & 1) * 2 + 1] += (int)(w[p][j].x + 0x8000u) >> 16;
                s[1][(j & 1) * 2] += (int)(short)(w[p][j].y & 0xFFFF); s[1][(j & 1) * 2 + 1] += (int)(w[p][j].y + 0x8000u) >> 16;
            }
            float a[2][4];
#pragma unroll
            for (int i = 0; i < 2; i++)
#pragma unroll
                for (int k = 0; k < 4; k++) a[i][k] = s[i][k] ? (float)s[i][k] * inv[k] : 0.f;
            if (fbm && blk < nblk) {
                const float* part = ka->part + (size_t)b * ka->O + blk * 128 + ln * 2;
#pragma unroll 1
                for (int j = 0; j < 8; j++)
                    if ((fbm >> j) & 1) {
#pragma unroll
                        for (int i = 0; i < 2; i++) {
                            const float x = part[(size_t)(j * 2) * EP + i], y = part[(size_t)(j * 2 + 1) * EP + i];
                            if (j & 1) { a[i][2] += x; a[i][3] += y; } else { a[i][0] += x; a[i][1] += y; }
                        }
                    }
            }
            const v2u r0 = edge_out(a[0], g[p][0], h[p][0], mm[p].x, mm[p].y, wu), r1 = edge_out(a[1], g[p][1], h[p][1], mm[p].z, mm[p].w, wu);
            const v4u r = {r0.x, r0.y, r1.x, r1.y};
            __builtin_amdgcn_raw_buffer_store_b128(r, rou, ((blk * 32 + b) * 128 + ln * 2) * 8, 0, OUT_AUX);
        }
    }
}

extern "C" __global__ __launch_bounds__(256) void k_naive(const int* __restrict__ idx_in, const int* __restrict__ idx_out,
                                               const unsigned short* __restrict__ angle, const unsigned short* __restrict__ ang_freq,
                                               const unsigned short* __restrict__ m, const unsigned short* __restrict__ w_down,
                                               float* __restrict__ part, int T, int EP) {
    const float freq = bf2f(ang_freq[0]);
    for (long long t = (long long)blockIdx.x * 256 + threadIdx.x; t < T; t += (long long)gridDim.x * 256) {
        v2u mv = ((const v2u*)m)[idx_in[t]];
        float a[4] = {lo_bf(mv.x), hi_bf(mv.x), lo_bf(mv.y), hi_bf(mv.y)};
        float c = __cosf(freq * bf2f(angle[t]));
        int eo = idx_out[t];
#pragma unroll
        for (int d = 0; d < 4; d++) {
            float s = 0.f;
#pragma unroll
            for (int k = 0; k < 4; k++) s += a[k] * bf2f(w_down[k * 4 + d]);
            atomicAdd(&part[(size_t)d * EP + eo], s * c);
        }
    }
}

__global__ __launch_bounds__(256) void k_final(const unsigned short* __restrict__ m, const unsigned short* __restrict__ dist,
                                               const unsigned short* __restrict__ centers, const unsigned short* __restrict__ w_rbf,
                                               const unsigned short* __restrict__ w_sbf, const unsigned short* __restrict__ w_up,
                                               const float* __restrict__ part, unsigned short* __restrict__ out, int E, int EP) {
    const int e = blockIdx.x * 256 + threadIdx.x;
    if (e >= E) return;
    float a[4], g[4], h[4];
#pragma unroll
    for (int k = 0; k < 4; k++) a[k] = part[(size_t)k * EP + e];
    edge_gh(bf2f(dist[e]), centers, w_sbf, w_rbf, g, h);
    const v2u mv = ((const v2u*)m)[e];
    unsigned wu[8];
#pragma unroll
    for (int i = 0; i < 8; i++) wu[i] = ((cptr)w_up)[i];
    ((v2u*)out)[e] = edge_out(a, g, h, mv.x, mv.y, wu);
}

extern "C" void kernel_launch(void* const* d_in, const int* in_sizes, int n_in,
                              void* d_out, int out_size, void* d_ws, size_t ws_size,
                              hipStream_t stream) {
    const unsigned short* m = (const unsigned short*)d_in[0];
    const int* idx_in = (const int*)d_in[1];
    const int* idx_out = (const int*)d_in[2];
    const unsigned short* dist = (const unsigned short*)d_in[3];
    const unsigned short* angle = (const unsigned short*)d_in[4];
    const unsigned short* centers = (const unsigned short*)d_in[5];
    const unsigned short* ang_freq = (const unsigned short*)d_in[6];
    const unsigned short* w_rbf = (const unsigned short*)d_in[7];
    const unsigned short* w_sbf = (const unsigned short*)d_in[8];
    const unsigned short* w_down = (const unsigned short*)d_in[9];
    const unsigned short* w_up = (const unsigned short*)d_in[10];
    const int E = in_sizes[0] / 4;
    const int T = in_sizes[1];
    const int NR = T / (P1_GRID * CH);
    const int per = ((T - NR * (P1_GRID * CH) + P1_GRID - 1) / P1_GRID + 3) / 4 * 4;
    const unsigned O = (unsigned)(((E + 127) / 128 + 31) / 32) * 128;
    const unsigned I = (unsigned)(E + 31) / 32;
    const int EP = (int)O * 32;
    Lay ly;
    const unsigned meanr = (unsigned)(((size_t)NR * CH + per) / NT);
    ly.F0 = meanr >= 100 ? 40 : 72;
    if (LA > 3) ly.F0 = (ly.F0 + (1 << LA) - 1) >> LA << LA;
    ly.fcap = ly.F0 / 4 * 505;
    ly.cap = 768;
    char* ws = (char*)d_ws;
    char* rec = ws;
    size_t o = 0x200000;
    ly.dumu = ly.cap * 32768;
    o += ((size_t)ly.dumu + 64) * 16;
    unsigned* praw = (unsigned*)(ws + o); o += (size_t)8 * EP * 4; o = (o + 255) / 256 * 256;
    unsigned* wgmode = (unsigned*)(ws + o); o += 8192;
    unsigned* mdp = (unsigned*)(ws + o); o += (size_t)2 * 32 * LMAX * 4;
    unsigned* tab = (unsigned*)(ws + o); o += (size_t)NT * NWS * 16;
    unsigned* flags = (unsigned*)(ws + o); o += FL_SIZE * 4; o = (o + 255) / 256 * 256;
    float* part = (float*)(ws + o); o += (size_t)16 * EP * 4;
    const bool ok = E >= 16384 && O <= LMAX && I <= LMAX && T > 0 && meanr <= 180 && o <= ws_size;
    if (!ok) {
        part = (float*)ws;
        const int EQ = (E + 3) / 4 * 4;
        (void)hipMemsetAsync(part, 0, (size_t)4 * EQ * 4, stream);
        if (T > 0) k_naive<<<2048, 256, 0, stream>>>(idx_in, idx_out, angle, ang_freq, m, w_down, part, T, EQ);
        k_final<<<(E + 255) / 256, 256, 0, stream>>>(m, dist, centers, w_rbf, w_sbf, w_up, part, (unsigned short*)d_out, E, EQ);
        return;
    }
    k_bin<<<P1_GRID, P1_THREADS, 0, stream>>>(idx_in, idx_out, angle, ang_freq, m, w_down, mdp, E, T, rec, tab, flags, NR, per, ly, wgmode);
    AccArgs aa;
    aa.rec = rec; aa.tab = tab; aa.flags = flags; aa.mdp = mdp; aa.part = part; aa.praw = praw; aa.wgmode = wgmode;
    aa.idx_in = idx_in; aa.idx_out = idx_out; aa.angle = angle; aa.ang_freq = ang_freq; aa.m = m; aa.w_down = w_down;
    aa.EP = EP; aa.O = O; aa.T = T; aa.c4 = (float)((double)E / 144.0);
    aa.dist = dist; aa.centers = centers; aa.w_rbf = w_rbf; aa.w_sbf = w_sbf; aa.w_up = w_up; aa.out = (unsigned short*)d_out; aa.E = E; aa.unused = 0;
    aa.ly = ly;
    k_acc<<<256, P2_THREADS, 0, stream>>>(aa);
}
```

```cpp
#include <hip/hip_runtime.h>
#include <hip/hip_bf16.h>
#include <stdint.h>

typedef int v4i __attribute__((ext_vector_type(4)));
typedef unsigned v4u __attribute__((ext_vector_type(4)));
typedef unsigned v2u __attribute__((ext_vector_type(2)));
typedef int v4i_rs __attribute__((ext_vector_type(4)));
typedef __attribute__((address_space(3))) unsigned lds_u32;
typedef __attribute__((address_space(3))) v4u lds_v4u;

static __device__ __forceinline__ float bf2f(unsigned short v) { return __uint_as_float(((unsigned)v) << 16); }
static __device__ __forceinline__ unsigned f2bf_bits(float f) {
    unsigned u = __float_as_uint(f);
    u += 0x7FFF + ((u >> 16) & 1);
    return u >> 16;
}
static __device__ __forceinline__ float lo_bf(unsigned u) { return __uint_as_float(u << 16); }
static __device__ __forceinline__ float hi_bf(unsigned u) { return __uint_as_float(u & 0xFFFF0000u); }
static __device__ __forceinline__ v4i_rs mk_rsrc(const void* p, unsigned bytes) {
    unsigned long long a = (unsigned long long)p;
    v4i_rs r = {(int)(unsigned)a, (int)(unsigned)((a >> 32) & 0xFFFF), (int)bytes, 0x00020000};
    return r;
}
static __device__ __forceinline__ lds_u32* ldsp(unsigned byte) { return (lds_u32*)(size_t)byte; }

#define P1_THREADS 1024
#define PD 1
#define UNR ((PD & 1) ? 2 * PD : PD)
#define P1_GRID 256
#define NWS 256
#define CH (P1_THREADS * 4)
#define NT 1024
#define LMAX 20480
#define P2_THREADS 1024
#define OOB 0x80000000u
#define OBASE 20480
#define IDXB 4096
#define ST_AUX 16
#define LB 4096
#define LS 132
#define TINC 683
#define WL_OFF (LB + NT * LS)
#define MS_OFF (WL_OFF + 16 * 512)
#define BIN_LDS (MS_OFF + 128 + 320)
#define NSTAT 14
#define WM_CTR 1024
#define WM_BOX (WM_CTR + 256)
#define RM 1
#define RXOR 0
#define CALN 3
#define VTHR 5
#define CORD 0
#define PSTR 256
#define NTH "nt"
#define OUT_AUX 16
#define PR_AUX 18
#define TCH 7
#define CZERO ((512u << 1) | (512u << 11) | (512u << 22))
#define FL_SIZE (2 * P1_GRID * 2)

struct Grp { v4i io; v4i ii; v2u an; };
static __device__ __forceinline__ void load_grp(Grp& r, v4i_rs ro, v4i_rs ri, v4i_rs ra, unsigned c, unsigned vo16, unsigned vo8, int parts = 7) {
    if (parts & 2) asm volatile("buffer_load_dwordx4 %0, %1, %2, %3 offen nt" : "=v"(r.io) : "v"(vo16), "s"(ro), "s"(c * 256));
    if (parts & 4) asm volatile("buffer_load_dwordx4 %0, %1, %2, %3 offen nt" : "=v"(r.ii) : "v"(vo16), "s"(ri), "s"(c * 256));
    if (parts & 1) asm volatile("buffer_load_dwordx2 %0, %1, %2, %3 offen nt" : "=v"(r.an) : "v"(vo8), "s"(ra), "s"(c * 128));
}
#define WAIT_GRP(n, g) asm volatile("s_waitcnt vmcnt(%3)" : "+v"((g).io), "+v"((g).ii), "+v"((g).an) : "n"(n))

static __device__ __forceinline__ unsigned wave_scan_max(unsigned x) {
    x = max(x, (unsigned)__builtin_amdgcn_update_dpp(0, (int)x, 0x111, 0xF, 0xF, false));
    x = max(x, (unsigned)__builtin_amdgcn_update_dpp(0, (int)x, 0x112, 0xF, 0xF, false));
    x = max(x, (unsigned)__builtin_amdgcn_update_dpp(0, (int)x, 0x114, 0xF, 0xF, false));
    x = max(x, (unsigned)__builtin_amdgcn_update_dpp(0, (int)x, 0x118, 0xF, 0xF, false));
    x = max(x, (unsigned)__builtin_amdgcn_update_dpp(0, (int)x, 0x142, 0xA, 0xF, false));
    x = max(x, (unsigned)__builtin_amdgcn_update_dpp(0, (int)x, 0x143, 0xC, 0xF, false));
    return x;
}
static __device__ __forceinline__ unsigned row_scan_add(unsigned x) {
    x += (unsigned)__builtin_amdgcn_update_dpp(0, (int)x, 0x111, 0xF, 0xF, false);
    x += (unsigned)__builtin_amdgcn_update_dpp(0, (int)x, 0x112, 0xF, 0xF, false);
    x += (unsigned)__builtin_amdgcn_update_dpp(0, (int)x, 0x114, 0xF, 0xF, false);
    x += (unsigned)__builtin_amdgcn_update_dpp(0, (int)x, 0x118, 0xF, 0xF, false);
    return x;
}
static __device__ __forceinline__ unsigned wave_scan_add(unsigned x) {
    x = row_scan_add(x);
    x += (unsigned)__builtin_amdgcn_update_dpp(0, (int)x, 0x142, 0xA, 0xF, false);
    x += (unsigned)__builtin_amdgcn_update_dpp(0, (int)x, 0x143, 0xC, 0xF, false);
    return x;
}

static __device__ __forceinline__ float wave_scan_addf(float x) {
    x += __int_as_float(__builtin_amdgcn_update_dpp(0, __float_as_int(x), 0x111, 0xF, 0xF, false));
    x += __int_as_float(__builtin_amdgcn_update_dpp(0, __float_as_int(x), 0x112, 0xF, 0xF, false));
    x += __int_as_float(__builtin_amdgcn_update_dpp(0, __float_as_int(x), 0x114, 0xF, 0xF, false));
    x += __int_as_float(__builtin_amdgcn_update_dpp(0, __float_as_int(x), 0x118, 0xF, 0xF, false));
    x += __int_as_float(__builtin_amdgcn_update_dpp(0, __float_as_int(x), 0x142, 0xA, 0xF, false));
    x += __int_as_float(__builtin_amdgcn_update_dpp(0, __float_as_int(x), 0x143, 0xC, 0xF, false));
    return x;
}

struct Lay { unsigned F0, cap, dumu, fcap; };
#define P_A 0
#define P_B 0
#define P_XO 4
#define P_DF (V & 3)
#define P_EA ((V >> 2) & 7)
#define P_EP ((V >> 5) & 3)
#define N_EA ((P_EA & 1) + ((P_EA >> 1) & 1) + (P_EA >> 2))
#define VDEF 8
#define P_LP 6
#define P_LS 15
#define P_JM 1
#define SHP 2
#define LA 3
#define XORD 0
#define NPOS 10
#define LMUL (8 + 32768)
#define LMASK ((((1 << LA) - 1) << 3) | ((1023 >> LA) << (15 + LA)))
static __device__ __forceinline__ char* rec_align(const char* p) { return (char*)(((size_t)p + 0x1FFFFF) & ~(size_t)0x1FFFFF); }
static __device__ __forceinline__ unsigned pairidx(unsigned w) { return (w >> 3) * 4 + (w & 3); }
static __device__ __forceinline__ unsigned logbase(unsigned wave, unsigned w, unsigned xo) {
    const unsigned p = xo & 16 ? (w & 3) * 32 + (w >> 3) : pairidx(w), k = xo & 15;
    const unsigned x = (p & ((1 << k) - 1)) | (wave << k) | ((p >> k) << (k + 4));
    return ((x & ((64 >> LA) - 1)) << (3 + LA)) | ((x >> (6 - LA)) << 10);
}

static __device__ __forceinline__ void conv_rec(unsigned io, unsigned ii, float ang, float frev, unsigned& A, unsigned& t4, unsigned& cq, unsigned& seen) {
    unsigned x, y;
    asm("v_and_b32 %0, 31, %4\n v_and_b32 %1, 0xf80, %3\n v_lshl_or_b32 %2, %0, 2, %1" : "=&v"(x), "=&v"(y), "=v"(t4) : "v"(io), "v"(ii));
    asm("v_lshrrev_b32 %0, 5, %3\n v_lshrrev_b32 %1, 3, %4\n v_bfi_b32 %0, %5, %3, %0\n v_add_u32 %1, %6, %1\n v_lshl_add_u32 %2, %0, 17, %1"
        : "=&v"(x), "=&v"(y), "=v"(A) : "v"(io), "v"(ii), "s"(127u), "n"(IDXB << 17));
    float c = __builtin_amdgcn_cosf(__builtin_amdgcn_fractf(frev * ang));
    const unsigned cb = __float_as_uint(__builtin_fmaf(c, 511.0f, 12583424.0f));
    seen |= cb;
    cq = cb & 0x3FFu;
}
#define PADV 1
static __device__ __forceinline__ unsigned pad_rec(unsigned q) { return PADV == 0 ? 0x7FFFu << 17 : PADV == 1 ? (0x7FFFu - (q & 511)) << 17 : (IDXB + (q & 511)) << 17; }

static __device__ __forceinline__ void prep_md(const v2u* mv, unsigned dst, const unsigned short* w_down, unsigned* mdp, unsigned plane, unsigned* mx, float* sq) {
    float wd[16];
#pragma unroll
    for (int i = 0; i < 8; i++) { unsigned u = ((const __attribute__((address_space(4))) unsigned*)w_down)[i]; wd[2 * i] = lo_bf(u); wd[2 * i + 1] = hi_bf(u); }
    unsigned o[2][4];
#pragma unroll
    for (int j = 0; j < 4; j++) {
        const float a[4] = {lo_bf(mv[j].x), hi_bf(mv[j].x), lo_bf(mv[j].y), hi_bf(mv[j].y)};
        float s[4];
#pragma unroll
        for (int d = 0; d < 4; d++) {
            s[d] = 0.f;
#pragma unroll
            for (int k = 0; k < 4; k++) s[d] += a[k] * wd[k * 4 + d];
            sq[d] += s[d] * s[d];
            mx[d] = max(mx[d], __float_as_uint(s[d]) & 0x7FFFFFFFu);
        }
#pragma unroll
        for (int p = 0; p < 2; p++) {
            const unsigned lo = f2bf_bits(s[2 * p]) & 0xFFFF;
            const unsigned sb = __float_as_uint(s[2 * p + 1]);
            const unsigned h = (unsigned)max((int)((sb & 0x7FFFFFFFu) - lo + 0x8000u), 0) & 0x7FFF0000u;
            o[p][j] = lo | h | (sb & 0x80000000u);
        }
    }
#pragma unroll
    for (int p = 0; p < 2; p++) {
        v4u v = {o[p][0], o[p][1], o[p][2], o[p][3]};
        *(v4u*)(mdp + (size_t)p * plane + dst) = v;
    }
}

static __device__ __forceinline__ unsigned touch_code(int tid) {
    unsigned alo, ahi, blo, r = 0;
    asm volatile("s_getpc_b64 s[96:97]\n s_add_u32 %0, s96, k_bin@rel32@lo+4\n s_addc_u32 %1, s97, k_bin@rel32@hi+12\n"
                 "s_getpc_b64 s[96:97]\n s_add_u32 %2, s96, k_naive@rel32@lo+4\n" : "=&s"(alo), "=&s"(ahi), "=&s"(blo) :: "scc", "s96", "s97");
    const unsigned size = blo - alo < 65536u ? blo - alo : 0u;
    const v4i_rs rc = {(int)alo, (int)(ahi & 0xFFFF), (int)size, 0x00020000};
    if (tid < 512) asm volatile("buffer_load_dword %0, %1, %2, 0 offen" : "=v"(r) : "v"(tid * 128), "s"(rc));
    return r;
}

struct Held { unsigned a[4], t[4], A[4], cd[4]; unsigned long long m[4]; };
struct Conv { unsigned A[4], t4[4], cq[4]; };
#define HB(k) "s_mov_b64 exec, %[m" #k "]\n ds_write_b32 %[a" #k "], %[A" #k "]\n ds_or_b32 %[t" #k "], %[c" #k "]\n"
static __device__ __forceinline__ void held_out(const Held& H) {
    asm volatile(HB(0) HB(1) HB(2) HB(3) "s_mov_b64 exec, -1\n"
                 :: [m0] "s"(H.m[0]), [m1] "s"(H.m[1]), [m2] "s"(H.m[2]), [m3] "s"(H.m[3]), [a0] "v"(H.a[0]), [a1] "v"(H.a[1]), [a2] "v"(H.a[2]), [a3] "v"(H.a[3]),
                   [t0] "v"(H.t[0]), [t1] "v"(H.t[1]), [t2] "v"(H.t[2]), [t3] "v"(H.t[3]),
                   [A0] "v"(H.A[0]), [A1] "v"(H.A[1]), [A2] "v"(H.A[2]), [A3] "v"(H.A[3]), [c0] "v"(H.cd[0]), [c1] "v"(H.cd[1]), [c2] "v"(H.cd[2]), [c3] "v"(H.cd[3])
                 : "memory");
}
static __device__ __forceinline__ void tickets(unsigned* v, const Conv& c, unsigned inc) {
    asm volatile("ds_add_rtn_u32 %0, %4, %8\n ds_add_rtn_u32 %1, %5, %8\n ds_add_rtn_u32 %2, %6, %8\n ds_add_rtn_u32 %3, %7, %8\n"
                 : "=&v"(v[0]), "=&v"(v[1]), "=&v"(v[2]), "=&v"(v[3]) : "v"(c.t4[0]), "v"(c.t4[1]), "v"(c.t4[2]), "v"(c.t4[3]), "v"(inc) : "memory");
}
static __device__ __forceinline__ void tickets_masked(unsigned* v, const Conv& c, unsigned inc, const unsigned long long* vm) {
#pragma unroll
    for (int k = 0; k < 4; k++) {
        v[k] = 0;
        asm volatile("s_mov_b64 exec, %3\n ds_add_rtn_u32 %0, %1, %2\n s_mov_b64 exec, -1" : "+v"(v[k]) : "v"(c.t4[k]), "v"(inc), "s"(vm[k]) : "memory");
    }
}
static __device__ __forceinline__ void place(Held& H, unsigned* v, Conv& c, const unsigned long long* vm, unsigned lb) {
    unsigned bt[4];
#pragma unroll
    for (int k = 0; k < 4; k++) asm("v_mad_u32_u24 %0, %1, %2, %3" : "=v"(bt[k]) : "v"(c.t4[k]), "n"(LS / 4), "v"(lb));
    asm volatile("s_waitcnt lgkmcnt(0)" : "+v"(v[0]), "+v"(v[1]), "+v"(v[2]), "+v"(v[3]));
#pragma unroll
    for (int k = 0; k < 4; k++) {
        H.m[k] = __ballot(v[k] >= 16384u);
        unsigned sh;
        asm("v_lshrrev_b32 %0, 7, %3\n v_lshrrev_b32 %2, 6, %3\n v_and_b32 %0, 0x7c, %0\n v_or_b32 %1, 12, %0\n v_add_u32 %0, %0, %4\n v_add_u32 %1, %1, %4"
            : "=&v"(H.a[k]), "=&v"(H.t[k]), "=&v"(sh) : "v"(v[k]), "v"(bt[k]));
        H.A[k] = c.A[k];
        asm("v_lshlrev_b32 %0, %1, %2" : "=v"(H.cd[k]) : "v"(sh), "v"(c.cq[k]));
        asm volatile("s_andn2_b64 exec, %[w], %[m]\n ds_write_b32 %[a], %[A]\n ds_or_b32 %[t], %[c]\n s_mov_b64 exec, -1\n"
                     :: [w] "s"(vm[k]), [m] "s"(H.m[k]), [a] "v"(H.a[k]), [A] "v"(H.A[k]), [t] "v"(H.t[k]), [c] "v"(H.cd[k]) : "memory", "scc");
    }
}
struct Own { unsigned la, p[4], nl, ovf; };
static __device__ __forceinline__ v4u read_unit(unsigned la) {
    const lds_u32* p = ldsp(la);
    v4u v = {p[0], p[1], p[2], p[3]};
    return v;
}
typedef __attribute__((address_space(3))) v2u lds_v2u;
static __device__ __forceinline__ unsigned long long low_lanes(unsigned groups) { return groups >= 8 ? ~0ull : (1ull << (groups * 8)) - 1; }
static __device__ __forceinline__ unsigned flush_addr(unsigned e, unsigned f, unsigned long long act, unsigned base) {
    unsigned vo = (__umul24(f, LMUL * 16) & (LMASK * 16u)) | base;
    asm volatile("s_mov_b64 exec, %1\n ds_write_b32 %2, %3 offset:12\n s_mov_b64 exec, -1\n v_cndmask_b32 %0, %4, %0, %1"
                 : "+v"(vo) : "s"(act), "v"(e), "v"(0u), "v"(OOB) : "memory");
    return vo;
}
struct Pend { v4u v0, v1; unsigned a0, a1; };
static __device__ __forceinline__ void pend_out(const Pend& p, __amdgpu_buffer_rsrc_t rr) {
    __builtin_amdgcn_raw_buffer_store_b128(p.v0, rr, p.a0, 0, ST_AUX);
    __builtin_amdgcn_raw_buffer_store_b128(p.v1, rr, p.a1, 0, ST_AUX);
}
static __device__ __forceinline__ void flush(Own& o, Pend& pd, unsigned& wc, unsigned cnt, int tid, unsigned base, __amdgpu_buffer_rsrc_t rr, bool now) {
    const bool full = cnt >= 16384u;
    const unsigned long long bal = __ballot(full);
    const unsigned nfull = __popcll(bal);
    const unsigned wl = WL_OFF + (tid >> 6) * 512;
    if (full) {
        const unsigned pos = __builtin_amdgcn_mbcnt_hi((unsigned)(bal >> 32), __builtin_amdgcn_mbcnt_lo((unsigned)bal, 0));
        *ldsp(wl + pos * 4) = o.la;
        cnt -= 24 * TINC;
        *ldsp(tid * 4) = cnt;
        o.ovf |= cnt;
        o.nl++;
        o.p[3] = __builtin_amdgcn_alignbit(o.p[3], o.p[2], 22); o.p[2] = __builtin_amdgcn_alignbit(o.p[2], o.p[1], 22); o.p[1] = __builtin_amdgcn_alignbit(o.p[1], o.p[0], 22);
        o.p[0] = (o.p[0] << 10) + pos + wc;
    }
    const unsigned g = (tid >> 3) & 7, k16 = (tid & 7) * 16;
    const unsigned e0 = *ldsp(wl + g * 4) + k16, e1 = *ldsp(wl + g * 4 + 32) + k16;
    pd.v0 = read_unit(e0); pd.v1 = read_unit(e1);
    asm volatile("" ::: "memory");
    pd.a0 = flush_addr(e0, g + wc, low_lanes(nfull), base);
    pd.a1 = flush_addr(e1, g + wc + 8, nfull > 8 ? low_lanes(nfull - 8) : 0ull, base);
    if (now) pend_out(pd, rr);
    for (unsigned p = 16; p < nfull; p += 8) {
        const unsigned e = *ldsp(wl + (p + g) * 4) + k16;
        const v4u v = read_unit(e);
        asm volatile("" ::: "memory");
        __builtin_amdgcn_raw_buffer_store_b128(v, rr, flush_addr(e, g + wc + p, low_lanes(nfull - p), base), 0, ST_AUX);
    }
    wc = min(wc + nfull, 960u);
    asm volatile("s_waitcnt lgkmcnt(0)" : "+v"(pd.v0), "+v"(pd.v1) :: "memory");
}

template <int V> static __device__ __forceinline__ void bin_body(unsigned* L, const int* __restrict__ idx_in, const int* __restrict__ idx_out,
                                                    const unsigned short* __restrict__ angle, const unsigned short* __restrict__ ang_freq,
                                                    const unsigned short* __restrict__ m, const unsigned short* __restrict__ w_down,
                                                    unsigned* __restrict__ mdp, int E, int T,
                                                    char* __restrict__ rec0, unsigned* __restrict__ tab, unsigned* __restrict__ flags,
                                                    int NR, int per, Lay ly, unsigned* __restrict__ wgmode) {
    const int tid = threadIdx.x;
    const unsigned run = blockIdx.x;
    char* rec = rec_align(rec0);
    unsigned tc0 = touch_code(tid);
    unsigned fbits;
    asm volatile("buffer_load_ushort %0, off, %1, 0" : "=v"(fbits) : "s"(mk_rsrc(ang_freq, 2)));
    float freq;
    const v4i_rs ro = mk_rsrc(idx_out, (unsigned)T * 4), ri = mk_rsrc(idx_in, (unsigned)T * 4), ra = mk_rsrc(angle, (unsigned)T * 2);
    const __amdgpu_buffer_rsrc_t rr = __builtin_amdgcn_make_buffer_rsrc((void*)rec, (short)0,
        (int)(ly.dumu * 16),
        0x00020000);
    const unsigned vo16 = ((tid >> P_LP) << P_LS) + (tid & ((1 << P_LP) - 1)) * 16, vo8 = vo16 >> 1;
    auto chunk = [&](unsigned c) {
        const unsigned nb = P_LS - 4 - P_LP, lo = 8 - P_LP;
        unsigned j = c & ((1 << nb) - 1);
        if (P_JM == 1) j = ((j & 7) << lo) | ((j >> 3) & ((1 << lo) - 1)) | (j >> (3 + lo) << (3 + lo));
        return ((c >> nb) << (nb + 6)) + (j << (P_LP - 4));
    };
#define NSH 4
    const unsigned shard = (run >> 3) & 3, member = (run & 7) | ((run >> 5) << 3);
    const bool leader = member == 0;
    const v4i_rs rw = mk_rsrc(wgmode + WM_CTR, (256 + 256) * 4);
    const int wave0 = __builtin_amdgcn_readfirstlane(tid >> 6) == 0;
    const unsigned ctr_o = shard * 128, box_o = (256 + shard * (256 / NSH) + member) * 4;
    if (wave0) asm volatile("buffer_store_dword %0, %1, %2, 0 offen sc1" :: "v"(0u), "v"(tid == 0 ? (leader ? ctr_o : box_o) : OOB), "s"(rw) : "memory");
    unsigned mail = NR < NSTAT ? 24u : 0u;
    if (leader && NR >= NSTAT) {
        const v4i_rs rp = mk_rsrc(rec, ly.dumu * 16);
        const unsigned pbase = __builtin_amdgcn_readfirstlane(logbase(tid >> 6, run, P_XO) + (((ly.F0 + (tid >> 6) * 24) * LMUL) & LMASK)) * 16;
        unsigned x = 0, xcc;
        asm volatile("buffer_atomic_add %0, %1, %2, %3 offen sc0" : "+v"(x) : "v"((tid & 63) < 2 * CALN ? (tid & 1) * 8192 + ((tid & 63) >> 1) * PSTR : OOB), "s"(rp), "s"(pbase));
        asm volatile("s_getreg_b32 %0, hwreg(HW_REG_XCC_ID)" : "=s"(xcc));
        unsigned mn[2] = {~0u, ~0u};
        unsigned long long ta, tb;
        asm volatile("s_waitcnt vmcnt(0)\n s_memtime %1" : "+v"(x), "=s"(ta));
#pragma unroll
        for (int i = 0; i < 2 * CALN; i++) {
            const int sd = (CORD ? i ^ (i >> 1) : i) & 1;
            asm volatile("v_mov_b32 %0, 0\n buffer_atomic_add %0, %5, %3, %4 offen sc0\n s_waitcnt vmcnt(0)\n s_memtime %1\n s_waitcnt lgkmcnt(0)"
                         : "+v"(x), "=s"(tb), "+s"(ta) : "s"(rp), "s"(pbase + sd * 8192 + (i >> 1) * PSTR), "v"((tid & 63) ? OOB : 0u));
            mn[sd] = min(mn[sd], (unsigned)(tb - ta));
            ta = tb;
        }
        *ldsp(WL_OFF + (tid >> 6) * 4) = (mn[1] < mn[0]) ^ ((pbase >> 21) & 1);
        __syncthreads();
        const unsigned votes = (unsigned)__ballot(*ldsp(WL_OFF + (tid & 15) * 4) != 0) & 0xFFFF, nv = __popc(votes);
        mail = 8 | (xcc & 3) | (nv >= 8 ? 4 : 0) | (nv > VTHR && nv < 16 - VTHR ? 16 : 0);
        if (wave0) asm volatile("buffer_store_dword %0, %1, %2, 0 offen sc1" :: "v"(mail), "v"((256 + shard * 64 + tid) * 4), "s"(rw) : "memory");
        __syncthreads();
    }
    v2u mv[4];
    const unsigned gp = run * 80 + (tid >> 5) * 4;
    {
        const v4i_rs rm = mk_rsrc(m, (unsigned)E * 8);
        const unsigned o = tid < 640 ? (((gp & ~255u) | ((gp >> 2) & 63)) * 32 + (tid & 31)) * 8 : OOB;
#pragma unroll
        for (int j = 0; j < 4; j++) asm volatile("buffer_load_dwordx2 %0, %1, %2, %3 offen " NTH : "=v"(mv[j]) : "v"(o), "s"(rm), "s"(j * 64 * 32 * 8));
    }
    Grp tg; bool tv[4];
    const unsigned tbase = (unsigned)NR * (P1_GRID * CH) + run * per;
    {
        const bool in = tid * 4 < per;
#pragma unroll
        for (int k = 0; k < 4; k++) tv[k] = in && tbase + tid * 4 + k < (unsigned)T;
        const unsigned o16 = in ? tid * 16 : OOB, o8 = in ? tid * 8 : OOB;
        asm volatile("buffer_load_dwordx4 %0, %1, %2, %3 offen nt" : "=v"(tg.io) : "v"(o16), "s"(ro), "s"(tbase * 4));
        asm volatile("buffer_load_dwordx4 %0, %1, %2, %3 offen nt" : "=v"(tg.ii) : "v"(o16), "s"(ri), "s"(tbase * 4));
        asm volatile("buffer_load_dwordx2 %0, %1, %2, %3 offen nt" : "=v"(tg.an) : "v"(o8), "s"(ra), "s"(tbase * 2));
    }
    const unsigned NFULL = (unsigned)NR * P1_GRID;
    Grp g0, cur[2];
    load_grp(g0, ro, ri, ra, NR > 0 ? chunk(run) : 0x3FFFFFu, vo16, vo8);
    load_grp(cur[0], ro, ri, ra, NR > 1 ? chunk(run + P1_GRID) : 0x3FFFFFu, vo16, vo8);
    Own own;
    own.la = LB + tid * LS; own.nl = 0; own.p[0] = own.p[1] = own.p[2] = own.p[3] = 0;
    unsigned wc = ly.F0;
    unsigned base = OOB;
    auto sided = [&]() {
        const unsigned lb0 = logbase(tid >> 6, run, P_XO), sw = (((run + mail) >> 2) ^ (mail & 16 ? tid >> 6 : lb0 >> 17)) & 1;
        base = ((lb0 | (sw << 9)) + (tid & 7)) * 16;
    };
    auto await = [&]() {
        for (int i = 0; i < 4096 && !mail; i++) {
            unsigned x = 0;
            asm volatile("buffer_atomic_add %0, %1, %2, 0 offen sc0\n s_waitcnt vmcnt(0)" : "+v"(x) : "v"((tid & 63) ? OOB : box_o), "s"(rw) : "memory");
            mail = __builtin_amdgcn_readfirstlane(x);
        }
        if (mail) sided(); else { mail = 1; own.ovf |= 16384u; }
    };
    if (mail) sided();
    own.ovf = (unsigned)(size_t)(lds_u32*)L << 14;
    *ldsp(tid * 4) = 64;
#pragma unroll
    for (int i = 0; i < 8; i++) *ldsp(LB + tid * LS + i * 16 + 12) = 0;
    { const v2u z = {0, 0}; *(lds_v2u*)(size_t)(WL_OFF + tid * 8) = z; }
    if (tid < 32) *ldsp(MS_OFF + tid * 4) = 0;
    if (run == 0 && tid < 64) wgmode[(tid >> 1) * 32 + (tid & 1)] = 0;
    if (run == 1 && tid < 64) {
        v4u v = {pad_rec(tid), pad_rec(tid + 64), pad_rec(tid + 128), CZERO};
        *(v4u*)(rec + ((size_t)ly.dumu + tid) * 16) = v;
    }
    Conv cv[2];
    unsigned seen = 0;
    auto convert = [&](Grp& gp, Conv& o) {
#pragma unroll
        for (int k = 0; k < 4; k++) {
            unsigned io = k == 0 ? gp.io.x : k == 1 ? gp.io.y : k == 2 ? gp.io.z : gp.io.w;
            unsigned ii = k == 0 ? gp.ii.x : k == 1 ? gp.ii.y : k == 2 ? gp.ii.z : gp.ii.w;
            unsigned aw = (k & 2) ? gp.an.y : gp.an.x;
            float ang = (k & 1) ? hi_bf(aw) : lo_bf(aw);
            conv_rec(io, ii, ang, freq, o.A[k], o.t4[k], o.cq[k], seen);
        }
#pragma unroll
        for (int k = 0; k < 4; k++) asm volatile("" : "+v"(o.A[k]), "+v"(o.t4[k]), "+v"(o.cq[k]));
    };
    const unsigned tinc = TINC, lb7 = LB;
    Held H;
    {
        asm volatile("s_waitcnt vmcnt(9)" : "+v"(mv[0]), "+v"(mv[1]), "+v"(mv[2]), "+v"(mv[3]));
        if (tid < 640) {
            unsigned mx[4] = {0, 0, 0, 0};
            float sq[4] = {0.f, 0.f, 0.f, 0.f};
            prep_md(mv, (tid & 31) * LMAX + gp, w_down, mdp, 32 * LMAX, mx, sq);
#pragma unroll
            for (int d = 0; d < 4; d++) {
                const unsigned v = wave_scan_max(mx[d]);
                const float s = wave_scan_addf(sq[d]);
                if ((tid & 63) == 63) { *ldsp(MS_OFF + 128 + (tid >> 6) * 32 + d * 4) = v; *ldsp(MS_OFF + 128 + (tid >> 6) * 32 + 16 + d * 4) = __float_as_uint(s); }
            }
        }
        asm volatile("s_waitcnt vmcnt(3)" : "+v"(tg.io), "+v"(tg.ii), "+v"(tg.an), "+v"(fbits),
                     "+v"(g0.io), "+v"(g0.ii), "+v"(g0.an));
        asm volatile("" :: "v"(tc0));
        freq = lo_bf(fbits) * 0.15915494309189535f;
        if (T & 1) {
            const unsigned rel = (unsigned)T - 1 - (tbase + tid * 4);
            if (tv[0] && rel < 4) { const unsigned av = angle[T - 1]; if (rel == 0) tg.an.x = av; else tg.an.y = av; }
        }
        __syncthreads();
        unsigned long long vm[4];
#pragma unroll
        for (int k = 0; k < 4; k++) vm[k] = __ballot(tv[k]);
        if (vm[0]) {
            unsigned v[4];
            convert(tg, cv[1]);
            tickets_masked(v, cv[1], tinc, vm);
            place(H, v, cv[1], vm, lb7);
            if ((H.m[0] & vm[0]) | (H.m[1] & vm[1]) | (H.m[2] & vm[2]) | (H.m[3] & vm[3])) own.ovf |= 16384u;
        }
        convert(g0, cv[0]);
#pragma unroll
        for (int k = 0; k < 4; k++) { H.m[k] = 0; H.a[k] = 0; H.t[k] = 0; H.A[k] = 0; H.cd[k] = 0; }
        asm volatile("s_waitcnt vmcnt(0)" ::: "memory");
        if (P_EA && !P_EP) load_grp(cur[1], ro, ri, ra, NR > 2 ? chunk(run + 2 * P1_GRID) : 0x3FFFFFu, vo16, vo8, P_EA);
    }
    const unsigned long long vm[4] = {~0ull, ~0ull, ~0ull, ~0ull};
    unsigned ca = run + P1_GRID, cb = run + 2 * P1_GRID;
    Pend pd; pd.a0 = pd.a1 = OOB; pd.v0 = pd.v1 = v4u{0, 0, 0, 0};
    unsigned got = 0;
    int st = leader ? 1 : 0;
    if (NR == 0) { asm volatile("s_waitcnt lgkmcnt(0)" ::: "memory"); __syncthreads(); }
    for (int rd = 0; NR > 0; rd += 2) {
        bool done = false;
#pragma unroll
        for (int r = 0; r < 2; r++) {
            unsigned v[4];
            if (P_EP == 2) load_grp(cur[r ^ 1], ro, ri, ra, cb < NFULL ? chunk(cb) : 0x3FFFFFu, vo16, vo8, P_EA);
            tickets(v, cv[r & 1], tinc);
            held_out(H);
            if (P_EP == 1) load_grp(cur[r ^ 1], ro, ri, ra, cb < NFULL ? chunk(cb) : 0x3FFFFFu, vo16, vo8, P_EA);
            if (P_DF == 1) pend_out(pd, rr);
            asm volatile("s_waitcnt vmcnt(%4)" : "+v"(cur[r].io), "+v"(cur[r].ii), "+v"(cur[r].an), "+v"(got) : "n"(2 + N_EA));
            convert(cur[r], cv[(r + 1) & 1]);
            if (wave0) {
                const unsigned g = __builtin_amdgcn_readfirstlane(got);
                unsigned nx = cb + P1_GRID;
                if (rd + r + 3 >= NSTAT) nx = st == 2 ? NSTAT * P1_GRID + g * NSH + shard : NFULL;
                if (st == 0 && g != 0) { st = 1; if (!mail) { mail = g; sided(); } }
                if (st == 0 && rd + r + 4 == NSTAT) {
                    for (int i = 0; i < 128 && st == 0; i++) {
                        unsigned x = 0;
                        asm volatile("buffer_atomic_add %0, %1, %2, 0 offen sc0\n s_waitcnt vmcnt(0)" : "+v"(x) : "v"(tid == 0 ? box_o : OOB), "s"(rw) : "memory");
                        if (__builtin_amdgcn_readfirstlane(x) != 0) st = 1;
                    }
                }
                if (st == 1 && rd + r + 4 >= NSTAT) st = 2;
                nx = cb < NFULL ? min(nx, NFULL) : NFULL;
                if ((tid & 63) == 0) { const v2u w2 = {nx, mail}; *(lds_v2u*)(size_t)(MS_OFF + 104) = w2; }
                got = st == 2 ? 1u : 0u;
                if (st != 1 && nx < NFULL)
                    asm volatile("buffer_atomic_add %0, %1, %2, 0 offen sc0" : "+v"(got) : "v"(tid == 0 ? (st == 2 ? ctr_o : box_o) : OOB), "s"(rw) : "memory");
                if (leader) asm volatile("buffer_store_dword %0, %1, %2, 0 offen sc1" :: "v"(mail), "v"(tid < 256 / NSH ? (256 + shard * (256 / NSH) + tid) * 4 : OOB), "s"(rw) : "memory");
            }
            load_grp(cur[r ^ 1], ro, ri, ra, cb < NFULL ? chunk(cb) : 0x3FFFFFu, vo16, vo8, 7 ^ P_EA);
            if (P_DF == 2) { asm volatile("" ::: "memory"); pend_out(pd, rr); asm volatile("" ::: "memory"); }
            place(H, v, cv[r & 1], vm, lb7);
            if (P_DF == 3) pend_out(pd, rr);
            asm volatile("s_waitcnt lgkmcnt(0)" ::: "memory");
            __syncthreads();
            const unsigned cnt = *ldsp(tid * 4);
            const unsigned nx = *ldsp(MS_OFF + 104);
            if (!mail) {
                mail = __builtin_amdgcn_readfirstlane(*ldsp(MS_OFF + 108));
                if (mail) sided(); else if (__ballot(cnt >= 16384u)) await();
            }
            flush(own, pd, wc, cnt, tid, base, rr, P_DF == 0);
            if (P_EA && !P_EP) {
                const unsigned cn = __builtin_amdgcn_readfirstlane(nx);
                load_grp(cur[r], ro, ri, ra, cn < NFULL ? chunk(cn) : 0x3FFFFFu, vo16, vo8, P_EA);
            }
            __syncthreads();
            if (ca >= NFULL) { done = true; break; }
            ca = cb;
            cb = ca < NFULL ? __builtin_amdgcn_readfirstlane(nx) : NFULL;
        }
        if (done) break;
    }
    asm volatile("" : "+v"(got));
    if (P_DF) pend_out(pd, rr);
    unsigned tc1 = touch_code(tid);
    if (!mail) await();
    held_out(H);
    asm volatile("s_waitcnt lgkmcnt(0)" ::: "memory");
    {
        v2u fl;
        if (tid < 64) {
            const unsigned sa = MS_OFF + 128 + (tid & 15) * 32 + (tid >> 4) * 4;
            unsigned mx = (tid & 15) < 10 ? *ldsp(sa) : 0u;
            float sq = (tid & 15) < 10 ? __uint_as_float(*ldsp(sa + 16)) : 0.f;
#pragma unroll
            for (int i = 0; i < 4; i++) {
                mx = max(mx, (unsigned)(i == 0 ? __builtin_amdgcn_update_dpp(0, (int)mx, 0x111, 0xF, 0xF, false) : i == 1 ? __builtin_amdgcn_update_dpp(0, (int)mx, 0x112, 0xF, 0xF, false)
                                        : i == 2 ? __builtin_amdgcn_update_dpp(0, (int)mx, 0x114, 0xF, 0xF, false) : __builtin_amdgcn_update_dpp(0, (int)mx, 0x118, 0xF, 0xF, false)));
                const int si = __float_as_int(sq);
                sq += __int_as_float(i == 0 ? __builtin_amdgcn_update_dpp(0, si, 0x111, 0xF, 0xF, false) : i == 1 ? __builtin_amdgcn_update_dpp(0, si, 0x112, 0xF, 0xF, false)
                                     : i == 2 ? __builtin_amdgcn_update_dpp(0, si, 0x114, 0xF, 0xF, false) : __builtin_amdgcn_update_dpp(0, si, 0x118, 0xF, 0xF, false));
            }
            mx = min(mx ? (mx >> 16) + 2 : 0u, 0x7F80u);
            fl.x = (mx << 16) | (unsigned)__builtin_amdgcn_update_dpp(0, (int)mx, 0x142, 0xA, 0xF, false);
            fl.y = (__float_as_uint(sq) & 0xFFFF0000u) | ((unsigned)__builtin_amdgcn_update_dpp(0, __float_as_int(sq), 0x142, 0xA, 0xF, false) >> 16);
        }
        if (own.ovf >= 16384u || own.nl > NPOS || wc > ly.cap || (seen & 0x30000000u)) *ldsp(MS_OFF + 96) = 1;
        const unsigned tt = ((tid & 31) << 5) | (tid >> 5);
        const unsigned d = *ldsp(tt * 4) - 64;
        unsigned P = (d * 3) >> 11;
        if (P >= 24) { *ldsp(MS_OFF + 96) = 1; P = 0; }
        const unsigned nu = (P + 2) / 3;
        const unsigned inc = wave_scan_add(nu);
        if ((tid & 63) == 63) *ldsp(MS_OFF + (tid >> 6) * 4) = inc;
        __syncthreads();
        unsigned wb = (tid & 63) < (tid >> 6) ? *ldsp(MS_OFF + (tid & 15) * 4) : 0;
        wb = __builtin_amdgcn_readlane(row_scan_add(wb), 15);
        unsigned fo = wb + inc - nu;
        if (fo + nu > ly.fcap) *ldsp(MS_OFF + 96) = 1;
        const unsigned fbase = pairidx(run) * (ly.F0 * 256);
        {
            const unsigned n = (fo * 33223) >> 24;
            fo = fo - n * 505 + n * 1024;
            fo |= ((((run + mail) >> 2) ^ (mail & 16 ? n : (fbase + fo) >> 17)) & 1) << 9;
            fo &= 0xFFFF;
        }
        *ldsp(WL_OFF + tt * 4) = (nu << 8) | (fo << 16);
        *ldsp(tid * 4) = (fo << 8) | P;
        __syncthreads();
        {
            const v4u te = {own.p[0], own.p[1], own.p[2], (own.p[3] & 15) | (min(own.nl, 15u) << 4) | ((base >> 1) & 4096) | *ldsp(WL_OFF + tid * 4)};
            __builtin_amdgcn_raw_buffer_store_b128(te, __builtin_amdgcn_make_buffer_rsrc((void*)(tab + run * NT * 4), (short)0, NT * 16, 0x00020000), tid * 16, 0, 16);
        }
        if (tid < 64) {
            if (*ldsp(MS_OFF + 96)) fl.x = 0x7F807F80u;
            if ((tid & 31) == 31) __hip_atomic_store((unsigned long long*)(flags + ((tid >> 5) * P1_GRID + run) * 2), ((unsigned long long)fl.y << 32) | fl.x, __ATOMIC_RELAXED, __HIP_MEMORY_SCOPE_AGENT);
        }
        const unsigned k = tid & 7;
        const unsigned pa = (run & 15) * 24 + k * 3;
        const __amdgpu_buffer_rsrc_t rf = __builtin_amdgcn_make_buffer_rsrc((void*)(rec + (size_t)fbase * 16), (short)0, (int)(ly.F0 * 4096), 0x00020000);
#pragma unroll 4
        for (int it = 0; it < 8; it++) {
            const unsigned u = (tid & ~63) + it * 8 + ((tid >> 3) & 7), t = ((u & 31) << 5) | (u >> 5);
            const unsigned w = *ldsp(u * 4), Pt = w & 255;
            v4u v = read_unit(LB + t * LS + k * 16);
            if (k * 3 + 1 >= Pt) { v.y = pad_rec(pa + 1); v.w |= 512u << 11; }
            if (k * 3 + 2 >= Pt) { v.z = pad_rec(pa + 2); v.w |= 512u << 22; }
            __builtin_amdgcn_raw_buffer_store_b128(v, rf, k * 3 < Pt ? ((w >> 8) + k) * 16 : OOB, 0, 16);
        }
        asm volatile("s_waitcnt vmcnt(8)" :: "v"(tc1));
    }
    WAIT_GRP(0, cur[0]); WAIT_GRP(0, cur[1]);
}

#define BIN_ARGS const int* __restrict__ idx_in, const int* __restrict__ idx_out, const unsigned short* __restrict__ angle, const unsigned short* __restrict__ ang_freq, \
    const unsigned short* __restrict__ m, const unsigned short* __restrict__ w_down, unsigned* __restrict__ mdp, int E, int T, \
    char* __restrict__ rec0, unsigned* __restrict__ tab, unsigned* __restrict__ flags, int NR, int per, Lay ly, unsigned* __restrict__ wgmode
#define BIN_PASS idx_in, idx_out, angle, ang_freq, m, w_down, mdp, E, T, rec0, tab, flags, NR, per, ly, wgmode
extern "C" __global__ __launch_bounds__(P1_THREADS) void k_bin(BIN_ARGS) {
    __shared__ __attribute__((aligned(16))) unsigned L[BIN_LDS / 4];
    bin_body<VDEF>(L, BIN_PASS);
}

struct AccArgs {
    const char* rec; const unsigned* tab; const unsigned* flags; const unsigned* mdp; float* part; unsigned* praw; unsigned* wgmode;
    const int* idx_in; const int* idx_out; const unsigned short* angle; const unsigned short* ang_freq; const unsigned short* m; const unsigned short* w_down;
    const unsigned short* dist; const unsigned short* centers; const unsigned short* w_rbf; const unsigned short* w_sbf; const unsigned short* w_up;
    unsigned short* out;
    int EP; unsigned O; int T; float c4; int E; int unused;
    Lay ly;
};
typedef char AccArgs_has_no_padding[sizeof(AccArgs) == 19 * 8 + 10 * 4 ? 1 : -1];

typedef const __attribute__((address_space(4))) unsigned* cptr;
static __device__ __forceinline__ void edge_gh(float d, const unsigned short* centers, const unsigned short* w_sbf, const unsigned short* w_rbf, float* g, float* h) {
#pragma unroll
    for (int k = 0; k < 4; k++) g[k] = h[k] = 0.f;
#pragma unroll
    for (int r = 0; r < 8; r++) {
        const unsigned cu = ((cptr)centers)[r >> 1];
        const float df = d - ((r & 1) ? hi_bf(cu) : lo_bf(cu));
        const float rb = __expf(-10.f * df * df);
#pragma unroll
        for (int k = 0; k < 2; k++) {
            const unsigned u = ((cptr)w_sbf)[r * 2 + k], v = ((cptr)w_rbf)[r * 2 + k];
            g[2 * k] += rb * lo_bf(u); g[2 * k + 1] += rb * hi_bf(u); h[2 * k] += rb * lo_bf(v); h[2 * k + 1] += rb * hi_bf(v);
        }
    }
}
static __device__ __forceinline__ v2u edge_out(const float* agg, const float* g, const float* h, unsigned m0, unsigned m1, const unsigned* wu) {
    float a[4];
#pragma unroll
    for (int k = 0; k < 4; k++) a[k] = agg[k] * g[k];
    const float mm[4] = {lo_bf(m0), hi_bf(m0), lo_bf(m1), hi_bf(m1)};
    float o[4];
#pragma unroll
    for (int dd = 0; dd < 4; dd++) {
        float s = 0.f;
#pragma unroll
        for (int k = 0; k < 4; k++) { const unsigned u = wu[k * 2 + (dd >> 1)]; s += a[k] * ((dd & 1) ? hi_bf(u) : lo_bf(u)); }
        o[dd] = mm[dd] + s * h[dd];
    }
    v2u r;
    asm("v_cvt_pk_bf16_f32 %0, %1, %2" : "=v"(r.x) : "v"(o[0]), "v"(o[1]));
    asm("v_cvt_pk_bf16_f32 %0, %1, %2" : "=v"(r.y) : "v"(o[2]), "v"(o[3]));
    return r;
}

typedef const __attribute__((address_space(4))) AccArgs* kaptr;
static __device__ __forceinline__ void acc_fallback(unsigned* lds, unsigned mode, const __attribute__((address_space(4))) AccArgs& a_, int tid) {
    const char* rec = rec_align(a_.rec); const unsigned* flags = a_.flags; const unsigned* mdp = a_.mdp;
    const int EP = a_.EP, T = a_.T;
    const Lay ly = {a_.ly.F0, a_.ly.cap, a_.ly.dumu, a_.ly.fcap};
    const int lane = tid & 63;
    const int xcd = blockIdx.x & 7, jj = blockIdx.x >> 3;
    const int qt = xcd >> 1, cp = jj & 1, b = (xcd & 1) * 16 + (jj >> 1);
    const unsigned O = a_.O;
    float* pout0 = a_.part + (size_t)(qt * 4 + cp * 2) * EP + (size_t)b * O;
    const unsigned G = tid >> 3, k = tid & 7;
    {
        unsigned* praw = a_.praw + (size_t)(qt * 2 + cp) * EP + (size_t)b * O;
        for (unsigned i = tid; i < O; i += P2_THREADS) praw[i] = 0;
    }
    if (mode == 1) {
        unsigned* acc = lds + OBASE;
        const float Xm = 2139095040.0f / (float)(NWS * (NPOS * 24 + 24)) - 2.0f;
#pragma unroll 1
        for (int c = 0; c < 2; c++) {
            __syncthreads();
            if (tid == 0) lds[0] = 0;
            __syncthreads();
            if (tid < NWS) { unsigned v = wave_scan_max((flags[(cp * P1_GRID + tid) * 2] >> (16 * c)) & 0xFFFF); if (lane == 63) atomicMax(&lds[0], v); }
            __syncthreads();
            unsigned mbits = __builtin_amdgcn_readfirstlane(lds[0]);
            if (mbits == 0) mbits = 0x3f80;
            const float M = __uint_as_float(mbits << 16);
            const float S = Xm / (511.0f * M), inv = M / Xm;
            float racc[20];
#pragma unroll
            for (int r = 0; r < 20; r++) racc[r] = 0.f;
#pragma unroll 1
            for (int s = 0; s < 8; s++) {
                __syncthreads();
                const unsigned* src = mdp + (size_t)cp * (32 * LMAX) + (size_t)(qt * 8 + s) * LMAX;
                for (unsigned i = tid; i < LMAX; i += P2_THREADS) {
                    unsigned w = src[i];
                    lds[(i & ~255u) | ((i & 3) << 6) | ((i >> 2) & 63)] = __float_as_uint((c ? __uint_as_float(w) : lo_bf(w)) * S);
                }
                for (int i = tid; i < OBASE; i += P2_THREADS) acc[i] = 0;
                __syncthreads();
                const unsigned t = b * 32 + qt * 8 + s;
                for (unsigned r = G; r < NWS; r += 128) {
                    v4u te = ((const v4u*)a_.tab)[r * NT + t];
                    const unsigned tw = te.w, nl = (tw >> 4) & 15, nu = (tw >> 8) & 15;
                    for (unsigned l = 0; l <= nl; l++) {
                        unsigned u;
                        if (l < nl) {
                            u = (((te.x & 1023) * LMUL) & LMASK) + logbase(t >> 6, r, P_XO) + ((tw >> 3) & 512) + k;
                            te.x = __builtin_amdgcn_alignbit(te.y, te.x, 10); te.y = __builtin_amdgcn_alignbit(te.z, te.y, 10); te.z = __builtin_amdgcn_alignbit(te.w, te.z, 10);
                        }
                        else { if (k >= nu) break; u = pairidx(r) * (ly.F0 * 256) + (tw >> 16) + k; }
                        v4u q = *(const v4u*)(rec + (size_t)u * 16);
                        unsigned a[3] = {q.x, q.y, q.z};
#pragma unroll
                        for (int j = 0; j < 3; j++) {
                            const int cq = (int)((q.w >> (j == 0 ? 1 : j == 1 ? 11 : 22)) & 1023) - 512;
                            const int x = __float2int_rn(__uint_as_float(lds[(a[j] >> 2) & 0x7FFF]) * (float)cq);
                            if ((a[j] >> 17) - IDXB < OBASE) atomicAdd(&acc[(a[j] >> 17) - IDXB], (unsigned)x);
                        }
                    }
                }
                __syncthreads();
#pragma unroll
                for (int r = 0; r < 20; r++) racc[r] += (float)(int)acc[r * P2_THREADS + tid] * inv;
            }
#pragma unroll
            for (int r = 0; r < 20; r++) if ((unsigned)(r * P2_THREADS + tid) < O) pout0[(size_t)c * EP + r * P2_THREADS + tid] = racc[r];
        }
    }
    if (mode == 2) {
        float* facc = (float*)lds;
        const float freq = bf2f(a_.ang_freq[0]);
        float wd[2][4];
#pragma unroll
        for (int kk = 0; kk < 4; kk++) { wd[0][kk] = bf2f(a_.w_down[kk * 4 + cp * 2]); wd[1][kk] = bf2f(a_.w_down[kk * 4 + cp * 2 + 1]); }
        __syncthreads();
        for (int i = tid; i < 2 * OBASE; i += P2_THREADS) facc[i] = 0.f;
        __syncthreads();
        for (int t = tid; t < T; t += P2_THREADS) {
            const unsigned io = a_.idx_out[t], ii = a_.idx_in[t];
            if (((io >> 7) & 31) == (unsigned)b && ((ii >> 3) & 3) == (unsigned)qt) {
                const v2u mv = ((const v2u*)a_.m)[ii];
                const float a[4] = {lo_bf(mv.x), hi_bf(mv.x), lo_bf(mv.y), hi_bf(mv.y)};
                const float cs = __cosf(freq * bf2f(a_.angle[t]));
                const unsigned ol = (io & 127u) | ((io >> 5) & ~127u);
#pragma unroll
                for (int c = 0; c < 2; c++)
                    atomicAdd(&facc[c * OBASE + ol], (a[0] * wd[c][0] + a[1] * wd[c][1] + a[2] * wd[c][2] + a[3] * wd[c][3]) * cs);
            }
        }
        __syncthreads();
        for (unsigned idx = tid; idx < O; idx += P2_THREADS) { pout0[idx] = facc[idx]; pout0[(size_t)EP + idx] = facc[OBASE + idx]; }
    }
}

#define SWAPX 2
#define RAUX ""
#define J0 (wave >> 1)
#define IDXMASK "0x8fffffff"
#define STR2(x) #x
#define STR(x) STR2(x)
#define PLAN_IT 160

extern "C" __global__ __launch_bounds__(P2_THREADS) void k_acc(AccArgs a_) {
    constexpr int AV = 0;
    const Lay ly = a_.ly;
    __shared__ __attribute__((aligned(16))) unsigned lds[2 * OBASE];
    const int tid = threadIdx.x;
    const int wave = __builtin_amdgcn_readfirstlane(tid >> 6), lane = tid & 63;
    const int xcd = blockIdx.x & 7, jj = blockIdx.x >> 3;
    const int qt = xcd >> 1, cp = jj & 1, b = (xcd & 1) * 16 + (jj >> 1);
    const unsigned O = a_.O;
    unsigned stv = 0;
    unsigned tc2 = touch_code(tid);
    unsigned* praw = a_.praw + (size_t)(qt * 2 + cp) * a_.EP + (size_t)b * O;
    const unsigned* mds = a_.mdp + (size_t)cp * (32 * LMAX) + (size_t)(qt * 8) * LMAX;
    unsigned mode = (unsigned)(size_t)lds ? 2u : 0u;
    unsigned exs = 0, bad = 0;
    v4u mm[2], dd;
    const int q = qt * 2 + cp;
    unsigned t16 = tid * 16;
    if (mode == 0) {
        const unsigned w = wave * 16 + ((lane & 15) ^ (cp ? SWAPX : 0)), t0 = b * 32 + qt * 8 + (lane >> 4) * 2;
        v4i_rs rs = mk_rsrc(rec_align(a_.rec), ly.dumu + 64);
        rs.y |= 16 << 16;
        const v4i_rs rm = mk_rsrc(mds, 8 * LMAX * 4);
        const v4i_rs rp0 = mk_rsrc(praw, O * 4);
        const v4i_rs rt = mk_rsrc(a_.tab, (NT * NWS * 4 + FL_SIZE) * 4);
        const v4i_rs rmm = mk_rsrc(a_.m, a_.E * 8), rd = mk_rsrc(a_.dist, a_.E * 2);
        asm volatile(
            "s_setprio 3\n"
            "buffer_load_dwordx4 v[32:35], %[tof], %[rt], 0 offen\n"
            "buffer_load_dwordx4 v[36:39], %[tof], %[rt], 0 offen offset:16\n"
            "v_and_b32 v10, 1023, %[tid16]\n"
            "v_bfrev_b32 v12, 1\n"
            "v_add_u32 v11, %[pb], v10\n"
            "v_bfrev_b32 v13, 1\n"
            "v_bfrev_b32 v14, 1\n"
            "v_bfrev_b32 v15, 1\n"
            "ds_write_b128 v11, v[12:15] offset:0\n"
            "ds_write_b128 v11, v[12:15] offset:1024\n"
            "ds_write_b128 v11, v[12:15] offset:2048\n"
            "ds_write_b128 v11, v[12:15] offset:3072\n"
            "ds_write_b128 v11, v[12:15] offset:4096\n"
            "v_bfrev_b32 v80, 1\n"
            "v_mov_b32 v84, 0xb9000000\n"
            "v_mov_b32 v88, 0\n"
            "v_add_u32 v89, 81920, %[tid16]\n"
            "v_add_u32 v90, 65536, v89\n"
            "v_lshrrev_b32 v16, 4, v10\n"
            "v_add_u32 v83, %[dumu], v16\n"
            "v_and_b32 v17, 7, v16\n"
            "v_lshl_or_b32 v82, v17, 28, v17\n"
            "v_lshrrev_b32 v18, 3, v16\n"
            "v_lshl_add_u32 v17, v17, 3, v18\n"
            "v_lshlrev_b32 v17, 2, v17\n"
            "v_add_u32 v17, %[pb], v17\n"
            "s_getpc_b64 s[70:71]\n"
            "Lpc%=:\n"
            "s_add_u32 s72, s70, Lchg2%=-Lpc%=\n s_addc_u32 s73, s71, 0\n"
            "s_add_u32 s66, s70, Lchg3%=-Lpc%=\n s_addc_u32 s67, s71, 0\n"
            "s_add_u32 s70, s70, Lchg1%=-Lpc%=\n s_addc_u32 s71, s71, 0\n"
            "s_waitcnt vmcnt(0)\n"
            "buffer_load_dwordx4 v[92:95], v10, %[rt], %[fof] offen\n"
            "buffer_load_dwordx4 v[96:99], v10, %[rt], %[fof] offen offset:1024\n"
            "s_mov_b32 s94, 0\n"
            "buffer_load_dwordx4 v[40:43], %[tid16], %[rm], s94 offen\n"
            "s_add_u32 s96, s94, 16384\n"
            "buffer_load_dwordx4 v[44:47], %[tid16], %[rm], s96 offen\n"
            "s_add_u32 s96, s94, 32768\n"
            "buffer_load_dwordx4 v[48:51], %[tid16], %[rm], s96 offen\n"
            "s_add_u32 s96, s94, 49152\n"
            "buffer_load_dwordx4 v[52:55], %[tid16], %[rm], s96 offen\n"
            "s_add_u32 s96, s94, 65536\n"
            "buffer_load_dwordx4 v[56:59], %[tid16], %[rm], s96 offen\n"
            "v_bfe_u32 v20, v35, 4, 4\n v_bfe_u32 v21, v39, 4, 4\n v_bfe_u32 v22, v35, 8, 4\n v_bfe_u32 v23, v39, 8, 4\n"
            "v_min_u32 v24, 1, v22\n v_min_u32 v25, 1, v23\n v_add_u32 v24, v24, v20\n v_add_u32 v25, v25, v21\n"
            "v_lshl_or_b32 v24, v25, 16, v24\n"
            "v_lshrrev_b32 v8, 16, v35\n v_lshrrev_b32 v9, 16, v39\n"
            "v_add_u32_dpp v25, v24, v24 row_shr:1 row_mask:0xf bank_mask:0xf bound_ctrl:1\n"
            "v_add_u32 v8, %[fb], v8\n v_add_u32 v9, %[fb], v9\n"
            "v_add_u32_dpp v25, v25, v25 row_shr:2 row_mask:0xf bank_mask:0xf bound_ctrl:1\n"
            "v_sub_u32 v26, 8, v22\n v_sub_u32 v27, 8, v23\n"
            "v_add_u32_dpp v25, v25, v25 row_shr:4 row_mask:0xf bank_mask:0xf bound_ctrl:1\n"
            "v_lshl_or_b32 v8, v26, 28, v8\n v_lshl_or_b32 v9, v27, 28, v9\n"
            "v_add_u32_dpp v25, v25, v25 row_shr:8 row_mask:0xf bank_mask:0xf bound_ctrl:1\n"
            "s_nop 1\n"
            "v_readlane_b32 s84, v25, 15\n"
            "v_readlane_b32 s85, v25, 31\n"
            "v_readlane_b32 s86, v25, 47\n"
            "v_readlane_b32 s87, v25, 63\n"
            "v_sub_u32 v25, v25, v24\n"
            "s_mov_b32 s96, 0\n s_mov_b64 s[76:77], 0\n"
            "s_add_u32 s84, s84, 0x70007\n"
            "s_bfe_u32 s88, s84, 0xd0003\n s_lshr_b32 s89, s84, 19\n"
            "s_max_u32 s88, s88, 8\n s_max_u32 s89, s89, 8\n"
            "s_add_u32 s90, s96, s88\n"
            "s_pack_ll_b32_b16 s91, s96, s90\n"
            "s_add_u32 s96, s90, s89\n"
            "s_lshl_b32 s89, s89, 8\n s_or_b32 s88, s88, s89\n s_and_b32 s88, s88, 0xffff\n"
            "s_or_b32 s76, s76, s88\n"
            "s_lshl_b32 s91, s91, 3\n"
            "v_mov_b32 v26, s91\n"
            "s_add_u32 s85, s85, 0x70007\n"
            "s_bfe_u32 s88, s85, 0xd0003\n s_lshr_b32 s89, s85, 19\n"
            "s_max_u32 s88, s88, 8\n s_max_u32 s89, s89, 8\n"
            "s_add_u32 s90, s96, s88\n"
            "s_pack_ll_b32_b16 s91, s96, s90\n"
            "s_add_u32 s96, s90, s89\n"
            "s_lshl_b32 s89, s89, 8\n s_or_b32 s88, s88, s89\n s_and_b32 s88, s88, 0xffff\n"
            "s_lshl_b32 s88, s88, 16\n"
            "s_or_b32 s76, s76, s88\n"
            "s_lshl_b32 s91, s91, 3\n"
            "s_mov_b32 exec_lo, 0xffff0000\n s_mov_b32 exec_hi, 0xffffffff\n"
            "v_mov_b32 v26, s91\n"
            "s_add_u32 s86, s86, 0x70007\n"
            "s_bfe_u32 s88, s86, 0xd0003\n s_lshr_b32 s89, s86, 19\n"
            "s_max_u32 s88, s88, 8\n s_max_u32 s89, s89, 8\n"
            "s_add_u32 s90, s96, s88\n"
            "s_pack_ll_b32_b16 s91, s96, s90\n"
            "s_add_u32 s96, s90, s89\n"
            "s_lshl_b32 s89, s89, 8\n s_or_b32 s88, s88, s89\n s_and_b32 s88, s88, 0xffff\n"
            "s_or_b32 s77, s77, s88\n"
            "s_lshl_b32 s91, s91, 3\n"
            "s_mov_b32 exec_lo, 0x0\n s_mov_b32 exec_hi, 0xffffffff\n"
            "v_mov_b32 v26, s91\n"
            "s_add_u32 s87, s87, 0x70007\n"
            "s_bfe_u32 s88, s87, 0xd0003\n s_lshr_b32 s89, s87, 19\n"
            "s_max_u32 s88, s88, 8\n s_max_u32 s89, s89, 8\n"
            "s_add_u32 s90, s96, s88\n"
            "s_pack_ll_b32_b16 s91, s96, s90\n"
            "s_add_u32 s96, s90, s89\n"
            "s_lshl_b32 s89, s89, 8\n s_or_b32 s88, s88, s89\n s_and_b32 s88, s88, 0xffff\n"
            "s_lshl_b32 s88, s88, 16\n"
            "s_or_b32 s77, s77, s88\n"
            "s_lshl_b32 s91, s91, 3\n"
            "s_mov_b32 exec_lo, 0x0\n s_mov_b32 exec_hi, 0xffff0000\n"
            "v_mov_b32 v26, s91\n"
            "s_mov_b64 exec, -1\n"
            "s_mov_b32 %[bad], 0\n"
            "s_cmp_gt_u32 s96, " STR(PLAN_IT) "\n"
            "s_cbranch_scc0 1f\n"
            "s_mov_b32 %[bad], 1\n s_mov_b32 s76, 0x08080808\n s_mov_b32 s77, 0x08080808\n"
            "s_branch Lnoplan%=\n"
            "1:\n"
            "v_add_u32 v25, v25, v26\n"
            "v_and_b32 v26, 0xffff, v25\n v_lshrrev_b32 v27, 16, v25\n"
            "v_lshlrev_b32 v26, 2, v26\n v_lshlrev_b32 v27, 2, v27\n"
            "v_add_u32 v26, %[pb], v26\n v_add_u32 v27, %[pb], v27\n"
            "v_lshl_add_u32 v28, v20, 2, v26\n v_lshl_add_u32 v29, v21, 2, v27\n"
            "v_cmp_ne_u32 vcc, 0, v22\n v_cmp_ne_u32 s[88:89], 0, v23\n"
            "v_lshrrev_b32 v30, 3, v35\n v_and_b32 v30, 0x200, v30\n v_or_b32 v30, %[xb], v30\n"
            "s_mov_b64 exec, vcc\n ds_write_b32 v28, v8\n s_mov_b64 exec, s[88:89]\n ds_write_b32 v29, v9\n s_mov_b64 exec, -1\n"
            "v_min_u32 v24, v20, v21\n v_mov_b32 v31, %[lmask]\n s_movk_i32 s91, 1023\n"
            "v_cmp_gt_u32 vcc, 4, v24\n"
            "s_cmp_eq_u64 vcc, 0\n s_cbranch_scc1 4f\n"
            "v_cmp_lt_u32 vcc, 0, v20\n v_cmp_lt_u32 s[88:89], 0, v21\n"
            "v_bfe_u32 v28, v32, 0, 10\n"
            "v_bfe_u32 v29, v36, 0, 10\n"
            "s_or_b64 s[84:85], vcc, s[88:89]\n"
            "s_cbranch_scc0 Lnoplan%=\n"
            "v_mul_u32_u24 v28, %[lmul], v28\n"
            "v_mul_u32_u24 v29, %[lmul], v29\n"
            "v_and_or_b32 v28, v28, v31, v30\n"
            "v_and_or_b32 v29, v29, v31, v30\n"
            "s_mov_b64 exec, vcc\n ds_write_b32 v26, v28 offset:0\n s_mov_b64 exec, s[88:89]\n ds_write_b32 v27, v29 offset:0\n s_mov_b64 exec, -1\n"
            "v_cmp_lt_u32 vcc, 1, v20\n v_cmp_lt_u32 s[88:89], 1, v21\n"
            "v_bfe_u32 v28, v32, 10, 10\n"
            "v_bfe_u32 v29, v36, 10, 10\n"
            "s_or_b64 s[84:85], vcc, s[88:89]\n"
            "s_cbranch_scc0 Lnoplan%=\n"
            "v_mul_u32_u24 v28, %[lmul], v28\n"
            "v_mul_u32_u24 v29, %[lmul], v29\n"
            "v_and_or_b32 v28, v28, v31, v30\n"
            "v_and_or_b32 v29, v29, v31, v30\n"
            "s_mov_b64 exec, vcc\n ds_write_b32 v26, v28 offset:4\n s_mov_b64 exec, s[88:89]\n ds_write_b32 v27, v29 offset:4\n s_mov_b64 exec, -1\n"
            "v_cmp_lt_u32 vcc, 2, v20\n v_cmp_lt_u32 s[88:89], 2, v21\n"
            "v_bfe_u32 v28, v32, 20, 10\n"
            "v_bfe_u32 v29, v36, 20, 10\n"
            "s_or_b64 s[84:85], vcc, s[88:89]\n"
            "s_cbranch_scc0 Lnoplan%=\n"
            "v_mul_u32_u24 v28, %[lmul], v28\n"
            "v_mul_u32_u24 v29, %[lmul], v29\n"
            "v_and_or_b32 v28, v28, v31, v30\n"
            "v_and_or_b32 v29, v29, v31, v30\n"
            "s_mov_b64 exec, vcc\n ds_write_b32 v26, v28 offset:8\n s_mov_b64 exec, s[88:89]\n ds_write_b32 v27, v29 offset:8\n s_mov_b64 exec, -1\n"
            "v_cmp_lt_u32 vcc, 3, v20\n v_cmp_lt_u32 s[88:89], 3, v21\n"
            "v_alignbit_b32 v28, v33, v32, 30\n"
            "v_and_b32 v28, s91, v28\n"
            "v_alignbit_b32 v29, v37, v36, 30\n"
            "v_and_b32 v29, s91, v29\n"
            "s_or_b64 s[84:85], vcc, s[88:89]\n"
            "s_cbranch_scc0 Lnoplan%=\n"
            "v_mul_u32_u24 v28, %[lmul], v28\n"
            "v_mul_u32_u24 v29, %[lmul], v29\n"
            "v_and_or_b32 v28, v28, v31, v30\n"
            "v_and_or_b32 v29, v29, v31, v30\n"
            "s_mov_b64 exec, vcc\n ds_write_b32 v26, v28 offset:12\n s_mov_b64 exec, s[88:89]\n ds_write_b32 v27, v29 offset:12\n s_mov_b64 exec, -1\n"
            "s_branch 5f\n"
            "4:\n"
            "v_bfe_u32 v12, v32, 0, 10\n"
            "v_bfe_u32 v13, v32, 10, 10\n"
            "v_bfe_u32 v14, v36, 0, 10\n"
            "v_bfe_u32 v15, v36, 10, 10\n"
            "v_mul_u32_u24 v12, %[lmul], v12\n"
            "v_mul_u32_u24 v13, %[lmul], v13\n"
            "v_mul_u32_u24 v14, %[lmul], v14\n"
            "v_mul_u32_u24 v15, %[lmul], v15\n"
            "v_and_or_b32 v12, v12, v31, v30\n"
            "v_and_or_b32 v13, v13, v31, v30\n"
            "v_and_or_b32 v14, v14, v31, v30\n"
            "v_and_or_b32 v15, v15, v31, v30\n"
            "ds_write2_b32 v26, v12, v13 offset0:0 offset1:1\n ds_write2_b32 v27, v14, v15 offset0:0 offset1:1\n"
            "v_bfe_u32 v12, v32, 20, 10\n"
            "v_alignbit_b32 v13, v33, v32, 30\n"
            "v_and_b32 v13, s91, v13\n"
            "v_bfe_u32 v14, v36, 20, 10\n"
            "v_alignbit_b32 v15, v37, v36, 30\n"
            "v_and_b32 v15, s91, v15\n"
            "v_mul_u32_u24 v12, %[lmul], v12\n"
            "v_mul_u32_u24 v13, %[lmul], v13\n"
            "v_mul_u32_u24 v14, %[lmul], v14\n"
            "v_mul_u32_u24 v15, %[lmul], v15\n"
            "v_and_or_b32 v12, v12, v31, v30\n"
            "v_and_or_b32 v13, v13, v31, v30\n"
            "v_and_or_b32 v14, v14, v31, v30\n"
            "v_and_or_b32 v15, v15, v31, v30\n"
            "ds_write2_b32 v26, v12, v13 offset0:2 offset1:3\n ds_write2_b32 v27, v14, v15 offset0:2 offset1:3\n"
            "5:\n"
            "v_cmp_lt_u32 vcc, 4, v20\n v_cmp_lt_u32 s[88:89], 4, v21\n"
            "v_bfe_u32 v28, v33, 8, 10\n"
            "v_bfe_u32 v29, v37, 8, 10\n"
            "s_or_b64 s[84:85], vcc, s[88:89]\n"
            "s_cbranch_scc0 Lnoplan%=\n"
            "v_mul_u32_u24 v28, %[lmul], v28\n"
            "v_mul_u32_u24 v29, %[lmul], v29\n"
            "v_and_or_b32 v28, v28, v31, v30\n"
            "v_and_or_b32 v29, v29, v31, v30\n"
            "s_mov_b64 exec, vcc\n ds_write_b32 v26, v28 offset:16\n s_mov_b64 exec, s[88:89]\n ds_write_b32 v27, v29 offset:16\n s_mov_b64 exec, -1\n"
            "v_cmp_lt_u32 vcc, 5, v20\n v_cmp_lt_u32 s[88:89], 5, v21\n"
            "v_bfe_u32 v28, v33, 18, 10\n"
            "v_bfe_u32 v29, v37, 18, 10\n"
            "s_or_b64 s[84:85], vcc, s[88:89]\n"
            "s_cbranch_scc0 Lnoplan%=\n"
            "v_mul_u32_u24 v28, %[lmul], v28\n"
            "v_mul_u32_u24 v29, %[lmul], v29\n"
            "v_and_or_b32 v28, v28, v31, v30\n"
            "v_and_or_b32 v29, v29, v31, v30\n"
            "s_mov_b64 exec, vcc\n ds_write_b32 v26, v28 offset:20\n s_mov_b64 exec, s[88:89]\n ds_write_b32 v27, v29 offset:20\n s_mov_b64 exec, -1\n"
            "v_cmp_lt_u32 vcc, 6, v20\n v_cmp_lt_u32 s[88:89], 6, v21\n"
            "v_alignbit_b32 v28, v34, v33, 28\n"
            "v_and_b32 v28, s91, v28\n"
            "v_alignbit_b32 v29, v38, v37, 28\n"
            "v_and_b32 v29, s91, v29\n"
            "s_or_b64 s[84:85], vcc, s[88:89]\n"
            "s_cbranch_scc0 Lnoplan%=\n"
            "v_mul_u32_u24 v28, %[lmul], v28\n"
            "v_mul_u32_u24 v29, %[lmul], v29\n"
            "v_and_or_b32 v28, v28, v31, v30\n"
            "v_and_or_b32 v29, v29, v31, v30\n"
            "s_mov_b64 exec, vcc\n ds_write_b32 v26, v28 offset:24\n s_mov_b64 exec, s[88:89]\n ds_write_b32 v27, v29 offset:24\n s_mov_b64 exec, -1\n"
            "v_cmp_lt_u32 vcc, 7, v20\n v_cmp_lt_u32 s[88:89], 7, v21\n"
            "v_bfe_u32 v28, v34, 6, 10\n"
            "v_bfe_u32 v29, v38, 6, 10\n"
            "s_or_b64 s[84:85], vcc, s[88:89]\n"
            "s_cbranch_scc0 Lnoplan%=\n"
            "v_mul_u32_u24 v28, %[lmul], v28\n"
            "v_mul_u32_u24 v29, %[lmul], v29\n"
            "v_and_or_b32 v28, v28, v31, v30\n"
            "v_and_or_b32 v29, v29, v31, v30\n"
            "s_mov_b64 exec, vcc\n ds_write_b32 v26, v28 offset:28\n s_mov_b64 exec, s[88:89]\n ds_write_b32 v27, v29 offset:28\n s_mov_b64 exec, -1\n"
            "v_cmp_lt_u32 vcc, 8, v20\n v_cmp_lt_u32 s[88:89], 8, v21\n"
            "v_bfe_u32 v28, v34, 16, 10\n"
            "v_bfe_u32 v29, v38, 16, 10\n"
            "s_or_b64 s[84:85], vcc, s[88:89]\n"
            "s_cbranch_scc0 Lnoplan%=\n"
            "v_mul_u32_u24 v28, %[lmul], v28\n"
            "v_mul_u32_u24 v29, %[lmul], v29\n"
            "v_and_or_b32 v28, v28, v31, v30\n"
            "v_and_or_b32 v29, v29, v31, v30\n"
            "s_mov_b64 exec, vcc\n ds_write_b32 v26, v28 offset:32\n s_mov_b64 exec, s[88:89]\n ds_write_b32 v27, v29 offset:32\n s_mov_b64 exec, -1\n"
            "v_cmp_lt_u32 vcc, 9, v20\n v_cmp_lt_u32 s[88:89], 9, v21\n"
            "v_alignbit_b32 v28, v35, v34, 26\n"
            "v_and_b32 v28, s91, v28\n"
            "v_alignbit_b32 v29, v39, v38, 26\n"
            "v_and_b32 v29, s91, v29\n"
            "s_or_b64 s[84:85], vcc, s[88:89]\n"
            "s_cbranch_scc0 Lnoplan%=\n"
            "v_mul_u32_u24 v28, %[lmul], v28\n"
            "v_mul_u32_u24 v29, %[lmul], v29\n"
            "v_and_or_b32 v28, v28, v31, v30\n"
            "v_and_or_b32 v29, v29, v31, v30\n"
            "s_mov_b64 exec, vcc\n ds_write_b32 v26, v28 offset:36\n s_mov_b64 exec, s[88:89]\n ds_write_b32 v27, v29 offset:36\n s_mov_b64 exec, -1\n"
            "Lnoplan%=:\n"
            "ds_read_b32 v60, v17 offset:0\n"
            "ds_read_b32 v61, v17 offset:256\n"
            "ds_read_b32 v62, v17 offset:512\n"
            "ds_read_b32 v63, v17 offset:768\n"
            "ds_read_b32 v64, v17 offset:1024\n"
            "ds_read_b32 v65, v17 offset:1280\n"
            "ds_read_b32 v66, v17 offset:1536\n"
            "ds_read_b32 v67, v17 offset:1792\n"
            "ds_read_b32 v68, v17 offset:2048\n"
            "ds_read_b32 v69, v17 offset:2304\n"
            "ds_read_b32 v70, v17 offset:2560\n"
            "ds_read_b32 v71, v17 offset:2816\n"
            "ds_read_b32 v72, v17 offset:3072\n"
            "ds_read_b32 v73, v17 offset:3328\n"
            "ds_read_b32 v74, v17 offset:3584\n"
            "ds_read_b32 v75, v17 offset:3840\n"
            "ds_read_b32 v76, v17 offset:4096\n"
            "ds_read_b32 v77, v17 offset:4352\n"
            "ds_read_b32 v78, v17 offset:4608\n"
            "ds_read_b32 v79, v17 offset:4864\n"
            "v_mov_b32 v100, 0\n"
            "v_mov_b32 v101, 0\n"
            "v_mov_b32 v102, 0\n"
            "v_mov_b32 v103, 0\n"
            "v_mov_b32 v107, v11\n"
            "s_mov_b32 s82, 0\n"
            "s_waitcnt lgkmcnt(15)\n"
            "v_mov_b32 v81, v60\n"
            "s_nop 1\n"
            "v_add_u32_dpp v111, v81, v82 row_newbcast:0 row_mask:0xf bank_mask:0x3\n"
            "v_add_u32_dpp v111, v81, v82 row_newbcast:8 row_mask:0xf bank_mask:0xc\n"
            "v_and_b32 v111, " IDXMASK ", v111\n"
            "v_min_u32 v111, v111, v83\n"
            "buffer_load_dwordx4 v[8:11], v111, %[rs], 0 idxen " RAUX "\n"
            "v_add_u32_dpp v111, v81, v82 row_newbcast:1 row_mask:0xf bank_mask:0x3\n"
            "v_add_u32_dpp v111, v81, v82 row_newbcast:9 row_mask:0xf bank_mask:0xc\n"
            "v_and_b32 v111, " IDXMASK ", v111\n"
            "v_min_u32 v111, v111, v83\n"
            "buffer_load_dwordx4 v[12:15], v111, %[rs], 0 idxen " RAUX "\n"
            "v_add_u32_dpp v111, v81, v82 row_newbcast:2 row_mask:0xf bank_mask:0x3\n"
            "v_add_u32_dpp v111, v81, v82 row_newbcast:10 row_mask:0xf bank_mask:0xc\n"
            "v_and_b32 v111, " IDXMASK ", v111\n"
            "v_min_u32 v111, v111, v83\n"
            "buffer_load_dwordx4 v[16:19], v111, %[rs], 0 idxen " RAUX "\n"
            "v_add_u32_dpp v111, v81, v82 row_newbcast:3 row_mask:0xf bank_mask:0x3\n"
            "v_add_u32_dpp v111, v81, v82 row_newbcast:11 row_mask:0xf bank_mask:0xc\n"
            "v_and_b32 v111, " IDXMASK ", v111\n"
            "v_min_u32 v111, v111, v83\n"
            "buffer_load_dwordx4 v[20:23], v111, %[rs], 0 idxen " RAUX "\n"
            "v_add_u32_dpp v111, v81, v82 row_newbcast:4 row_mask:0xf bank_mask:0x3\n"
            "v_add_u32_dpp v111, v81, v82 row_newbcast:12 row_mask:0xf bank_mask:0xc\n"
            "v_and_b32 v111, " IDXMASK ", v111\n"
            "v_min_u32 v111, v111, v83\n"
            "buffer_load_dwordx4 v[24:27], v111, %[rs], 0 idxen " RAUX "\n"
            "v_add_u32_dpp v111, v81, v82 row_newbcast:5 row_mask:0xf bank_mask:0x3\n"
            "v_add_u32_dpp v111, v81, v82 row_newbcast:13 row_mask:0xf bank_mask:0xc\n"
            "v_and_b32 v111, " IDXMASK ", v111\n"
            "v_min_u32 v111, v111, v83\n"
            "buffer_load_dwordx4 v[28:31], v111, %[rs], 0 idxen " RAUX "\n"
            "v_add_u32_dpp v111, v81, v82 row_newbcast:6 row_mask:0xf bank_mask:0x3\n"
            "v_add_u32_dpp v111, v81, v82 row_newbcast:14 row_mask:0xf bank_mask:0xc\n"
            "v_and_b32 v111, " IDXMASK ", v111\n"
            "v_min_u32 v111, v111, v83\n"
            "buffer_load_dwordx4 v[32:35], v111, %[rs], 0 idxen " RAUX "\n"
            "v_add_u32_dpp v111, v81, v82 row_newbcast:7 row_mask:0xf bank_mask:0x3\n"
            "v_add_u32_dpp v111, v81, v82 row_newbcast:15 row_mask:0xf bank_mask:0xc\n"
            "v_and_b32 v111, " IDXMASK ", v111\n"
            "v_min_u32 v111, v111, v83\n"
            "buffer_load_dwordx4 v[36:39], v111, %[rs], 0 idxen " RAUX "\n"
            "s_min_u32 s82, s82, 19\n"
            "s_add_u32 s82, s82, 1\n"
            "s_set_gpr_idx_on s82, gpr_idx(SRC0)\n"
            "s_nop 0\n"
            "v_mov_b32 v81, v60\n"
            "s_set_gpr_idx_off\n"
            "s_setprio 0\n"
            "ds_write_b128 v107, v[100:103] offset:0\n"
            "ds_write_b128 v107, v[100:103] offset:1024\n"
            "ds_write_b128 v107, v[100:103] offset:2048\n"
            "ds_write_b128 v107, v[100:103] offset:3072\n"
            "ds_write_b128 v107, v[100:103] offset:4096\n"
            "s_waitcnt vmcnt(13)\n"
            "v_pk_max_u16 v92, v92, v94\n v_pk_max_u16 v96, v96, v98\n"
            "v_lshlrev_b32 v104, 16, v93\n v_lshlrev_b32 v105, 16, v95\n v_lshlrev_b32 v106, 16, v97\n v_lshlrev_b32 v108, 16, v99\n"
            "v_pk_max_u16 v92, v92, v96\n"
            "v_add_f32 v104, v104, v105\n v_add_f32 v106, v106, v108\n v_add_f32 v93, v93, v95\n v_add_f32 v97, v97, v99\n"
            "v_and_b32 v94, 0xffff, v92\n v_lshrrev_b32 v92, 16, v92\n"
            "v_add_f32 v104, v104, v106\n v_add_f32 v93, v93, v97\n"
            "s_nop 0\n"
            "v_max_u32_dpp v94, v94, v94 row_shr:1 row_mask:0xf bank_mask:0xf bound_ctrl:1\n"
            "v_max_u32_dpp v92, v92, v92 row_shr:1 row_mask:0xf bank_mask:0xf bound_ctrl:1\n"
            "v_add_f32_dpp v104, v104, v104 row_shr:1 row_mask:0xf bank_mask:0xf bound_ctrl:1\n"
            "v_add_f32_dpp v93, v93, v93 row_shr:1 row_mask:0xf bank_mask:0xf bound_ctrl:1\n"
            "v_max_u32_dpp v94, v94, v94 row_shr:2 row_mask:0xf bank_mask:0xf bound_ctrl:1\n"
            "v_max_u32_dpp v92, v92, v92 row_shr:2 row_mask:0xf bank_mask:0xf bound_ctrl:1\n"
            "v_add_f32_dpp v104, v104, v104 row_shr:2 row_mask:0xf bank_mask:0xf bound_ctrl:1\n"
            "v_add_f32_dpp v93, v93, v93 row_shr:2 row_mask:0xf bank_mask:0xf bound_ctrl:1\n"
            "v_max_u32_dpp v94, v94, v94 row_shr:4 row_mask:0xf bank_mask:0xf bound_ctrl:1\n"
            "v_max_u32_dpp v92, v92, v92 row_shr:4 row_mask:0xf bank_mask:0xf bound_ctrl:1\n"
            "v_add_f32_dpp v104, v104, v104 row_shr:4 row_mask:0xf bank_mask:0xf bound_ctrl:1\n"
            "v_add_f32_dpp v93, v93, v93 row_shr:4 row_mask:0xf bank_mask:0xf bound_ctrl:1\n"
            "v_max_u32_dpp v94, v94, v94 row_shr:8 row_mask:0xf bank_mask:0xf bound_ctrl:1\n"
            "v_max_u32_dpp v92, v92, v92 row_shr:8 row_mask:0xf bank_mask:0xf bound_ctrl:1\n"
            "v_add_f32_dpp v104, v104, v104 row_shr:8 row_mask:0xf bank_mask:0xf bound_ctrl:1\n"
            "v_add_f32_dpp v93, v93, v93 row_shr:8 row_mask:0xf bank_mask:0xf bound_ctrl:1\n"
            "v_max_u32_dpp v94, v94, v94 row_bcast:15 row_mask:0xa bank_mask:0xf\n"
            "v_max_u32_dpp v92, v92, v92 row_bcast:15 row_mask:0xa bank_mask:0xf\n"
            "v_add_f32_dpp v104, v104, v104 row_bcast:15 row_mask:0xa bank_mask:0xf\n"
            "v_add_f32_dpp v93, v93, v93 row_bcast:15 row_mask:0xa bank_mask:0xf\n"
            "v_max_u32_dpp v94, v94, v94 row_bcast:31 row_mask:0xc bank_mask:0xf\n"
            "v_max_u32_dpp v92, v92, v92 row_bcast:31 row_mask:0xc bank_mask:0xf\n"
            "v_add_f32_dpp v104, v104, v104 row_bcast:31 row_mask:0xc bank_mask:0xf\n"
            "v_add_f32_dpp v93, v93, v93 row_bcast:31 row_mask:0xc bank_mask:0xf\n"
            "s_nop 1\n"
            "v_readlane_b32 s84, v94, 63\n v_readlane_b32 s85, v92, 63\n v_readlane_b32 s86, v104, 63\n v_readlane_b32 s87, v93, 63\n"
            "s_mov_b32 %[mode], 0\n"
            "s_max_u32 s96, s84, s85\n s_cmp_ge_u32 s96, 0x7f00\n s_cselect_b32 %[mode], 2, 0\n"
            "s_cmp_eq_u32 s84, 0\n s_cselect_b32 s84, 0x3f80, s84\n s_cselect_b32 s86, 0x7f000000, s86\n"
            "s_cmp_eq_u32 s85, 0\n s_cselect_b32 s85, 0x3f80, s85\n s_cselect_b32 s87, 0x7f000000, s87\n"
            "s_min_u32 s96, s84, s85\n s_cmp_lt_u32 s96, 0x800\n s_cselect_b32 %[mode], 2, %[mode]\n"
            "s_cmp_lg_u32 %[mode], 0\n"
            "s_cbranch_scc1 Lend%=\n"
            "s_lshl_b32 s84, s84, 16\n s_lshl_b32 s85, s85, 16\n"
            "v_mov_b32 v104, s84\n v_mov_b32 v105, s85\n"
            "v_mul_f32 v106, 0x3e82d0e5, v104\n v_mul_f32 v108, 0x3e82d0e5, v105\n"
            "v_mul_f32 v104, v104, v104\n v_mul_f32 v105, v105, v105\n"
            "v_add_u32 v106, 0x7fffff, v106\n v_add_u32 v108, 0x7fffff, v108\n"
            "v_mul_f32 v104, %[c4], v104\n v_mul_f32 v105, %[c4], v105\n"
            "v_and_b32 v106, 0x7f800000, v106\n v_and_b32 v108, 0x7f800000, v108\n"
            "v_cmp_lt_f32 vcc, s86, v104\n v_cmp_lt_f32 s[88:89], s87, v105\n"
            "v_add_u32 v86, 0xc00000, v106\n v_add_u32 v87, 0xc00000, v108\n"
            "v_lshrrev_b32 v106, 23, v106\n v_lshrrev_b32 v108, 15, v108\n"
            "v_or_b32 %[exs], v106, v108\n"
            "s_or_b64 vcc, vcc, s[88:89]\n"
            "s_cselect_b32 %[mode], 1, 0\n"
            "s_cbranch_scc1 Lend%=\n"
            "s_waitcnt vmcnt(8)\n"
            "s_mov_b32 m0, %[wb]\n"
            "s_nop 0\n"
            "ds_write_addtid_b32 v40 offset:0\n"
            "ds_write_addtid_b32 v41 offset:256\n"
            "ds_write_addtid_b32 v42 offset:512\n"
            "ds_write_addtid_b32 v43 offset:768\n"
            "ds_write_addtid_b32 v44 offset:16384\n"
            "ds_write_addtid_b32 v45 offset:16640\n"
            "ds_write_addtid_b32 v46 offset:16896\n"
            "ds_write_addtid_b32 v47 offset:17152\n"
            "ds_write_addtid_b32 v48 offset:32768\n"
            "ds_write_addtid_b32 v49 offset:33024\n"
            "ds_write_addtid_b32 v50 offset:33280\n"
            "ds_write_addtid_b32 v51 offset:33536\n"
            "ds_write_addtid_b32 v52 offset:49152\n"
            "ds_write_addtid_b32 v53 offset:49408\n"
            "ds_write_addtid_b32 v54 offset:49664\n"
            "ds_write_addtid_b32 v55 offset:49920\n"
            "s_add_u32 m0, %[wb], 16384\n"
            "s_nop 0\n"
            "ds_write_addtid_b32 v56 offset:49152\n"
            "ds_write_addtid_b32 v57 offset:49408\n"
            "ds_write_addtid_b32 v58 offset:49664\n"
            "ds_write_addtid_b32 v59 offset:49920\n"
            "s_mov_b32 s92, 0\n"
            "s_and_b32 s75, s76, 255\n"
            "s_movk_i32 s74, 1000\n"
            "s_sub_i32 s79, s75, %[j0]\n"
            "s_max_i32 s79, s79, 8\n"
            "s_cmp_eq_u32 s92, 7\n s_cselect_b32 s79, 8, s79\n"
            "s_setprio 3\n"
            "s_waitcnt lgkmcnt(0)\n"
            "s_barrier\n"
            "s_waitcnt vmcnt(7)\n"
            "v_and_b32 v104, 0x1fffc, v8\n"
            "v_and_b32 v105, 0x1fffc, v9\n"
            "v_and_b32 v106, 0x1fffc, v10\n"
            "ds_read_b32 v93, v104\n"
            "ds_read_b32 v95, v105\n"
            "ds_read_b32 v97, v106\n"
            "s_waitcnt lgkmcnt(0)\n"
            "Ls0_%=:\n"
            "s_cmp_gt_i32 s75, s74\n"
            "s_cbranch_scc1 1f\n"
            "s_waitcnt vmcnt(6)\n"
            "1:\n"
            "s_waitcnt vmcnt(11)\n"
            "s_cmp_eq_u32 s75, s79\n"
            "s_cbranch_scc1 Le0_%=\n"
            "Lc0_%=:\n"
            "v_and_b32 v104, 0x1fffc, v12\n"
            "v_and_b32 v105, 0x1fffc, v13\n"
            "v_and_b32 v106, 0x1fffc, v14\n"
            "ds_read_b32 v99, v104\n"
            "ds_read_b32 v101, v105\n"
            "ds_read_b32 v103, v106\n"
            "v_add_u32_dpp v111, v81, v82 row_newbcast:0 row_mask:0xf bank_mask:0x3\n"
            "v_add_u32_dpp v111, v81, v82 row_newbcast:8 row_mask:0xf bank_mask:0xc\n"
            "v_and_b32 v108, 0x7fe, v11\n"
            "v_and_b32 v109, 0x1ff800, v11\n"
            "v_lshrrev_b32 v110, 22, v11\n"
            "v_lshrrev_b32 v112, 15, v8\n"
            "v_lshrrev_b32 v113, 15, v9\n"
            "v_lshrrev_b32 v114, 15, v10\n"
            "v_and_b32 v111, " IDXMASK ", v111\n"
            "v_min_u32 v111, v111, v83\n"
            "buffer_load_dwordx4 v[8:11], v111, %[rs], 0 idxen " RAUX "\n"
            "v_fmamk_f32 v108, v108, 0x7e800000, v84\n"
            "v_fmamk_f32 v109, v109, 0x79800000, v84\n"
            "v_fmamk_f32 v110, v110, 0x7f000000, v84\n"
            "v_or_b32 v112, 3, v112\n"
            "v_or_b32 v113, 3, v113\n"
            "v_or_b32 v114, 3, v114\n"
            "s_sub_u32 s75, s75, 1\n"
            "s_waitcnt lgkmcnt(6)\n"
            "v_lshlrev_b32 v92, 16, v93\n"
            "v_lshlrev_b32 v94, 16, v95\n"
            "v_lshlrev_b32 v96, 16, v97\n"
            "v_pk_fma_f32 v[92:93], v[92:93], v[108:109], v[86:87] op_sel:[0,0,0] op_sel_hi:[1,0,1]\n"
            "v_pk_fma_f32 v[94:95], v[94:95], v[108:109], v[86:87] op_sel:[0,1,0] op_sel_hi:[1,1,1]\n"
            "v_pk_fma_f32 v[96:97], v[96:97], v[110:111], v[86:87] op_sel:[0,0,0] op_sel_hi:[1,0,1]\n"
            "v_lshl_add_u32 v92, v93, 16, v92\n"
            "v_lshl_add_u32 v94, v95, 16, v94\n"
            "v_lshl_add_u32 v96, v97, 16, v96\n"
            "v_sub_u32 v92, v92, v86\n"
            "v_mad_i32_i16 v88, v93, v112, v88\n"
            "ds_add_u32 v112, v92 offset:65533\n"
            "v_sub_u32 v94, v94, v86\n"
            "v_mad_i32_i16 v88, v95, v113, v88\n"
            "ds_add_u32 v113, v94 offset:65533\n"
            "v_sub_u32 v96, v96, v86\n"
            "v_mad_i32_i16 v88, v97, v114, v88\n"
            "ds_add_u32 v114, v96 offset:65533\n"
            "Ls1_%=:\n"
            "s_cmp_gt_i32 s75, s74\n"
            "s_cbranch_scc1 1f\n"
            "s_waitcnt vmcnt(6)\n"
            "1:\n"
            "s_waitcnt vmcnt(11)\n"
            "s_cmp_eq_u32 s75, s79\n"
            "s_cbranch_scc1 Le1_%=\n"
            "Lc1_%=:\n"
            "v_and_b32 v104, 0x1fffc, v16\n"
            "v_and_b32 v105, 0x1fffc, v17\n"
            "v_and_b32 v106, 0x1fffc, v18\n"
            "ds_read_b32 v93, v104\n"
            "ds_read_b32 v95, v105\n"
            "ds_read_b32 v97, v106\n"
            "v_add_u32_dpp v111, v81, v82 row_newbcast:1 row_mask:0xf bank_mask:0x3\n"
            "v_add_u32_dpp v111, v81, v82 row_newbcast:9 row_mask:0xf bank_mask:0xc\n"
            "v_and_b32 v108, 0x7fe, v15\n"
            "v_and_b32 v109, 0x1ff800, v15\n"
            "v_lshrrev_b32 v110, 22, v15\n"
            "v_lshrrev_b32 v112, 15, v12\n"
            "v_lshrrev_b32 v113, 15, v13\n"
            "v_lshrrev_b32 v114, 15, v14\n"
            "v_and_b32 v111, " IDXMASK ", v111\n"
            "v_min_u32 v111, v111, v83\n"
            "buffer_load_dwordx4 v[12:15], v111, %[rs], 0 idxen " RAUX "\n"
            "v_fmamk_f32 v108, v108, 0x7e800000, v84\n"
            "v_fmamk_f32 v109, v109, 0x79800000, v84\n"
            "v_fmamk_f32 v110, v110, 0x7f000000, v84\n"
            "v_or_b32 v112, 3, v112\n"
            "v_or_b32 v113, 3, v113\n"
            "v_or_b32 v114, 3, v114\n"
            "s_sub_u32 s75, s75, 1\n"
            "s_waitcnt lgkmcnt(6)\n"
            "v_lshlrev_b32 v98, 16, v99\n"
            "v_lshlrev_b32 v100, 16, v101\n"
            "v_lshlrev_b32 v102, 16, v103\n"
            "v_pk_fma_f32 v[98:99], v[98:99], v[108:109], v[86:87] op_sel:[0,0,0] op_sel_hi:[1,0,1]\n"
            "v_pk_fma_f32 v[100:101], v[100:101], v[108:109], v[86:87] op_sel:[0,1,0] op_sel_hi:[1,1,1]\n"
            "v_pk_fma_f32 v[102:103], v[102:103], v[110:111], v[86:87] op_sel:[0,0,0] op_sel_hi:[1,0,1]\n"
            "v_lshl_add_u32 v98, v99, 16, v98\n"
            "v_lshl_add_u32 v100, v101, 16, v100\n"
            "v_lshl_add_u32 v102, v103, 16, v102\n"
            "v_sub_u32 v98, v98, v86\n"
            "v_mad_i32_i16 v88, v99, v112, v88\n"
            "ds_add_u32 v112, v98 offset:65533\n"
            "v_sub_u32 v100, v100, v86\n"
            "v_mad_i32_i16 v88, v101, v113, v88\n"
            "ds_add_u32 v113, v100 offset:65533\n"
            "v_sub_u32 v102, v102, v86\n"
            "v_mad_i32_i16 v88, v103, v114, v88\n"
            "ds_add_u32 v114, v102 offset:65533\n"
            "Ls2_%=:\n"
            "s_cmp_gt_i32 s75, s74\n"
            "s_cbranch_scc1 1f\n"
            "s_waitcnt vmcnt(6)\n"
            "1:\n"
            "s_waitcnt vmcnt(11)\n"
            "s_cmp_eq_u32 s75, s79\n"
            "s_cbranch_scc1 Le2_%=\n"
            "Lc2_%=:\n"
            "v_and_b32 v104, 0x1fffc, v20\n"
            "v_and_b32 v105, 0x1fffc, v21\n"
            "v_and_b32 v106, 0x1fffc, v22\n"
            "ds_read_b32 v99, v104\n"
            "ds_read_b32 v101, v105\n"
            "ds_read_b32 v103, v106\n"
            "v_add_u32_dpp v111, v81, v82 row_newbcast:2 row_mask:0xf bank_mask:0x3\n"
            "v_add_u32_dpp v111, v81, v82 row_newbcast:10 row_mask:0xf bank_mask:0xc\n"
            "v_and_b32 v108, 0x7fe, v19\n"
            "v_and_b32 v109, 0x1ff800, v19\n"
            "v_lshrrev_b32 v110, 22, v19\n"
            "v_lshrrev_b32 v112, 15, v16\n"
            "v_lshrrev_b32 v113, 15, v17\n"
            "v_lshrrev_b32 v114, 15, v18\n"
            "v_and_b32 v111, " IDXMASK ", v111\n"
            "v_min_u32 v111, v111, v83\n"
            "buffer_load_dwordx4 v[16:19], v111, %[rs], 0 idxen " RAUX "\n"
            "v_fmamk_f32 v108, v108, 0x7e800000, v84\n"
            "v_fmamk_f32 v109, v109, 0x79800000, v84\n"
            "v_fmamk_f32 v110, v110, 0x7f000000, v84\n"
            "v_or_b32 v112, 3, v112\n"
            "v_or_b32 v113, 3, v113\n"
            "v_or_b32 v114, 3, v114\n"
            "s_sub_u32 s75, s75, 1\n"
            "s_waitcnt lgkmcnt(6)\n"
            "v_lshlrev_b32 v92, 16, v93\n"
            "v_lshlrev_b32 v94, 16, v95\n"
            "v_lshlrev_b32 v96, 16, v97\n"
            "v_pk_fma_f32 v[92:93], v[92:93], v[108:109], v[86:87] op_sel:[0,0,0] op_sel_hi:[1,0,1]\n"
            "v_pk_fma_f32 v[94:95], v[94:95], v[108:109], v[86:87] op_sel:[0,1,0] op_sel_hi:[1,1,1]\n"
            "v_pk_fma_f32 v[96:97], v[96:97], v[110:111], v[86:87] op_sel:[0,0,0] op_sel_hi:[1,0,1]\n"
            "v_lshl_add_u32 v92, v93, 16, v92\n"
            "v_lshl_add_u32 v94, v95, 16, v94\n"
            "v_lshl_add_u32 v96, v97, 16, v96\n"
            "v_sub_u32 v92, v92, v86\n"
            "v_mad_i32_i16 v88, v93, v112, v88\n"
            "ds_add_u32 v112, v92 offset:65533\n"
            "v_sub_u32 v94, v94, v86\n"
            "v_mad_i32_i16 v88, v95, v113, v88\n"
            "ds_add_u32 v113, v94 offset:65533\n"
            "v_sub_u32 v96, v96, v86\n"
            "v_mad_i32_i16 v88, v97, v114, v88\n"
            "ds_add_u32 v114, v96 offset:65533\n"
            "Ls3_%=:\n"
            "s_cmp_gt_i32 s75, s74\n"
            "s_cbranch_scc1 1f\n"
            "s_waitcnt vmcnt(6)\n"
            "1:\n"
            "s_waitcnt vmcnt(11)\n"
            "s_cmp_eq_u32 s75, s79\n"
            "s_cbranch_scc1 Le3_%=\n"
            "Lc3_%=:\n"
            "v_and_b32 v104, 0x1fffc, v24\n"
            "v_and_b32 v105, 0x1fffc, v25\n"
            "v_and_b32 v106, 0x1fffc, v26\n"
            "ds_read_b32 v93, v104\n"
            "ds_read_b32 v95, v105\n"
            "ds_read_b32 v97, v106\n"
            "v_add_u32_dpp v111, v81, v82 row_newbcast:3 row_mask:0xf bank_mask:0x3\n"
            "v_add_u32_dpp v111, v81, v82 row_newbcast:11 row_mask:0xf bank_mask:0xc\n"
            "v_and_b32 v108, 0x7fe, v23\n"
            "v_and_b32 v109, 0x1ff800, v23\n"
            "v_lshrrev_b32 v110, 22, v23\n"
            "v_lshrrev_b32 v112, 15, v20\n"
            "v_lshrrev_b32 v113, 15, v21\n"
            "v_lshrrev_b32 v114, 15, v22\n"
            "v_and_b32 v111, " IDXMASK ", v111\n"
            "v_min_u32 v111, v111, v83\n"
            "buffer_load_dwordx4 v[20:23], v111, %[rs], 0 idxen " RAUX "\n"
            "v_fmamk_f32 v108, v108, 0x7e800000, v84\n"
            "v_fmamk_f32 v109, v109, 0x79800000, v84\n"
            "v_fmamk_f32 v110, v110, 0x7f000000, v84\n"
            "v_or_b32 v112, 3, v112\n"
            "v_or_b32 v113, 3, v113\n"
            "v_or_b32 v114, 3, v114\n"
            "s_sub_u32 s75, s75, 1\n"
            "s_waitcnt lgkmcnt(6)\n"
            "v_lshlrev_b32 v98, 16, v99\n"
            "v_lshlrev_b32 v100, 16, v101\n"
            "v_lshlrev_b32 v102, 16, v103\n"
            "v_pk_fma_f32 v[98:99], v[98:99], v[108:109], v[86:87] op_sel:[0,0,0] op_sel_hi:[1,0,1]\n"
            "v_pk_fma_f32 v[100:101], v[100:101], v[108:109], v[86:87] op_sel:[0,1,0] op_sel_hi:[1,1,1]\n"
            "v_pk_fma_f32 v[102:103], v[102:103], v[110:111], v[86:87] op_sel:[0,0,0] op_sel_hi:[1,0,1]\n"
            "v_lshl_add_u32 v98, v99, 16, v98\n"
            "v_lshl_add_u32 v100, v101, 16, v100\n"
            "v_lshl_add_u32 v102, v103, 16, v102\n"
            "v_sub_u32 v98, v98, v86\n"
            "v_mad_i32_i16 v88, v99, v112, v88\n"
            "ds_add_u32 v112, v98 offset:65533\n"
            "v_sub_u32 v100, v100, v86\n"
            "v_mad_i32_i16 v88, v101, v113, v88\n"
            "ds_add_u32 v113, v100 offset:65533\n"
            "v_sub_u32 v102, v102, v86\n"
            "v_mad_i32_i16 v88, v103, v114, v88\n"
            "ds_add_u32 v114, v102 offset:65533\n"
            "Ls4_%=:\n"
            "s_cmp_gt_i32 s75, s74\n"
            "s_cbranch_scc1 1f\n"
            "s_waitcnt vmcnt(6)\n"
            "1:\n"
            "s_waitcnt vmcnt(11)\n"
            "s_cmp_eq_u32 s75, s79\n"
            "s_cbranch_scc1 Le4_%=\n"
            "Lc4_%=:\n"
            "v_and_b32 v104, 0x1fffc, v28\n"
            "v_and_b32 v105, 0x1fffc, v29\n"
            "v_and_b32 v106, 0x1fffc, v30\n"
            "ds_read_b32 v99, v104\n"
            "ds_read_b32 v101, v105\n"
            "ds_read_b32 v103, v106\n"
            "v_add_u32_dpp v111, v81, v82 row_newbcast:4 row_mask:0xf bank_mask:0x3\n"
            "v_add_u32_dpp v111, v81, v82 row_newbcast:12 row_mask:0xf bank_mask:0xc\n"
            "v_and_b32 v108, 0x7fe, v27\n"
            "v_and_b32 v109, 0x1ff800, v27\n"
            "v_lshrrev_b32 v110, 22, v27\n"
            "v_lshrrev_b32 v112, 15, v24\n"
            "v_lshrrev_b32 v113, 15, v25\n"
            "v_lshrrev_b32 v114, 15, v26\n"
            "v_and_b32 v111, " IDXMASK ", v111\n"
            "v_min_u32 v111, v111, v83\n"
            "buffer_load_dwordx4 v[24:27], v111, %[rs], 0 idxen " RAUX "\n"
            "v_fmamk_f32 v108, v108, 0x7e800000, v84\n"
            "v_fmamk_f32 v109, v109, 0x79800000, v84\n"
            "v_fmamk_f32 v110, v110, 0x7f000000, v84\n"
            "v_or_b32 v112, 3, v112\n"
            "v_or_b32 v113, 3, v113\n"
            "v_or_b32 v114, 3, v114\n"
            "s_sub_u32 s75, s75, 1\n"
            "s_waitcnt lgkmcnt(6)\n"
            "v_lshlrev_b32 v92, 16, v93\n"
            "v_lshlrev_b32 v94, 16, v95\n"
            "v_lshlrev_b32 v96, 16, v97\n"
            "v_pk_fma_f32 v[92:93], v[92:93], v[108:109], v[86:87] op_sel:[0,0,0] op_sel_hi:[1,0,1]\n"
            "v_pk_fma_f32 v[94:95], v[94:95], v[108:109], v[86:87] op_sel:[0,1,0] op_sel_hi:[1,1,1]\n"
            "v_pk_fma_f32 v[96:97], v[96:97], v[110:111], v[86:87] op_sel:[0,0,0] op_sel_hi:[1,0,1]\n"
            "v_lshl_add_u32 v92, v93, 16, v92\n"
            "v_lshl_add_u32 v94, v95, 16, v94\n"
            "v_lshl_add_u32 v96, v97, 16, v96\n"
            "v_sub_u32 v92, v92, v86\n"
            "v_mad_i32_i16 v88, v93, v112, v88\n"
            "ds_add_u32 v112, v92 offset:65533\n"
            "v_sub_u32 v94, v94, v86\n"
            "v_mad_i32_i16 v88, v95, v113, v88\n"
            "ds_add_u32 v113, v94 offset:65533\n"
            "v_sub_u32 v96, v96, v86\n"
            "v_mad_i32_i16 v88, v97, v114, v88\n"
            "ds_add_u32 v114, v96 offset:65533\n"
            "Ls5_%=:\n"
            "s_cmp_gt_i32 s75, s74\n"
            "s_cbranch_scc1 1f\n"
            "s_waitcnt vmcnt(6)\n"
            "1:\n"
            "s_waitcnt vmcnt(11)\n"
            "s_cmp_eq_u32 s75, s79\n"
            "s_cbranch_scc1 Le5_%=\n"
            "Lc5_%=:\n"
            "v_and_b32 v104, 0x1fffc, v32\n"
            "v_and_b32 v105, 0x1fffc, v33\n"
            "v_and_b32 v106, 0x1fffc, v34\n"
            "ds_read_b32 v93, v104\n"
            "ds_read_b32 v95, v105\n"
            "ds_read_b32 v97, v106\n"
            "v_add_u32_dpp v111, v81, v82 row_newbcast:5 row_mask:0xf bank_mask:0x3\n"
            "v_add_u32_dpp v111, v81, v82 row_newbcast:13 row_mask:0xf bank_mask:0xc\n"
            "v_and_b32 v108, 0x7fe, v31\n"
            "v_and_b32 v109, 0x1ff800, v31\n"
            "v_lshrrev_b32 v110, 22, v31\n"
            "v_lshrrev_b32 v112, 15, v28\n"
            "v_lshrrev_b32 v113, 15, v29\n"
            "v_lshrrev_b32 v114, 15, v30\n"
            "v_and_b32 v111, " IDXMASK ", v111\n"
            "v_min_u32 v111, v111, v83\n"
            "buffer_load_dwordx4 v[28:31], v111, %[rs], 0 idxen " RAUX "\n"
            "v_fmamk_f32 v108, v108, 0x7e800000, v84\n"
            "v_fmamk_f32 v109, v109, 0x79800000, v84\n"
            "v_fmamk_f32 v110, v110, 0x7f000000, v84\n"
            "v_or_b32 v112, 3, v112\n"
            "v_or_b32 v113, 3, v113\n"
            "v_or_b32 v114, 3, v114\n"
            "s_sub_u32 s75, s75, 1\n"
            "s_waitcnt lgkmcnt(6)\n"
            "v_lshlrev_b32 v98, 16, v99\n"
            "v_lshlrev_b32 v100, 16, v101\n"
            "v_lshlrev_b32 v102, 16, v103\n"
            "v_pk_fma_f32 v[98:99], v[98:99], v[108:109], v[86:87] op_sel:[0,0,0] op_sel_hi:[1,0,1]\n"
            "v_pk_fma_f32 v[100:101], v[100:101], v[108:109], v[86:87] op_sel:[0,1,0] op_sel_hi:[1,1,1]\n"
            "v_pk_fma_f32 v[102:103], v[102:103], v[110:111], v[86:87] op_sel:[0,0,0] op_sel_hi:[1,0,1]\n"
            "v_lshl_add_u32 v98, v99, 16, v98\n"
            "v_lshl_add_u32 v100, v101, 16, v100\n"
            "v_lshl_add_u32 v102, v103, 16, v102\n"
            "v_sub_u32 v98, v98, v86\n"
            "v_mad_i32_i16 v88, v99, v112, v88\n"
            "ds_add_u32 v112, v98 offset:65533\n"
            "v_sub_u32 v100, v100, v86\n"
            "v_mad_i32_i16 v88, v101, v113, v88\n"
            "ds_add_u32 v113, v100 offset:65533\n"
            "v_sub_u32 v102, v102, v86\n"
            "v_mad_i32_i16 v88, v103, v114, v88\n"
            "ds_add_u32 v114, v102 offset:65533\n"
            "Ls6_%=:\n"
            "s_cmp_gt_i32 s75, s74\n"
            "s_cbranch_scc1 1f\n"
            "s_waitcnt vmcnt(6)\n"
            "1:\n"
            "s_waitcnt vmcnt(11)\n"
            "s_cmp_eq_u32 s75, s79\n"
            "s_cbranch_scc1 Le6_%=\n"
            "Lc6_%=:\n"
            "v_and_b32 v104, 0x1fffc, v36\n"
            "v_and_b32 v105, 0x1fffc, v37\n"
            "v_and_b32 v106, 0x1fffc, v38\n"
            "ds_read_b32 v99, v104\n"
            "ds_read_b32 v101, v105\n"
            "ds_read_b32 v103, v106\n"
            "v_add_u32_dpp v111, v81, v82 row_newbcast:6 row_mask:0xf bank_mask:0x3\n"
            "v_add_u32_dpp v111, v81, v82 row_newbcast:14 row_mask:0xf bank_mask:0xc\n"
            "v_and_b32 v108, 0x7fe, v35\n"
            "v_and_b32 v109, 0x1ff800, v35\n"
            "v_lshrrev_b32 v110, 22, v35\n"
            "v_lshrrev_b32 v112, 15, v32\n"
            "v_lshrrev_b32 v113, 15, v33\n"
            "v_lshrrev_b32 v114, 15, v34\n"
            "v_and_b32 v111, " IDXMASK ", v111\n"
            "v_min_u32 v111, v111, v83\n"
            "buffer_load_dwordx4 v[32:35], v111, %[rs], 0 idxen " RAUX "\n"
            "v_fmamk_f32 v108, v108, 0x7e800000, v84\n"
            "v_fmamk_f32 v109, v109, 0x79800000, v84\n"
            "v_fmamk_f32 v110, v110, 0x7f000000, v84\n"
            "v_or_b32 v112, 3, v112\n"
            "v_or_b32 v113, 3, v113\n"
            "v_or_b32 v114, 3, v114\n"
            "s_sub_u32 s75, s75, 1\n"
            "s_waitcnt lgkmcnt(6)\n"
            "v_lshlrev_b32 v92, 16, v93\n"
            "v_lshlrev_b32 v94, 16, v95\n"
            "v_lshlrev_b32 v96, 16, v97\n"
            "v_pk_fma_f32 v[92:93], v[92:93], v[108:109], v[86:87] op_sel:[0,0,0] op_sel_hi:[1,0,1]\n"
            "v_pk_fma_f32 v[94:95], v[94:95], v[108:109], v[86:87] op_sel:[0,1,0] op_sel_hi:[1,1,1]\n"
            "v_pk_fma_f32 v[96:97], v[96:97], v[110:111], v[86:87] op_sel:[0,0,0] op_sel_hi:[1,0,1]\n"
            "v_lshl_add_u32 v92, v93, 16, v92\n"
            "v_lshl_add_u32 v94, v95, 16, v94\n"
            "v_lshl_add_u32 v96, v97, 16, v96\n"
            "v_sub_u32 v92, v92, v86\n"
            "v_mad_i32_i16 v88, v93, v112, v88\n"
            "ds_add_u32 v112, v92 offset:65533\n"
            "v_sub_u32 v94, v94, v86\n"
            "v_mad_i32_i16 v88, v95, v113, v88\n"
            "ds_add_u32 v113, v94 offset:65533\n"
            "v_sub_u32 v96, v96, v86\n"
            "v_mad_i32_i16 v88, v97, v114, v88\n"
            "ds_add_u32 v114, v96 offset:65533\n"
            "Ls7_%=:\n"
            "s_cmp_gt_i32 s75, s74\n"
            "s_cbranch_scc1 1f\n"
            "s_waitcnt vmcnt(6)\n"
            "1:\n"
            "s_waitcnt vmcnt(11)\n"
            "s_cmp_eq_u32 s75, s79\n"
            "s_cbranch_scc1 Le7_%=\n"
            "Lc7_%=:\n"
            "v_and_b32 v104, 0x1fffc, v8\n"
            "v_and_b32 v105, 0x1fffc, v9\n"
            "v_and_b32 v106, 0x1fffc, v10\n"
            "ds_read_b32 v93, v104\n"
            "ds_read_b32 v95, v105\n"
            "ds_read_b32 v97, v106\n"
            "v_add_u32_dpp v111, v81, v82 row_newbcast:7 row_mask:0xf bank_mask:0x3\n"
            "v_add_u32_dpp v111, v81, v82 row_newbcast:15 row_mask:0xf bank_mask:0xc\n"
            "v_and_b32 v108, 0x7fe, v39\n"
            "v_and_b32 v109, 0x1ff800, v39\n"
            "v_lshrrev_b32 v110, 22, v39\n"
            "v_lshrrev_b32 v112, 15, v36\n"
            "v_lshrrev_b32 v113, 15, v37\n"
            "v_lshrrev_b32 v114, 15, v38\n"
            "v_and_b32 v111, " IDXMASK ", v111\n"
            "v_min_u32 v111, v111, v83\n"
            "buffer_load_dwordx4 v[36:39], v111, %[rs], 0 idxen " RAUX "\n"
            "s_min_u32 s82, s82, 19\n"
            "s_add_u32 s82, s82, 1\n"
            "s_set_gpr_idx_on s82, gpr_idx(SRC0)\n"
            "s_nop 0\n"
            "v_mov_b32 v81, v60\n"
            "s_set_gpr_idx_off\n"
            "v_fmamk_f32 v108, v108, 0x7e800000, v84\n"
            "v_fmamk_f32 v109, v109, 0x79800000, v84\n"
            "v_fmamk_f32 v110, v110, 0x7f000000, v84\n"
            "v_or_b32 v112, 3, v112\n"
            "v_or_b32 v113, 3, v113\n"
            "v_or_b32 v114, 3, v114\n"
            "s_sub_u32 s75, s75, 1\n"
            "s_waitcnt lgkmcnt(6)\n"
            "v_lshlrev_b32 v98, 16, v99\n"
            "v_lshlrev_b32 v100, 16, v101\n"
            "v_lshlrev_b32 v102, 16, v103\n"
            "v_pk_fma_f32 v[98:99], v[98:99], v[108:109], v[86:87] op_sel:[0,0,0] op_sel_hi:[1,0,1]\n"
            "v_pk_fma_f32 v[100:101], v[100:101], v[108:109], v[86:87] op_sel:[0,1,0] op_sel_hi:[1,1,1]\n"
            "v_pk_fma_f32 v[102:103], v[102:103], v[110:111], v[86:87] op_sel:[0,0,0] op_sel_hi:[1,0,1]\n"
            "v_lshl_add_u32 v98, v99, 16, v98\n"
            "v_lshl_add_u32 v100, v101, 16, v100\n"
            "v_lshl_add_u32 v102, v103, 16, v102\n"
            "v_sub_u32 v98, v98, v86\n"
            "v_mad_i32_i16 v88, v99, v112, v88\n"
            "ds_add_u32 v112, v98 offset:65533\n"
            "v_sub_u32 v100, v100, v86\n"
            "v_mad_i32_i16 v88, v101, v113, v88\n"
            "ds_add_u32 v113, v100 offset:65533\n"
            "v_sub_u32 v102, v102, v86\n"
            "v_mad_i32_i16 v88, v103, v114, v88\n"
            "ds_add_u32 v114, v102 offset:65533\n"
            "s_branch Ls0_%=\n"
            "Le0_%=:\n"
            "s_cmp_lt_u32 s75, 8\n"
            "s_cbranch_scc1 1f\n"
            "s_swappc_b64 s[80:81], s[66:67]\n"
            "s_branch Lc0_%=\n"
            "1:\n"
            "s_cmp_lg_u32 s75, 7\n"
            "s_cbranch_scc1 1f\n"
            "s_setprio 2\n"
            "s_mov_b32 s79, 3\n"
            "s_branch Lc0_%=\n"
            "1:\n"
            "s_cmp_lg_u32 s75, 3\n"
            "s_cbranch_scc1 1f\n"
            "s_setprio 1\n"
            "s_mov_b32 s79, 1\n"
            "s_branch Lc0_%=\n"
            "1:\n"
            "s_setprio 0\n"
            "v_add_u32_dpp v111, v81, v82 row_newbcast:0 row_mask:0xf bank_mask:0x3\n"
            "v_add_u32_dpp v111, v81, v82 row_newbcast:8 row_mask:0xf bank_mask:0xc\n"
            "v_and_b32 v108, 0x7fe, v11\n"
            "v_and_b32 v109, 0x1ff800, v11\n"
            "v_lshrrev_b32 v110, 22, v11\n"
            "v_lshrrev_b32 v112, 15, v8\n"
            "v_lshrrev_b32 v113, 15, v9\n"
            "v_lshrrev_b32 v114, 15, v10\n"
            "v_and_b32 v111, " IDXMASK ", v111\n"
            "v_min_u32 v111, v111, v83\n"
            "buffer_load_dwordx4 v[8:11], v111, %[rs], 0 idxen " RAUX "\n"
            "v_fmamk_f32 v108, v108, 0x7e800000, v84\n"
            "v_fmamk_f32 v109, v109, 0x79800000, v84\n"
            "v_fmamk_f32 v110, v110, 0x7f000000, v84\n"
            "v_or_b32 v112, 3, v112\n"
            "v_or_b32 v113, 3, v113\n"
            "v_or_b32 v114, 3, v114\n"
            "s_waitcnt lgkmcnt(3)\n"
            "s_swappc_b64 s[80:81], s[70:71]\n"
            "v_lshlrev_b32 v92, 16, v93\n"
            "v_lshlrev_b32 v94, 16, v95\n"
            "v_lshlrev_b32 v96, 16, v97\n"
            "v_pk_fma_f32 v[92:93], v[92:93], v[108:109], v[86:87] op_sel:[0,0,0] op_sel_hi:[1,0,1]\n"
            "v_pk_fma_f32 v[94:95], v[94:95], v[108:109], v[86:87] op_sel:[0,1,0] op_sel_hi:[1,1,1]\n"
            "v_pk_fma_f32 v[96:97], v[96:97], v[110:111], v[86:87] op_sel:[0,0,0] op_sel_hi:[1,0,1]\n"
            "v_lshl_add_u32 v92, v93, 16, v92\n"
            "v_lshl_add_u32 v94, v95, 16, v94\n"
            "v_lshl_add_u32 v96, v97, 16, v96\n"
            "v_sub_u32 v92, v92, v86\n"
            "v_mad_i32_i16 v88, v93, v112, v88\n"
            "ds_add_u32 v112, v92 offset:65533\n"
            "v_sub_u32 v94, v94, v86\n"
            "v_mad_i32_i16 v88, v95, v113, v88\n"
            "ds_add_u32 v113, v94 offset:65533\n"
            "v_sub_u32 v96, v96, v86\n"
            "v_mad_i32_i16 v88, v97, v114, v88\n"
            "ds_add_u32 v114, v96 offset:65533\n"
            "v_and_b32 v104, 0x1fffc, v12\n"
            "v_and_b32 v105, 0x1fffc, v13\n"
            "v_and_b32 v106, 0x1fffc, v14\n"
            "s_swappc_b64 s[80:81], s[72:73]\n"
            "ds_read_b32 v99, v104\n"
            "ds_read_b32 v101, v105\n"
            "ds_read_b32 v103, v106\n"
            "s_waitcnt lgkmcnt(0)\n"
            "s_branch Ls1_%=\n"
            "Le1_%=:\n"
            "s_cmp_lt_u32 s75, 8\n"
            "s_cbranch_scc1 1f\n"
            "s_swappc_b64 s[80:81], s[66:67]\n"
            "s_branch Lc1_%=\n"
            "1:\n"
            "s_cmp_lg_u32 s75, 7\n"
            "s_cbranch_scc1 1f\n"
            "s_setprio 2\n"
            "s_mov_b32 s79, 3\n"
            "s_branch Lc1_%=\n"
            "1:\n"
            "s_cmp_lg_u32 s75, 3\n"
            "s_cbranch_scc1 1f\n"
            "s_setprio 1\n"
            "s_mov_b32 s79, 1\n"
            "s_branch Lc1_%=\n"
            "1:\n"
            "s_setprio 0\n"
            "v_add_u32_dpp v111, v81, v82 row_newbcast:1 row_mask:0xf bank_mask:0x3\n"
            "v_add_u32_dpp v111, v81, v82 row_newbcast:9 row_mask:0xf bank_mask:0xc\n"
            "v_and_b32 v108, 0x7fe, v15\n"
            "v_and_b32 v109, 0x1ff800, v15\n"
            "v_lshrrev_b32 v110, 22, v15\n"
            "v_lshrrev_b32 v112, 15, v12\n"
            "v_lshrrev_b32 v113, 15, v13\n"
            "v_lshrrev_b32 v114, 15, v14\n"
            "v_and_b32 v111, " IDXMASK ", v111\n"
            "v_min_u32 v111, v111, v83\n"
            "buffer_load_dwordx4 v[12:15], v111, %[rs], 0 idxen " RAUX "\n"
            "v_fmamk_f32 v108, v108, 0x7e800000, v84\n"
            "v_fmamk_f32 v109, v109, 0x79800000, v84\n"
            "v_fmamk_f32 v110, v110, 0x7f000000, v84\n"
            "v_or_b32 v112, 3, v112\n"
            "v_or_b32 v113, 3, v113\n"
            "v_or_b32 v114, 3, v114\n"
            "s_waitcnt lgkmcnt(3)\n"
            "s_swappc_b64 s[80:81], s[70:71]\n"
            "v_lshlrev_b32 v98, 16, v99\n"
            "v_lshlrev_b32 v100, 16, v101\n"
            "v_lshlrev_b32 v102, 16, v103\n"
            "v_pk_fma_f32 v[98:99], v[98:99], v[108:109], v[86:87] op_sel:[0,0,0] op_sel_hi:[1,0,1]\n"
            "v_pk_fma_f32 v[100:101], v[100:101], v[108:109], v[86:87] op_sel:[0,1,0] op_sel_hi:[1,1,1]\n"
            "v_pk_fma_f32 v[102:103], v[102:103], v[110:111], v[86:87] op_sel:[0,0,0] op_sel_hi:[1,0,1]\n"
            "v_lshl_add_u32 v98, v99, 16, v98\n"
            "v_lshl_add_u32 v100, v101, 16, v100\n"
            "v_lshl_add_u32 v102, v103, 16, v102\n"
            "v_sub_u32 v98, v98, v86\n"
            "v_mad_i32_i16 v88, v99, v112, v88\n"
            "ds_add_u32 v112, v98 offset:65533\n"
            "v_sub_u32 v100, v100, v86\n"
            "v_mad_i32_i16 v88, v101, v113, v88\n"
            "ds_add_u32 v113, v100 offset:65533\n"
            "v_sub_u32 v102, v102, v86\n"
            "v_mad_i32_i16 v88, v103, v114, v88\n"
            "ds_add_u32 v114, v102 offset:65533\n"
            "v_and_b32 v104, 0x1fffc, v16\n"
            "v_and_b32 v105, 0x1fffc, v17\n"
            "v_and_b32 v106, 0x1fffc, v18\n"
            "s_swappc_b64 s[80:81], s[72:73]\n"
            "ds_read_b32 v93, v104\n"
            "ds_read_b32 v95, v105\n"
            "ds_read_b32 v97, v106\n"
            "s_waitcnt lgkmcnt(0)\n"
            "s_branch Ls2_%=\n"
            "Le2_%=:\n"
            "s_cmp_lt_u32 s75, 8\n"
            "s_cbranch_scc1 1f\n"
            "s_swappc_b64 s[80:81], s[66:67]\n"
            "s_branch Lc2_%=\n"
            "1:\n"
            "s_cmp_lg_u32 s75, 7\n"
            "s_cbranch_scc1 1f\n"
            "s_setprio 2\n"
            "s_mov_b32 s79, 3\n"
            "s_branch Lc2_%=\n"
            "1:\n"
            "s_cmp_lg_u32 s75, 3\n"
            "s_cbranch_scc1 1f\n"
            "s_setprio 1\n"
            "s_mov_b32 s79, 1\n"
            "s_branch Lc2_%=\n"
            "1:\n"
            "s_setprio 0\n"
            "v_add_u32_dpp v111, v81, v82 row_newbcast:2 row_mask:0xf bank_mask:0x3\n"
            "v_add_u32_dpp v111, v81, v82 row_newbcast:10 row_mask:0xf bank_mask:0xc\n"
            "v_and_b32 v108, 0x7fe, v19\n"
            "v_and_b32 v109, 0x1ff800, v19\n"
            "v_lshrrev_b32 v110, 22, v19\n"
            "v_lshrrev_b32 v112, 15, v16\n"
            "v_lshrrev_b32 v113, 15, v17\n"
            "v_lshrrev_b32 v114, 15, v18\n"
            "v_and_b32 v111, " IDXMASK ", v111\n"
            "v_min_u32 v111, v111, v83\n"
            "buffer_load_dwordx4 v[16:19], v111, %[rs], 0 idxen " RAUX "\n"
            "v_fmamk_f32 v108, v108, 0x7e800000, v84\n"
            "v_fmamk_f32 v109, v109, 0x79800000, v84\n"
            "v_fmamk_f32 v110, v110, 0x7f000000, v84\n"
            "v_or_b32 v112, 3, v112\n"
            "v_or_b32 v113, 3, v113\n"
            "v_or_b32 v114, 3, v114\n"
            "s_waitcnt lgkmcnt(3)\n"
            "s_swappc_b64 s[80:81], s[70:71]\n"
            "v_lshlrev_b32 v92, 16, v93\n"
            "v_lshlrev_b32 v94, 16, v95\n"
            "v_lshlrev_b32 v96, 16, v97\n"
            "v_pk_fma_f32 v[92:93], v[92:93], v[108:109], v[86:87] op_sel:[0,0,0] op_sel_hi:[1,0,1]\n"
            "v_pk_fma_f32 v[94:95], v[94:95], v[108:109], v[86:87] op_sel:[0,1,0] op_sel_hi:[1,1,1]\n"
            "v_pk_fma_f32 v[96:97], v[96:97], v[110:111], v[86:87] op_sel:[0,0,0] op_sel_hi:[1,0,1]\n"
            "v_lshl_add_u32 v92, v93, 16, v92\n"
            "v_lshl_add_u32 v94, v95, 16, v94\n"
            "v_lshl_add_u32 v96, v97, 16, v96\n"
            "v_sub_u32 v92, v92, v86\n"
            "v_mad_i32_i16 v88, v93, v112, v88\n"
            "ds_add_u32 v112, v92 offset:65533\n"
            "v_sub_u32 v94, v94, v86\n"
            "v_mad_i32_i16 v88, v95, v113, v88\n"
            "ds_add_u32 v113, v94 offset:65533\n"
            "v_sub_u32 v96, v96, v86\n"
            "v_mad_i32_i16 v88, v97, v114, v88\n"
            "ds_add_u32 v114, v96 offset:65533\n"
            "v_and_b32 v104, 0x1fffc, v20\n"
            "v_and_b32 v105, 0x1fffc, v21\n"
            "v_and_b32 v106, 0x1fffc, v22\n"
            "s_swappc_b64 s[80:81], s[72:73]\n"
            "ds_read_b32 v99, v104\n"
            "ds_read_b32 v101, v105\n"
            "ds_read_b32 v103, v106\n"
            "s_waitcnt lgkmcnt(0)\n"
            "s_branch Ls3_%=\n"
            "Le3_%=:\n"
            "s_cmp_lt_u32 s75, 8\n"
            "s_cbranch_scc1 1f\n"
            "s_swappc_b64 s[80:81], s[66:67]\n"
            "s_branch Lc3_%=\n"
            "1:\n"
            "s_cmp_lg_u32 s75, 7\n"
            "s_cbranch_scc1 1f\n"
            "s_setprio 2\n"
            "s_mov_b32 s79, 3\n"
            "s_branch Lc3_%=\n"
            "1:\n"
            "s_cmp_lg_u32 s75, 3\n"
            "s_cbranch_scc1 1f\n"
            "s_setprio 1\n"
            "s_mov_b32 s79, 1\n"
            "s_branch Lc3_%=\n"
            "1:\n"
            "s_setprio 0\n"
            "v_add_u32_dpp v111, v81, v82 row_newbcast:3 row_mask:0xf bank_mask:0x3\n"
            "v_add_u32_dpp v111, v81, v82 row_newbcast:11 row_mask:0xf bank_mask:0xc\n"
            "v_and_b32 v108, 0x7fe, v23\n"
            "v_and_b32 v109, 0x1ff800, v23\n"
            "v_lshrrev_b32 v110, 22, v23\n"
            "v_lshrrev_b32 v112, 15, v20\n"
            "v_lshrrev_b32 v113, 15, v21\n"
            "v_lshrrev_b32 v114, 15, v22\n"
            "v_and_b32 v111, " IDXMASK ", v111\n"
            "v_min_u32 v111, v111, v83\n"
            "buffer_load_dwordx4 v[20:23], v111, %[rs], 0 idxen " RAUX "\n"
            "v_fmamk_f32 v108, v108, 0x7e800000, v84\n"
            "v_fmamk_f32 v109, v109, 0x79800000, v84\n"
            "v_fmamk_f32 v110, v110, 0x7f000000, v84\n"
            "v_or_b32 v112, 3, v112\n"
            "v_or_b32 v113, 3, v113\n"
            "v_or_b32 v114, 3, v114\n"
            "s_waitcnt lgkmcnt(3)\n"
            "s_swappc_b64 s[80:81], s[70:71]\n"
            "v_lshlrev_b32 v98, 16, v99\n"
            "v_lshlrev_b32 v100, 16, v101\n"
            "v_lshlrev_b32 v102, 16, v103\n"
            "v_pk_fma_f32 v[98:99], v[98:99], v[108:109], v[86:87] op_sel:[0,0,0] op_sel_hi:[1,0,1]\n"
            "v_pk_fma_f32 v[100:101], v[100:101], v[108:109], v[86:87] op_sel:[0,1,0] op_sel_hi:[1,1,1]\n"
            "v_pk_fma_f32 v[102:103], v[102:103], v[110:111], v[86:87] op_sel:[0,0,0] op_sel_hi:[1,0,1]\n"
            "v_lshl_add_u32 v98, v99, 16, v98\n"
            "v_lshl_add_u32 v100, v101, 16, v100\n"
            "v_lshl_add_u32 v102, v103, 16, v102\n"
            "v_sub_u32 v98, v98, v86\n"
            "v_mad_i32_i16 v88, v99, v112, v88\n"
            "ds_add_u32 v112, v98 offset:65533\n"
            "v_sub_u32 v100, v100, v86\n"
            "v_mad_i32_i16 v88, v101, v113, v88\n"
            "ds_add_u32 v113, v100 offset:65533\n"
            "v_sub_u32 v102, v102, v86\n"
            "v_mad_i32_i16 v88, v103, v114, v88\n"
            "ds_add_u32 v114, v102 offset:65533\n"
            "v_and_b32 v104, 0x1fffc, v24\n"
            "v_and_b32 v105, 0x1fffc, v25\n"
            "v_and_b32 v106, 0x1fffc, v26\n"
            "s_swappc_b64 s[80:81], s[72:73]\n"
            "ds_read_b32 v93, v104\n"
            "ds_read_b32 v95, v105\n"
            "ds_read_b32 v97, v106\n"
            "s_waitcnt lgkmcnt(0)\n"
            "s_branch Ls4_%=\n"
            "Le4_%=:\n"
            "s_cmp_lt_u32 s75, 8\n"
            "s_cbranch_scc1 1f\n"
            "s_swappc_b64 s[80:81], s[66:67]\n"
            "s_branch Lc4_%=\n"
            "1:\n"
            "s_cmp_lg_u32 s75, 7\n"
            "s_cbranch_scc1 1f\n"
            "s_setprio 2\n"
            "s_mov_b32 s79, 3\n"
            "s_branch Lc4_%=\n"
            "1:\n"
            "s_cmp_lg_u32 s75, 3\n"
            "s_cbranch_scc1 1f\n"
            "s_setprio 1\n"
            "s_mov_b32 s79, 1\n"
            "s_branch Lc4_%=\n"
            "1:\n"
            "s_setprio 0\n"
            "v_add_u32_dpp v111, v81, v82 row_newbcast:4 row_mask:0xf bank_mask:0x3\n"
            "v_add_u32_dpp v111, v81, v82 row_newbcast:12 row_mask:0xf bank_mask:0xc\n"
            "v_and_b32 v108, 0x7fe, v27\n"
            "v_and_b32 v109, 0x1ff800, v27\n"
            "v_lshrrev_b32 v110, 22, v27\n"
            "v_lshrrev_b32 v112, 15, v24\n"
            "v_lshrrev_b32 v113, 15, v25\n"
            "v_lshrrev_b32 v114, 15, v26\n"
            "v_and_b32 v111, " IDXMASK ", v111\n"
            "v_min_u32 v111, v111, v83\n"
            "buffer_load_dwordx4 v[24:27], v111, %[rs], 0 idxen " RAUX "\n"
            "v_fmamk_f32 v108, v108, 0x7e800000, v84\n"
            "v_fmamk_f32 v109, v109, 0x79800000, v84\n"
            "v_fmamk_f32 v110, v110, 0x7f000000, v84\n"
            "v_or_b32 v112, 3, v112\n"
            "v_or_b32 v113, 3, v113\n"
            "v_or_b32 v114, 3, v114\n"
            "s_waitcnt lgkmcnt(3)\n"
            "s_swappc_b64 s[80:81], s[70:71]\n"
            "v_lshlrev_b32 v92, 16, v93\n"
            "v_lshlrev_b32 v94, 16, v95\n"
            "v_lshlrev_b32 v96, 16, v97\n"
            "v_pk_fma_f32 v[92:93], v[92:93], v[108:109], v[86:87] op_sel:[0,0,0] op_sel_hi:[1,0,1]\n"
            "v_pk_fma_f32 v[94:95], v[94:95], v[108:109], v[86:87] op_sel:[0,1,0] op_sel_hi:[1,1,1]\n"
            "v_pk_fma_f32 v[96:97], v[96:97], v[110:111], v[86:87] op_sel:[0,0,0] op_sel_hi:[1,0,1]\n"
            "v_lshl_add_u32 v92, v93, 16, v92\n"
            "v_lshl_add_u32 v94, v95, 16, v94\n"
            "v_lshl_add_u32 v96, v97, 16, v96\n"
            "v_sub_u32 v92, v92, v86\n"
            "v_mad_i32_i16 v88, v93, v112, v88\n"
            "ds_add_u32 v112, v92 offset:65533\n"
            "v_sub_u32 v94, v94, v86\n"
            "v_mad_i32_i16 v88, v95, v113, v88\n"
            "ds_add_u32 v113, v94 offset:65533\n"
            "v_sub_u32 v96, v96, v86\n"
            "v_mad_i32_i16 v88, v97, v114, v88\n"
            "ds_add_u32 v114, v96 offset:65533\n"
            "v_and_b32 v104, 0x1fffc, v28\n"
            "v_and_b32 v105, 0x1fffc, v29\n"
            "v_and_b32 v106, 0x1fffc, v30\n"
            "s_swappc_b64 s[80:81], s[72:73]\n"
            "ds_read_b32 v99, v104\n"
            "ds_read_b32 v101, v105\n"
            "ds_read_b32 v103, v106\n"
            "s_waitcnt lgkmcnt(0)\n"
            "s_branch Ls5_%=\n"
            "Le5_%=:\n"
            "s_cmp_lt_u32 s75, 8\n"
            "s_cbranch_scc1 1f\n"
            "s_swappc_b64 s[80:81], s[66:67]\n"
            "s_branch Lc5_%=\n"
            "1:\n"
            "s_cmp_lg_u32 s75, 7\n"
            "s_cbranch_scc1 1f\n"
            "s_setprio 2\n"
            "s_mov_b32 s79, 3\n"
            "s_branch Lc5_%=\n"
            "1:\n"
            "s_cmp_lg_u32 s75, 3\n"
            "s_cbranch_scc1 1f\n"
            "s_setprio 1\n"
            "s_mov_b32 s79, 1\n"
            "s_branch Lc5_%=\n"
            "1:\n"
            "s_setprio 0\n"
            "v_add_u32_dpp v111, v81, v82 row_newbcast:5 row_mask:0xf bank_mask:0x3\n"
            "v_add_u32_dpp v111, v81, v82 row_newbcast:13 row_mask:0xf bank_mask:0xc\n"
            "v_and_b32 v108, 0x7fe, v31\n"
            "v_and_b32 v109, 0x1ff800, v31\n"
            "v_lshrrev_b32 v110, 22, v31\n"
            "v_lshrrev_b32 v112, 15, v28\n"
            "v_lshrrev_b32 v113, 15, v29\n"
            "v_lshrrev_b32 v114, 15, v30\n"
            "v_and_b32 v111, " IDXMASK ", v111\n"
            "v_min_u32 v111, v111, v83\n"
            "buffer_load_dwordx4 v[28:31], v111, %[rs], 0 idxen " RAUX "\n"
            "v_fmamk_f32 v108, v108, 0x7e800000, v84\n"
            "v_fmamk_f32 v109, v109, 0x79800000, v84\n"
            "v_fmamk_f32 v110, v110, 0x7f000000, v84\n"
            "v_or_b32 v112, 3, v112\n"
            "v_or_b32 v113, 3, v113\n"
            "v_or_b32 v114, 3, v114\n"
            "s_waitcnt lgkmcnt(3)\n"
            "s_swappc_b64 s[80:81], s[70:71]\n"
            "v_lshlrev_b32 v98, 16, v99\n"
            "v_lshlrev_b32 v100, 16, v101\n"
            "v_lshlrev_b32 v102, 16, v103\n"
            "v_pk_fma_f32 v[98:99], v[98:99], v[108:109], v[86:87] op_sel:[0,0,0] op_sel_hi:[1,0,1]\n"
            "v_pk_fma_f32 v[100:101], v[100:101], v[108:109], v[86:87] op_sel:[0,1,0] op_sel_hi:[1,1,1]\n"
            "v_pk_fma_f32 v[102:103], v[102:103], v[110:111], v[86:87] op_sel:[0,0,0] op_sel_hi:[1,0,1]\n"
            "v_lshl_add_u32 v98, v99, 16, v98\n"
            "v_lshl_add_u32 v100, v101, 16, v100\n"
            "v_lshl_add_u32 v102, v103, 16, v102\n"
            "v_sub_u32 v98, v98, v86\n"
            "v_mad_i32_i16 v88, v99, v112, v88\n"
            "ds_add_u32 v112, v98 offset:65533\n"
            "v_sub_u32 v100, v100, v86\n"
            "v_mad_i32_i16 v88, v101, v113, v88\n"
            "ds_add_u32 v113, v100 offset:65533\n"
            "v_sub_u32 v102, v102, v86\n"
            "v_mad_i32_i16 v88, v103, v114, v88\n"
            "ds_add_u32 v114, v102 offset:65533\n"
            "v_and_b32 v104, 0x1fffc, v32\n"
            "v_and_b32 v105, 0x1fffc, v33\n"
            "v_and_b32 v106, 0x1fffc, v34\n"
            "s_swappc_b64 s[80:81], s[72:73]\n"
            "ds_read_b32 v93, v104\n"
            "ds_read_b32 v95, v105\n"
            "ds_read_b32 v97, v106\n"
            "s_waitcnt lgkmcnt(0)\n"
            "s_branch Ls6_%=\n"
            "Le6_%=:\n"
            "s_cmp_lt_u32 s75, 8\n"
            "s_cbranch_scc1 1f\n"
            "s_swappc_b64 s[80:81], s[66:67]\n"
            "s_branch Lc6_%=\n"
            "1:\n"
            "s_cmp_lg_u32 s75, 7\n"
            "s_cbranch_scc1 1f\n"
            "s_setprio 2\n"
            "s_mov_b32 s79, 3\n"
            "s_branch Lc6_%=\n"
            "1:\n"
            "s_cmp_lg_u32 s75, 3\n"
            "s_cbranch_scc1 1f\n"
            "s_setprio 1\n"
            "s_mov_b32 s79, 1\n"
            "s_branch Lc6_%=\n"
            "1:\n"
            "s_setprio 0\n"
            "v_add_u32_dpp v111, v81, v82 row_newbcast:6 row_mask:0xf bank_mask:0x3\n"
            "v_add_u32_dpp v111, v81, v82 row_newbcast:14 row_mask:0xf bank_mask:0xc\n"
            "v_and_b32 v108, 0x7fe, v35\n"
            "v_and_b32 v109, 0x1ff800, v35\n"
            "v_lshrrev_b32 v110, 22, v35\n"
            "v_lshrrev_b32 v112, 15, v32\n"
            "v_lshrrev_b32 v113, 15, v33\n"
            "v_lshrrev_b32 v114, 15, v34\n"
            "v_and_b32 v111, " IDXMASK ", v111\n"
            "v_min_u32 v111, v111, v83\n"
            "buffer_load_dwordx4 v[32:35], v111, %[rs], 0 idxen " RAUX "\n"
            "v_fmamk_f32 v108, v108, 0x7e800000, v84\n"
            "v_fmamk_f32 v109, v109, 0x79800000, v84\n"
            "v_fmamk_f32 v110, v110, 0x7f000000, v84\n"
            "v_or_b32 v112, 3, v112\n"
            "v_or_b32 v113, 3, v113\n"
            "v_or_b32 v114, 3, v114\n"
            "s_waitcnt lgkmcnt(3)\n"
            "s_swappc_b64 s[80:81], s[70:71]\n"
            "v_lshlrev_b32 v92, 16, v93\n"
            "v_lshlrev_b32 v94, 16, v95\n"
            "v_lshlrev_b32 v96, 16, v97\n"
            "v_pk_fma_f32 v[92:93], v[92:93], v[108:109], v[86:87] op_sel:[0,0,0] op_sel_hi:[1,0,1]\n"
            "v_pk_fma_f32 v[94:95], v[94:95], v[108:109], v[86:87] op_sel:[0,1,0] op_sel_hi:[1,1,1]\n"
            "v_pk_fma_f32 v[96:97], v[96:97], v[110:111], v[86:87] op_sel:[0,0,0] op_sel_hi:[1,0,1]\n"
            "v_lshl_add_u32 v92, v93, 16, v92\n"
            "v_lshl_add_u32 v94, v95, 16, v94\n"
            "v_lshl_add_u32 v96, v97, 16, v96\n"
            "v_sub_u32 v92, v92, v86\n"
            "v_mad_i32_i16 v88, v93, v112, v88\n"
            "ds_add_u32 v112, v92 offset:65533\n"
            "v_sub_u32 v94, v94, v86\n"
            "v_mad_i32_i16 v88, v95, v113, v88\n"
            "ds_add_u32 v113, v94 offset:65533\n"
            "v_sub_u32 v96, v96, v86\n"
            "v_mad_i32_i16 v88, v97, v114, v88\n"
            "ds_add_u32 v114, v96 offset:65533\n"
            "v_and_b32 v104, 0x1fffc, v36\n"
            "v_and_b32 v105, 0x1fffc, v37\n"
            "v_and_b32 v106, 0x1fffc, v38\n"
            "s_swappc_b64 s[80:81], s[72:73]\n"
            "ds_read_b32 v99, v104\n"
            "ds_read_b32 v101, v105\n"
            "ds_read_b32 v103, v106\n"
            "s_waitcnt lgkmcnt(0)\n"
            "s_branch Ls7_%=\n"
            "Le7_%=:\n"
            "s_cmp_lt_u32 s75, 8\n"
            "s_cbranch_scc1 1f\n"
            "s_swappc_b64 s[80:81], s[66:67]\n"
            "s_branch Lc7_%=\n"
            "1:\n"
            "s_cmp_lg_u32 s75, 7\n"
            "s_cbranch_scc1 1f\n"
            "s_setprio 2\n"
            "s_mov_b32 s79, 3\n"
            "s_branch Lc7_%=\n"
            "1:\n"
            "s_cmp_lg_u32 s75, 3\n"
            "s_cbranch_scc1 1f\n"
            "s_setprio 1\n"
            "s_mov_b32 s79, 1\n"
            "s_branch Lc7_%=\n"
            "1:\n"
            "s_setprio 0\n"
            "v_add_u32_dpp v111, v81, v82 row_newbcast:7 row_mask:0xf bank_mask:0x3\n"
            "v_add_u32_dpp v111, v81, v82 row_newbcast:15 row_mask:0xf bank_mask:0xc\n"
            "v_and_b32 v108, 0x7fe, v39\n"
            "v_and_b32 v109, 0x1ff800, v39\n"
            "v_lshrrev_b32 v110, 22, v39\n"
            "v_lshrrev_b32 v112, 15, v36\n"
            "v_lshrrev_b32 v113, 15, v37\n"
            "v_lshrrev_b32 v114, 15, v38\n"
            "v_and_b32 v111, " IDXMASK ", v111\n"
            "v_min_u32 v111, v111, v83\n"
            "buffer_load_dwordx4 v[36:39], v111, %[rs], 0 idxen " RAUX "\n"
            "s_min_u32 s82, s82, 19\n"
            "s_add_u32 s82, s82, 1\n"
            "s_set_gpr_idx_on s82, gpr_idx(SRC0)\n"
            "s_nop 0\n"
            "v_mov_b32 v81, v60\n"
            "s_set_gpr_idx_off\n"
            "v_fmamk_f32 v108, v108, 0x7e800000, v84\n"
            "v_fmamk_f32 v109, v109, 0x79800000, v84\n"
            "v_fmamk_f32 v110, v110, 0x7f000000, v84\n"
            "v_or_b32 v112, 3, v112\n"
            "v_or_b32 v113, 3, v113\n"
            "v_or_b32 v114, 3, v114\n"
            "s_waitcnt lgkmcnt(3)\n"
            "s_swappc_b64 s[80:81], s[70:71]\n"
            "v_lshlrev_b32 v98, 16, v99\n"
            "v_lshlrev_b32 v100, 16, v101\n"
            "v_lshlrev_b32 v102, 16, v103\n"
            "v_pk_fma_f32 v[98:99], v[98:99], v[108:109], v[86:87] op_sel:[0,0,0] op_sel_hi:[1,0,1]\n"
            "v_pk_fma_f32 v[100:101], v[100:101], v[108:109], v[86:87] op_sel:[0,1,0] op_sel_hi:[1,1,1]\n"
            "v_pk_fma_f32 v[102:103], v[102:103], v[110:111], v[86:87] op_sel:[0,0,0] op_sel_hi:[1,0,1]\n"
            "v_lshl_add_u32 v98, v99, 16, v98\n"
            "v_lshl_add_u32 v100, v101, 16, v100\n"
            "v_lshl_add_u32 v102, v103, 16, v102\n"
            "v_sub_u32 v98, v98, v86\n"
            "v_mad_i32_i16 v88, v99, v112, v88\n"
            "ds_add_u32 v112, v98 offset:65533\n"
            "v_sub_u32 v100, v100, v86\n"
            "v_mad_i32_i16 v88, v101, v113, v88\n"
            "ds_add_u32 v113, v100 offset:65533\n"
            "v_sub_u32 v102, v102, v86\n"
            "v_mad_i32_i16 v88, v103, v114, v88\n"
            "ds_add_u32 v114, v102 offset:65533\n"
            "v_and_b32 v104, 0x1fffc, v8\n"
            "v_and_b32 v105, 0x1fffc, v9\n"
            "v_and_b32 v106, 0x1fffc, v10\n"
            "s_swappc_b64 s[80:81], s[72:73]\n"
            "ds_read_b32 v93, v104\n"
            "ds_read_b32 v95, v105\n"
            "ds_read_b32 v97, v106\n"
            "s_waitcnt lgkmcnt(0)\n"
            "s_branch Ls0_%=\n"
            "Lchg1%=:\n"
            "s_add_u32 s92, s92, 1\n"
            "s_cmp_eq_u32 s92, 8\n"
            "s_cbranch_scc1 1f\n"
            "s_barrier\n"
            "s_mov_b32 m0, %[wb]\n"
            "s_nop 0\n"
            "ds_write_addtid_b32 v40 offset:0\n"
            "ds_write_addtid_b32 v41 offset:256\n"
            "ds_write_addtid_b32 v42 offset:512\n"
            "ds_write_addtid_b32 v43 offset:768\n"
            "ds_write_addtid_b32 v44 offset:16384\n"
            "ds_write_addtid_b32 v45 offset:16640\n"
            "ds_write_addtid_b32 v46 offset:16896\n"
            "ds_write_addtid_b32 v47 offset:17152\n"
            "ds_write_addtid_b32 v48 offset:32768\n"
            "ds_write_addtid_b32 v49 offset:33024\n"
            "ds_write_addtid_b32 v50 offset:33280\n"
            "ds_write_addtid_b32 v51 offset:33536\n"
            "ds_write_addtid_b32 v52 offset:49152\n"
            "ds_write_addtid_b32 v53 offset:49408\n"
            "ds_write_addtid_b32 v54 offset:49664\n"
            "ds_write_addtid_b32 v55 offset:49920\n"
            "s_add_u32 m0, %[wb], 16384\n"
            "s_nop 0\n"
            "ds_write_addtid_b32 v56 offset:49152\n"
            "ds_write_addtid_b32 v57 offset:49408\n"
            "ds_write_addtid_b32 v58 offset:49664\n"
            "ds_write_addtid_b32 v59 offset:49920\n"
            "1:\n"
            "s_setpc_b64 s[80:81]\n"
            "Lchg2%=:\n"
            "s_cmp_eq_u32 s92, 8\n"
            "s_cbranch_scc1 Lfin%=\n"
            "s_lshr_b64 s[76:77], s[76:77], 8\n"
            "s_and_b32 s75, s76, 255\n"
            "s_movk_i32 s74, 1000\n"
            "s_sub_i32 s79, s75, %[j0]\n"
            "s_max_i32 s79, s79, 8\n"
            "s_cmp_eq_u32 s92, 7\n s_cselect_b32 s79, 8, s79\n"
            "s_setprio 3\n"
            "s_waitcnt lgkmcnt(3)\n"
            "s_barrier\n"
            "s_setpc_b64 s[80:81]\n"
            "Lchg3%=:\n"
            "s_sub_u32 s74, s75, 7\n"
            "s_mov_b32 s79, 7\n"
            "s_cmp_eq_u32 s92, 7\n"
            "s_cbranch_scc1 1f\n"
            "s_add_u32 s94, s94, %[i4]\n"
            "buffer_load_dwordx4 v[40:43], %[tid16], %[rm], s94 offen\n"
            "s_add_u32 s96, s94, 16384\n"
            "buffer_load_dwordx4 v[44:47], %[tid16], %[rm], s96 offen\n"
            "s_add_u32 s96, s94, 32768\n"
            "buffer_load_dwordx4 v[48:51], %[tid16], %[rm], s96 offen\n"
            "s_add_u32 s96, s94, 49152\n"
            "buffer_load_dwordx4 v[52:55], %[tid16], %[rm], s96 offen\n"
            "s_add_u32 s96, s94, 65536\n"
            "buffer_load_dwordx4 v[56:59], %[tid16], %[rm], s96 offen\n"
            "s_branch 2f\n"
            "1:\n"
            "buffer_load_ushort v48, %[ed], %[rd], 0 offen " NTH "\n"
            "buffer_load_ushort v49, %[ed], %[rd], 0 offen offset:2 " NTH "\n"
            "buffer_load_ushort v50, %[ed], %[rd], %[e1] offen " NTH "\n"
            "buffer_load_ushort v51, %[ed], %[rd], %[e1] offen offset:2 " NTH "\n"
            "v_lshlrev_b32 v52, 2, %[ed]\n"
            "s_lshl_b32 s96, %[e1], 2\n"
            "buffer_load_dwordx4 v[40:43], v52, %[rmm], 0 offen " NTH "\n"
            "buffer_load_dwordx4 v[44:47], v52, %[rmm], s96 offen " NTH "\n"
            "2:\n"
            "s_setpc_b64 s[80:81]\n"
            "Lfin%=:\n"
            "s_waitcnt lgkmcnt(0)\n"
            "s_barrier\n"
            "ds_read_b128 v[60:63], v89 offset:0\n"
            "ds_read_b128 v[64:67], v89 offset:16384\n"
            "ds_read_b128 v[68:71], v89 offset:32768\n"
            "ds_read_b128 v[72:75], v89 offset:49152\n"
            "ds_read_b128 v[76:79], v90\n"
            "v_subrev_u32 v117, 65533, v89\n"
            "v_mov_b32 v118, 0\n"
            "v_bfe_u32 v116, %[tid16], 9, 3\n"
            "v_cmp_eq_u32 vcc, %[q], v116\n"
            "v_bfrev_b32 v115, 1\n"
            "s_nop 0\n"
            "v_cndmask_b32 v115, %[tid16], v115, vcc\n"
            "s_mov_b32 s96, 16384\n s_mov_b32 s97, 32768\n s_mov_b32 s92, 49152\n s_mov_b32 s93, 65536\n"
            "s_waitcnt lgkmcnt(4)\n"
            "buffer_store_dwordx4 v[60:63], v115, %[rp0], 0 offen sc1\n"
            "s_waitcnt lgkmcnt(3)\n"
            "buffer_store_dwordx4 v[64:67], v115, %[rp0], s96 offen sc1\n"
            "s_waitcnt lgkmcnt(2)\n"
            "buffer_store_dwordx4 v[68:71], v115, %[rp0], s97 offen sc1\n"
            "s_waitcnt lgkmcnt(1)\n"
            "buffer_store_dwordx4 v[72:75], v115, %[rp0], s92 offen sc1\n"
            "s_waitcnt lgkmcnt(0)\n"
            "buffer_store_dwordx4 v[76:79], v115, %[rp0], s93 offen sc1\n"
            "v_add_u32 v112, 0x8000, v60\n"
            "v_add_u32 v116, 0, v117\n"
            "v_mad_i32_i16 v118, v112, v116, v118 op_sel:[1,0,0,0]\n"
            "v_add_u32 v112, 0x8000, v61\n"
            "v_add_u32 v116, 4, v117\n"
            "v_mad_i32_i16 v118, v112, v116, v118 op_sel:[1,0,0,0]\n"
            "v_add_u32 v112, 0x8000, v62\n"
            "v_add_u32 v116, 8, v117\n"
            "v_mad_i32_i16 v118, v112, v116, v118 op_sel:[1,0,0,0]\n"
            "v_add_u32 v112, 0x8000, v63\n"
            "v_add_u32 v116, 12, v117\n"
            "v_mad_i32_i16 v118, v112, v116, v118 op_sel:[1,0,0,0]\n"
            "v_add_u32 v117, 16384, v117\n"
            "v_add_u32 v112, 0x8000, v64\n"
            "v_add_u32 v116, 0, v117\n"
            "v_mad_i32_i16 v118, v112, v116, v118 op_sel:[1,0,0,0]\n"
            "v_add_u32 v112, 0x8000, v65\n"
            "v_add_u32 v116, 4, v117\n"
            "v_mad_i32_i16 v118, v112, v116, v118 op_sel:[1,0,0,0]\n"
            "v_add_u32 v112, 0x8000, v66\n"
            "v_add_u32 v116, 8, v117\n"
            "v_mad_i32_i16 v118, v112, v116, v118 op_sel:[1,0,0,0]\n"
            "v_add_u32 v112, 0x8000, v67\n"
            "v_add_u32 v116, 12, v117\n"
            "v_mad_i32_i16 v118, v112, v116, v118 op_sel:[1,0,0,0]\n"
            "v_add_u32 v117, 16384, v117\n"
            "v_add_u32 v112, 0x8000, v68\n"
            "v_add_u32 v116, 0, v117\n"
            "v_mad_i32_i16 v118, v112, v116, v118 op_sel:[1,0,0,0]\n"
            "v_add_u32 v112, 0x8000, v69\n"
            "v_add_u32 v116, 4, v117\n"
            "v_mad_i32_i16 v118, v112, v116, v118 op_sel:[1,0,0,0]\n"
            "v_add_u32 v112, 0x8000, v70\n"
            "v_add_u32 v116, 8, v117\n"
            "v_mad_i32_i16 v118, v112, v116, v118 op_sel:[1,0,0,0]\n"
            "v_add_u32 v112, 0x8000, v71\n"
            "v_add_u32 v116, 12, v117\n"
            "v_mad_i32_i16 v118, v112, v116, v118 op_sel:[1,0,0,0]\n"
            "v_add_u32 v117, 16384, v117\n"
            "v_add_u32 v112, 0x8000, v72\n"
            "v_add_u32 v116, 0, v117\n"
            "v_mad_i32_i16 v118, v112, v116, v118 op_sel:[1,0,0,0]\n"
            "v_add_u32 v112, 0x8000, v73\n"
            "v_add_u32 v116, 4, v117\n"
            "v_mad_i32_i16 v118, v112, v116, v118 op_sel:[1,0,0,0]\n"
            "v_add_u32 v112, 0x8000, v74\n"
            "v_add_u32 v116, 8, v117\n"
            "v_mad_i32_i16 v118, v112, v116, v118 op_sel:[1,0,0,0]\n"
            "v_add_u32 v112, 0x8000, v75\n"
            "v_add_u32 v116, 12, v117\n"
            "v_mad_i32_i16 v118, v112, v116, v118 op_sel:[1,0,0,0]\n"
            "v_add_u32 v117, 16384, v117\n"
            "v_add_u32 v112, 0x8000, v76\n"
            "v_add_u32 v116, 0, v117\n"
            "v_mad_i32_i16 v118, v112, v116, v118 op_sel:[1,0,0,0]\n"
            "v_add_u32 v112, 0x8000, v77\n"
            "v_add_u32 v116, 4, v117\n"
            "v_mad_i32_i16 v118, v112, v116, v118 op_sel:[1,0,0,0]\n"
            "v_add_u32 v112, 0x8000, v78\n"
            "v_add_u32 v116, 8, v117\n"
            "v_mad_i32_i16 v118, v112, v116, v118 op_sel:[1,0,0,0]\n"
            "v_add_u32 v112, 0x8000, v79\n"
            "v_add_u32 v116, 12, v117\n"
            "v_mad_i32_i16 v118, v112, v116, v118 op_sel:[1,0,0,0]\n"
            "v_add_u32 v117, 16384, v117\n"
            "v_sub_u32 v118, v88, v118\n"
            "v_mov_b32 v116, %[wb]\n"
            "v_mov_b32 v117, %[bad]\n"
            "v_add_u32_dpp v118, v118, v118 row_shr:1 row_mask:0xf bank_mask:0xf bound_ctrl:1\n"
            "s_nop 1\n"
            "v_add_u32_dpp v118, v118, v118 row_shr:2 row_mask:0xf bank_mask:0xf bound_ctrl:1\n"
            "s_nop 1\n"
            "v_add_u32_dpp v118, v118, v118 row_shr:4 row_mask:0xf bank_mask:0xf bound_ctrl:1\n"
            "s_nop 1\n"
            "v_add_u32_dpp v118, v118, v118 row_shr:8 row_mask:0xf bank_mask:0xf bound_ctrl:1\n"
            "s_nop 1\n"
            "v_add_u32_dpp v118, v118, v118 row_bcast:15 row_mask:0xa bank_mask:0xf\n"
            "s_nop 1\n"
            "v_add_u32_dpp v118, v118, v118 row_bcast:31 row_mask:0xc bank_mask:0xf\n"
            "s_nop 1\n"
            "v_lshrrev_b32 v116, 7, v116\n"
            "s_mov_b32 exec_lo, 0\n s_mov_b32 exec_hi, 0x80000000\n"
            "ds_write2_b32 v116, v118, v117 offset1:1\n"
            "s_mov_b64 exec, -1\n"
            "s_waitcnt vmcnt(5)\n"
            "s_branch Lend2%=\n"
            "Lend%=:\n"
            "s_waitcnt vmcnt(0)\n"
            "Lend2%=:\n"
            "s_setprio 0\n"
            "s_waitcnt lgkmcnt(0)\n"

            : [mode] "=&s"(mode), [bad] "=&s"(bad), [exs] "=&v"(exs), [tid16] "+v"(t16), "+v"(tc2), "=&{v[40:43]}"(mm[0]), "=&{v[44:47]}"(mm[1]), "=&{v[48:51]}"(dd)
            : [rs] "s"(rs), [rm] "s"(rm), [rp0] "s"(rp0), [rt] "s"(rt), [i4] "s"(LMAX * 4), [c4] "s"(a_.c4), [pb] "s"(81920 + wave * 5120), [wb] "s"(wave * 1024), [q] "s"(q), [j0] "s"(J0),
              [rmm] "s"(rmm), [rd] "s"(rd), [e1] "s"(128 * 32 * 128 * 2), [ed] "v"((((wave * 8 + q) * 32 + b) * 128 + lane * 2) * 2),
              [fof] "s"((NT * NWS * 4 + cp * (P1_GRID * 2)) * 4), [dumu] "s"(ly.dumu), [lmul] "n"(LMUL), [lmask] "n"(LMASK),
              [tof] "v"((w * NT + t0) * 16), [fb] "v"(pairidx(w) * (ly.F0 * 256)), [xb] "v"(logbase(b >> 1, w, P_XO))
            : "memory", "vcc", "scc", "m0", "s66", "s67", "s70", "s71", "s72", "s73", "s74", "s75", "s76", "s77", "s79", "s80", "s81", "s82",
              "s84", "s85", "s86", "s87", "s88", "s89", "s90", "s91", "s92", "s93", "s94", "s96", "s97",
              "v8", "v9", "v10", "v11", "v12", "v13", "v14", "v15", "v16", "v17", "v18", "v19", "v20", "v21", "v22", "v23", "v24", "v25", "v26", "v27", "v28", "v29", "v30", "v31", "v32", "v33", "v34", "v35", "v36", "v37", "v38", "v39", "v52", "v53", "v54", "v55", "v56", "v57", "v58", "v59", "v60", "v61", "v62", "v63", "v64", "v65", "v66", "v67", "v68", "v69", "v70", "v71", "v72", "v73", "v74", "v75", "v76", "v77", "v78", "v79", "v80", "v81", "v82", "v83", "v84", "v85", "v86", "v87", "v88", "v89", "v90", "v91", "v92", "v93", "v94", "v95", "v96", "v97", "v98", "v99", "v100", "v101", "v102", "v103", "v104", "v105", "v106", "v107", "v108", "v109", "v110", "v111", "v112", "v113", "v114", "v115", "v116", "v117", "v118");
    }
    const int tid2 = (int)(t16 >> 4);
    kaptr ka = (kaptr)__builtin_amdgcn_kernarg_segment_ptr();
    asm volatile("" : "+s"(ka));
    unsigned long long* wm = (unsigned long long*)(ka->wgmode + b * 32);
    const int E = ka->E;
    const unsigned nblk = ka->O >> 7;
    const int ln = tid2 & 63, wv = __builtin_amdgcn_readfirstlane(tid2 >> 6);
    const bool two = (16 + wv) * 8 + q < nblk;
    float g[2][2][4], h[2][2][4];
    auto gh = [&]() {
#pragma unroll
        for (int p = 0; p < 2; p++) {
            if (p == 0 || two) {
                edge_gh(lo_bf(p ? dd.z : dd.x), ka->centers, ka->w_sbf, ka->w_rbf, g[p][0], h[p][0]);
                edge_gh(lo_bf(p ? dd.w : dd.y), ka->centers, ka->w_sbf, ka->w_rbf, g[p][1], h[p][1]);
            }
        }
    };
    v2u own[2] = {{0, 0}, {0, 0}};
    if (mode == 0) {
        gh();
        own[0] = *(const v2u*)(lds + OBASE + (wv * 8 + q) * 128 + ln * 2);
        if (two) own[1] = *(const v2u*)(lds + OBASE + ((16 + wv) * 8 + q) * 128 + ln * 2);
        asm volatile("s_waitcnt vmcnt(0)" : "+v"(g[0][0][0]), "+v"(h[0][1][3]) :: "memory");
        __syncthreads();
        const v2u cs = *(const v2u*)(lds + (ln & 15) * 2);
        if (__builtin_amdgcn_readlane(row_scan_add(cs.x), 15) != 0 || __ballot(cs.y != 0)) mode = 1;
    }
    if (mode != 0) {
        acc_fallback(lds, mode, *ka, tid2);
        asm volatile("s_waitcnt vmcnt(0)" ::: "memory");
        __syncthreads();
        if (tid2 == P2_THREADS - 1) { __builtin_amdgcn_fence(__ATOMIC_RELEASE, "agent"); asm volatile("s_waitcnt vmcnt(0)" ::: "memory"); }
        const __amdgpu_buffer_rsrc_t rmm = __builtin_amdgcn_make_buffer_rsrc((void*)ka->m, (short)0, E * 8, 0x00020000);
        const __amdgpu_buffer_rsrc_t rdd = __builtin_amdgcn_make_buffer_rsrc((void*)ka->dist, (short)0, E * 2, 0x00020000);
        const unsigned e = ((wv * 8 + q) * 32 + b) * 128 + ln * 2, e1 = e + 128 * 32 * 128;
        mm[0] = __builtin_amdgcn_raw_buffer_load_b128(rmm, e * 8, 0, 0); mm[1] = __builtin_amdgcn_raw_buffer_load_b128(rmm, e1 * 8, 0, 0);
        dd.x = __builtin_amdgcn_raw_buffer_load_b16(rdd, e * 2, 0, 0); dd.y = __builtin_amdgcn_raw_buffer_load_b16(rdd, e * 2 + 2, 0, 0);
        dd.z = __builtin_amdgcn_raw_buffer_load_b16(rdd, e1 * 2, 0, 0); dd.w = __builtin_amdgcn_raw_buffer_load_b16(rdd, e1 * 2 + 2, 0, 0);
        gh();
        own[0].x = own[0].y = own[1].x = own[1].y = 0;
    }
    const unsigned* prb = ka->praw + (size_t)b * ka->O; unsigned short* outp = ka->out;
    int EP = ka->EP;
    unsigned wu[8];
#pragma unroll
    for (int i = 0; i < 8; i++) wu[i] = ((cptr)ka->w_up)[i];
    if (tid2 == P2_THREADS - 1) {
        const unsigned long long add = (1u | (mode ? 256u << q : 0u)) | (qt == 0 ? (unsigned long long)(__builtin_amdgcn_readfirstlane(exs) & 0xFFFF) << (32 + 16 * cp) : 0ull);
        unsigned long long v = __hip_atomic_fetch_add(wm, add, __ATOMIC_RELAXED, __HIP_MEMORY_SCOPE_AGENT) + add;
        for (int spin = 0; spin < (1 << 22) && (v & 255) != 8; spin++) {
            v = __hip_atomic_load(wm, __ATOMIC_RELAXED, __HIP_MEMORY_SCOPE_AGENT);
            if ((v & 255) != 8) __builtin_amdgcn_s_sleep(2);
        }
        if (v & 0xFF00) { __builtin_amdgcn_fence(__ATOMIC_ACQUIRE, "agent"); asm volatile("s_waitcnt vmcnt(0)" ::: "memory"); }
        lds[64] = ((unsigned)v >> 8) & 255; lds[65] = (unsigned)(v >> 32);
    }
    asm volatile("" : "+s"(prb), "+s"(outp), "+s"(EP), "+s"(wu[0]), "+s"(wu[1]), "+s"(wu[2]), "+s"(wu[3]), "+s"(wu[4]), "+s"(wu[5]), "+s"(wu[6]), "+s"(wu[7]));
    const __amdgpu_buffer_rsrc_t rpr = __builtin_amdgcn_make_buffer_rsrc((void*)prb, (short)0, (7 * EP + (int)(nblk << 7)) * 4, 0x00020000);
    const __amdgpu_buffer_rsrc_t rou = __builtin_amdgcn_make_buffer_rsrc((void*)outp, (short)0, E * 8, 0x00020000);
    __syncthreads();
    v2u w[2][8];
#pragma unroll
    for (int p = 0; p < 2; p++) {
        if (p == 0 || two) {
#pragma unroll
            for (int j = 0; j < 8; j++)
                w[p][j] = __builtin_amdgcn_raw_buffer_load_b64(rpr, (((p * 16 + wv) * 8 + q) * 128 + ln * 2) * 4, j == q ? (int)OOB : j * EP * 4, PR_AUX);
        }
    }
    const unsigned fbm = __builtin_amdgcn_readfirstlane(lds[64]), exw = __builtin_amdgcn_readfirstlane(lds[65]);
    float inv[4];
#pragma unroll
    for (int k = 0; k < 4; k++) inv[k] = __uint_as_float(((exw >> (8 * k)) & 255) << 23) * (1.0f / 511.0f);
#pragma unroll
    for (int p = 0; p < 2; p++) {
        const unsigned blk = (p * 16 + wv) * 8 + q;
        if (p == 0 || two) {
            int s[2][4];
            {
                const int l0 = (int)(short)(own[p].x & 0xFFFF), h0 = (int)(own[p].x + 0x8000u) >> 16, l1 = (int)(short)(own[p].y & 0xFFFF), h1 = (int)(own[p].y + 0x8000u) >> 16;
                s[0][0] = cp ? 0 : l0; s[0][1] = cp ? 0 : h0; s[0][2] = cp ? l0 : 0; s[0][3] = cp ? h0 : 0;
                s[1][0] = cp ? 0 : l1; s[1][1] = cp ? 0 : h1; s[1][2] = cp ? l1 : 0; s[1][3] = cp ? h1 : 0;
            }
#pragma unroll
            for (int j = 0; j < 8; j++) {
                s[0][(j & 1) * 2] += (int)(short)(w[p][j].x & 0xFFFF); s[0][(j & 1) * 2 + 1] += (int)(w[p][j].x + 0x8000u) >> 16;
                s[1][(j & 1) * 2] += (int)(short)(w[p][j].y & 0xFFFF); s[1][(j & 1) * 2 + 1] += (int)(w[p][j].y + 0x8000u) >> 16;
            }
            float a[2][4];
#pragma unroll
            for (int i = 0; i < 2; i++)
#pragma unroll
                for (int k = 0; k < 4; k++) a[i][k] = s[i][k] ? (float)s[i][k] * inv[k] : 0.f;
            if (fbm && blk < nblk) {
                const float* part = ka->part + (size_t)b * ka->O + blk * 128 + ln * 2;
#pragma unroll 1
                for (int j = 0; j < 8; j++)
                    if ((fbm >> j) & 1) {
#pragma unroll
                        for (int i = 0; i < 2; i++) {
                            const float x = part[(size_t)(j * 2) * EP + i], y = part[(size_t)(j * 2 + 1) * EP + i];
                            if (j & 1) { a[i][2] += x; a[i][3] += y; } else { a[i][0] += x; a[i][1] += y; }
                        }
                    }
            }
            const v2u r0 = edge_out(a[0], g[p][0], h[p][0], mm[p].x, mm[p].y, wu), r1 = edge_out(a[1], g[p][1], h[p][1], mm[p].z, mm[p].w, wu);
            const v4u r = {r0.x, r0.y, r1.x, r1.y};
            __builtin_amdgcn_raw_buffer_store_b128(r, rou, ((blk * 32 + b) * 128 + ln * 2) * 8, 0, OUT_AUX);
        }
    }
}

extern "C" __global__ __launch_bounds__(256) void k_naive(const int* __restrict__ idx_in, const int* __restrict__ idx_out,
                                               const unsigned short* __restrict__ angle, const unsigned short* __restrict__ ang_freq,
                                               const unsigned short* __restrict__ m, const unsigned short* __restrict__ w_down,
                                               float* __restrict__ part, int T, int EP) {
    const float freq = bf2f(ang_freq[0]);
    for (long long t = (long long)blockIdx.x * 256 + threadIdx.x; t < T; t += (long long)gridDim.x * 256) {
        v2u mv = ((const v2u*)m)[idx_in[t]];
        float a[4] = {lo_bf(mv.x), hi_bf(mv.x), lo_bf(mv.y), hi_bf(mv.y)};
        float c = __cosf(freq * bf2f(angle[t]));
        int eo = idx_out[t];
#pragma unroll
        for (int d = 0; d < 4; d++) {
            float s = 0.f;
#pragma unroll
            for (int k = 0; k < 4; k++) s += a[k] * bf2f(w_down[k * 4 + d]);
            atomicAdd(&part[(size_t)d * EP + eo], s * c);
        }
    }
}

__global__ __launch_bounds__(256) void k_final(const unsigned short* __restrict__ m, const unsigned short* __restrict__ dist,
                                               const unsigned short* __restrict__ centers, const unsigned short* __restrict__ w_rbf,
                                               const unsigned short* __restrict__ w_sbf, const unsigned short* __restrict__ w_up,
                                               const float* __restrict__ part, unsigned short* __restrict__ out, int E, int EP) {
    const int e = blockIdx.x * 256 + threadIdx.x;
    if (e >= E) return;
    float a[4], g[4], h[4];
#pragma unroll
    for (int k = 0; k < 4; k++) a[k] = part[(size_t)k * EP + e];
    edge_gh(bf2f(dist[e]), centers, w_sbf, w_rbf, g, h);
    const v2u mv = ((const v2u*)m)[e];
    unsigned wu[8];
#pragma unroll
    for (int i = 0; i < 8; i++) wu[i] = ((cptr)w_up)[i];
    ((v2u*)out)[e] = edge_out(a, g, h, mv.x, mv.y, wu);
}

extern "C" void kernel_launch(void* const* d_in, const int* in_sizes, int n_in,
                              void* d_out, int out_size, void* d_ws, size_t ws_size,
                              hipStream_t stream) {
    const unsigned short* m = (const unsigned short*)d_in[0];
    const int* idx_in = (const int*)d_in[1];
    const int* idx_out = (const int*)d_in[2];
    const unsigned short* dist = (const unsigned short*)d_in[3];
    const unsigned short* angle = (const unsigned short*)d_in[4];
    const unsigned short* centers = (const unsigned short*)d_in[5];
    const unsigned short* ang_freq = (const unsigned short*)d_in[6];
    const unsigned short* w_rbf = (const unsigned short*)d_in[7];
    const unsigned short* w_sbf = (const unsigned short*)d_in[8];
    const unsigned short* w_down = (const unsigned short*)d_in[9];
    const unsigned short* w_up = (const unsigned short*)d_in[10];
    const int E = in_sizes[0] / 4;
    const int T = in_sizes[1];
    const int NR = T / (P1_GRID * CH);
    const int per = ((T - NR * (P1_GRID * CH) + P1_GRID - 1) / P1_GRID + 3) / 4 * 4;
    const unsigned O = (unsigned)(((E + 127) / 128 + 31) / 32) * 128;
    const unsigned I = (unsigned)(E + 31) / 32;
    const int EP = (int)O * 32;
    Lay ly;
    const unsigned meanr = (unsigned)(((size_t)NR * CH + per) / NT);
    ly.F0 = meanr >= 100 ? 40 : 72;
    if (LA > 3) ly.F0 = (ly.F0 + (1 << LA) - 1) >> LA << LA;
    ly.fcap = ly.F0 / 4 * 505;
    ly.cap = 768;
    char* ws = (char*)d_ws;
    char* rec = ws;
    size_t o = 0x200000;
    ly.dumu = ly.cap * 32768;
    o += ((size_t)ly.dumu + 64) * 16;
    unsigned* praw = (unsigned*)(ws + o); o += (size_t)8 * EP * 4; o = (o + 255) / 256 * 256;
    unsigned* wgmode = (unsigned*)(ws + o); o += 8192;
    unsigned* mdp = (unsigned*)(ws + o); o += (size_t)2 * 32 * LMAX * 4;
    unsigned* tab = (unsigned*)(ws + o); o += (size_t)NT * NWS * 16;
    unsigned* flags = (unsigned*)(ws + o); o += FL_SIZE * 4; o = (o + 255) / 256 * 256;
    float* part = (float*)(ws + o); o += (size_t)16 * EP * 4;
    const bool ok = E >= 16384 && O <= LMAX && I <= LMAX && T > 0 && meanr <= 180 && o <= ws_size;
    if (!ok) {
        part = (float*)ws;
        const int EQ = (E + 3) / 4 * 4;
        (void)hipMemsetAsync(part, 0, (size_t)4 * EQ * 4, stream);
        if (T > 0) k_naive<<<2048, 256, 0, stream>>>(idx_in, idx_out, angle, ang_freq, m, w_down, part, T, EQ);
        k_final<<<(E + 255) / 256, 256, 0, stream>>>(m, dist, centers, w_rbf, w_sbf, w_up, part, (unsigned short*)d_out, E, EQ);
        return;
    }
    k_bin<<<P1_GRID, P1_THREADS, 0, stream>>>(idx_in, idx_out, angle, ang_freq, m, w_down, mdp, E, T, rec, tab, flags, NR, per, ly, wgmode);
    AccArgs aa;
    aa.rec = rec; aa.tab = tab; aa.flags = flags; aa.mdp = mdp; aa.part = part; aa.praw = praw; aa.wgmode = wgmode;
    aa.idx_in = idx_in; aa.idx_out = idx_out; aa.angle = angle; aa.ang_freq = ang_freq; aa.m = m; aa.w_down = w_down;
    aa.EP = EP; aa.O = O; aa.T = T; aa.c4 = (float)((double)E / 144.0);
    aa.dist = dist; aa.centers = centers; aa.w_rbf = w_rbf; aa.w_sbf = w_sbf; aa.w_up = w_up; aa.out = (unsigned short*)d_out; aa.E = E; aa.unused = 0;
    aa.ly = ly;
    k_acc<<<256, P2_THREADS, 0, stream>>>(aa);
}
```

```cpp
#include <hip/hip_runtime.h>
#include <hip/hip_bf16.h>
#include <stdint.h>

typedef int v4i __attribute__((ext_vector_type(4)));
typedef unsigned v4u __attribute__((ext_vector_type(4)));
typedef unsigned v2u __attribute__((ext_vector_type(2)));
typedef int v4i_rs __attribute__((ext_vector_type(4)));
typedef __attribute__((address_space(3))) unsigned lds_u32;
typedef __attribute__((address_space(3))) v4u lds_v4u;

static __device__ __forceinline__ float bf2f(unsigned short v) { return __uint_as_float(((unsigned)v) << 16); }
static __device__ __forceinline__ unsigned f2bf_bits(float f) {
    unsigned u = __float_as_uint(f);
    u += 0x7FFF + ((u >> 16) & 1);
    return u >> 16;
}
static __device__ __forceinline__ float lo_bf(unsigned u) { return __uint_as_float(u << 16); }
static __device__ __forceinline__ float hi_bf(unsigned u) { return __uint_as_float(u & 0xFFFF0000u); }
static __device__ __forceinline__ v4i_rs mk_rsrc(const void* p, unsigned bytes) {
    unsigned long long a = (unsigned long long)p;
    v4i_rs r = {(int)(unsigned)a, (int)(unsigned)((a >> 32) & 0xFFFF), (int)bytes, 0x00020000};
    return r;
}
static __device__ __forceinline__ lds_u32* ldsp(unsigned byte) { return (lds_u32*)(size_t)byte; }

#define P1_THREADS 1024
#define PD 1
#define UNR ((PD & 1) ? 2 * PD : PD)
#define P1_GRID 256
#define NWS 256
#define CH (P1_THREADS * 4)
#define NT 1024
#define LMAX 20480
#define P2_THREADS 1024
#define OOB 0x80000000u
#define OBASE 20480
#define IDXB 4096
#define ST_AUX 16
#define LB 4096
#define LS 132
#define TINC 683
#define WL_OFF (LB + NT * LS)
#define MS_OFF (WL_OFF + 16 * 512)
#define BIN_LDS (MS_OFF + 128 + 320)
#define NSTAT 14
#define WM_CTR 1024
#define WM_BOX (WM_CTR + 256)
#define RM 1
#define RXOR 0
#define CALN 3
#define VTHR 5
#define CORD 0
#define PSTR 256
#define NTH "nt"
#define OUT_AUX 16
#define PR_AUX 18
#define TCH 7
#define CZERO ((512u << 1) | (512u << 11) | (512u << 22))
#define FL_SIZE (2 * P1_GRID * 2)

struct Grp { v4i io; v4i ii; v2u an; };
static __device__ __forceinline__ void load_grp(Grp& r, v4i_rs ro, v4i_rs ri, v4i_rs ra, unsigned c, unsigned vo16, unsigned vo8, int parts = 7) {
    if (parts & 2) asm volatile("buffer_load_dwordx4 %0, %1, %2, %3 offen nt" : "=v"(r.io) : "v"(vo16), "s"(ro), "s"(c * 256));
    if (parts & 4) asm volatile("buffer_load_dwordx4 %0, %1, %2, %3 offen nt" : "=v"(r.ii) : "v"(vo16), "s"(ri), "s"(c * 256));
    if (parts & 1) asm volatile("buffer_load_dwordx2 %0, %1, %2, %3 offen nt" : "=v"(r.an) : "v"(vo8), "s"(ra), "s"(c * 128));
}
#define WAIT_GRP(n, g) asm volatile("s_waitcnt vmcnt(%3)" : "+v"((g).io), "+v"((g).ii), "+v"((g).an) : "n"(n))

static __device__ __forceinline__ unsigned wave_scan_max(unsigned x) {
    x = max(x, (unsigned)__builtin_amdgcn_update_dpp(0, (int)x, 0x111, 0xF, 0xF, false));
    x = max(x, (unsigned)__builtin_amdgcn_update_dpp(0, (int)x, 0x112, 0xF, 0xF, false));
    x = max(x, (unsigned)__builtin_amdgcn_update_dpp(0, (int)x, 0x114, 0xF, 0xF, false));
    x = max(x, (unsigned)__builtin_amdgcn_update_dpp(0, (int)x, 0x118, 0xF, 0xF, false));
    x = max(x, (unsigned)__builtin_amdgcn_update_dpp(0, (int)x, 0x142, 0xA, 0xF, false));
    x = max(x, (unsigned)__builtin_amdgcn_update_dpp(0, (int)x, 0x143, 0xC, 0xF, false));
    return x;
}
static __device__ __forceinline__ unsigned row_scan_add(unsigned x) {
    x += (unsigned)__builtin_amdgcn_update_dpp(0, (int)x, 0x111, 0xF, 0xF, false);
    x += (unsigned)__builtin_amdgcn_update_dpp(0, (int)x, 0x112, 0xF, 0xF, false);
    x += (unsigned)__builtin_amdgcn_update_dpp(0, (int)x, 0x114, 0xF, 0xF, false);
    x += (unsigned)__builtin_amdgcn_update_dpp(0, (int)x, 0x118, 0xF, 0xF, false);
    return x;
}
static __device__ __forceinline__ unsigned wave_scan_add(unsigned x) {
    x = row_scan_add(x);
    x += (unsigned)__builtin_amdgcn_update_dpp(0, (int)x, 0x142, 0xA, 0xF, false);
    x += (unsigned)__builtin_amdgcn_update_dpp(0, (int)x, 0x143, 0xC, 0xF, false);
    return x;
}

static __device__ __forceinline__ float wave_scan_addf(float x) {
    x += __int_as_float(__builtin_amdgcn_update_dpp(0, __float_as_int(x), 0x111, 0xF, 0xF, false));
    x += __int_as_float(__builtin_amdgcn_update_dpp(0, __float_as_int(x), 0x112, 0xF, 0xF, false));
    x += __int_as_float(__builtin_amdgcn_update_dpp(0, __float_as_int(x), 0x114, 0xF, 0xF, false));
    x += __int_as_float(__builtin_amdgcn_update_dpp(0, __float_as_int(x), 0x118, 0xF, 0xF, false));
    x += __int_as_float(__builtin_amdgcn_update_dpp(0, __float_as_int(x), 0x142, 0xA, 0xF, false));
    x += __int_as_float(__builtin_amdgcn_update_dpp(0, __float_as_int(x), 0x143, 0xC, 0xF, false));
    return x;
}

struct Lay { unsigned F0, cap, dumu, fcap; };
#define P_A 0
#define P_B 0
#define P_XO 4
#define P_DF (P_NEW ? 0 : V & 3)
#define P_NEW ((V >> 7) & 1)
#define P_X (V & 3)
#define P_GAP ((V >> 8) & 1)
#define P_EA ((V >> 2) & 7)
#define P_EP ((V >> 5) & 3)
#define N_EA ((P_EA & 1) + ((P_EA >> 1) & 1) + (P_EA >> 2))
#define VDEF 264
#define P_LP 6
#define P_LS 15
#define P_JM 1
#define SHP 2
#define LA 3
#define XORD 0
#define NPOS 10
#define LMUL (8 + 32768)
#define LMASK ((((1 << LA) - 1) << 3) | ((1023 >> LA) << (15 + LA)))
static __device__ __forceinline__ char* rec_align(const char* p) { return (char*)(((size_t)p + 0x1FFFFF) & ~(size_t)0x1FFFFF); }
static __device__ __forceinline__ unsigned pairidx(unsigned w) { return (w >> 3) * 4 + (w & 3); }
static __device__ __forceinline__ unsigned logbase(unsigned wave, unsigned w, unsigned xo) {
    const unsigned p = xo & 16 ? (w & 3) * 32 + (w >> 3) : pairidx(w), k = xo & 15;
    const unsigned x = (p & ((1 << k) - 1)) | (wave << k) | ((p >> k) << (k + 4));
    return ((x & ((64 >> LA) - 1)) << (3 + LA)) | ((x >> (6 - LA)) << 10);
}

static __device__ __forceinline__ void conv_rec(unsigned io, unsigned ii, float ang, float frev, unsigned& A, unsigned& t4, unsigned& cq, unsigned& seen) {
    unsigned x, y;
    asm("v_and_b32 %0, 31, %4\n v_and_b32 %1, 0xf80, %3\n v_lshl_or_b32 %2, %0, 2, %1" : "=&v"(x), "=&v"(y), "=v"(t4) : "v"(io), "v"(ii));
    asm("v_lshrrev_b32 %0, 5, %3\n v_lshrrev_b32 %1, 3, %4\n v_bfi_b32 %0, %5, %3, %0\n v_add_u32 %1, %6, %1\n v_lshl_add_u32 %2, %0, 17, %1"
        : "=&v"(x), "=&v"(y), "=v"(A) : "v"(io), "v"(ii), "s"(127u), "n"(IDXB << 17));
    float c = __builtin_amdgcn_cosf(__builtin_amdgcn_fractf(frev * ang));
    const unsigned cb = __float_as_uint(__builtin_fmaf(c, 511.0f, 12583424.0f));
    seen |= cb;
    cq = cb & 0x3FFu;
}
static __device__ __forceinline__ void conv_rec2(unsigned io, unsigned ii, float ang, float frev, unsigned& A, unsigned& t4, unsigned& cq, unsigned& seen) {
    unsigned x, y;
    asm volatile("v_and_b32 %0, 31, %4\n v_and_b32 %1, 0xf80, %3\n v_lshl_or_b32 %2, %0, 2, %1" : "=&v"(x), "=&v"(y), "=v"(t4) : "v"(io), "v"(ii));
    asm volatile("v_lshrrev_b32 %0, 5, %3\n v_lshrrev_b32 %1, 3, %4\n v_bfi_b32 %0, %5, %3, %0\n v_add_u32 %1, %6, %1\n v_lshl_add_u32 %2, %0, 17, %1"
        : "=&v"(x), "=&v"(y), "=v"(A) : "v"(io), "v"(ii), "s"(127u), "n"(IDXB << 17));
    float c = __builtin_amdgcn_cosf(__builtin_amdgcn_fractf(frev * ang));
    const unsigned cb = __float_as_uint(__builtin_fmaf(c, 511.0f, 12583424.0f));
    seen |= cb;
    cq = cb & 0x3FFu;
}
#define PADV 1
static __device__ __forceinline__ unsigned pad_rec(unsigned q) { return PADV == 0 ? 0x7FFFu << 17 : PADV == 1 ? (0x7FFFu - (q & 511)) << 17 : (IDXB + (q & 511)) << 17; }

static __device__ __forceinline__ void prep_md(const v2u* mv, unsigned dst, const unsigned short* w_down, unsigned* mdp, unsigned plane, unsigned* mx, float* sq) {
    float wd[16];
#pragma unroll
    for (int i = 0; i < 8; i++) { unsigned u = ((const __attribute__((address_space(4))) unsigned*)w_down)[i]; wd[2 * i] = lo_bf(u); wd[2 * i + 1] = hi_bf(u); }
    unsigned o[2][4];
#pragma unroll
    for (int j = 0; j < 4; j++) {
        const float a[4] = {lo_bf(mv[j].x), hi_bf(mv[j].x), lo_bf(mv[j].y), hi_bf(mv[j].y)};
        float s[4];
#pragma unroll
        for (int d = 0; d < 4; d++) {
            s[d] = 0.f;
#pragma unroll
            for (int k = 0; k < 4; k++) s[d] += a[k] * wd[k * 4 + d];
            sq[d] += s[d] * s[d];
            mx[d] = max(mx[d], __float_as_uint(s[d]) & 0x7FFFFFFFu);
        }
#pragma unroll
        for (int p = 0; p < 2; p++) {
            const unsigned lo = f2bf_bits(s[2 * p]) & 0xFFFF;
            const unsigned sb = __float_as_uint(s[2 * p + 1]);
            const unsigned h = (unsigned)max((int)((sb & 0x7FFFFFFFu) - lo + 0x8000u), 0) & 0x7FFF0000u;
            o[p][j] = lo | h | (sb & 0x80000000u);
        }
    }
#pragma unroll
    for (int p = 0; p < 2; p++) {
        v4u v = {o[p][0], o[p][1], o[p][2], o[p][3]};
        *(v4u*)(mdp + (size_t)p * plane + dst) = v;
    }
}

static __device__ __forceinline__ unsigned touch_code(int tid) {
    unsigned alo, ahi, blo, r = 0;
    asm volatile("s_getpc_b64 s[96:97]\n s_add_u32 %0, s96, k_bin@rel32@lo+4\n s_addc_u32 %1, s97, k_bin@rel32@hi+12\n"
                 "s_getpc_b64 s[96:97]\n s_add_u32 %2, s96, k_naive@rel32@lo+4\n" : "=&s"(alo), "=&s"(ahi), "=&s"(blo) :: "scc", "s96", "s97");
    const unsigned size = blo - alo < 65536u ? blo - alo : 0u;
    const v4i_rs rc = {(int)alo, (int)(ahi & 0xFFFF), (int)size, 0x00020000};
    if (tid < 512) asm volatile("buffer_load_dword %0, %1, %2, 0 offen" : "=v"(r) : "v"(tid * 128), "s"(rc));
    return r;
}

#define touch_code2 touch_code
struct Held { unsigned a[4], t[4], A[4], cd[4]; unsigned long long m[4]; };
struct Conv { unsigned A[4], t4[4], cq[4]; };
#define HB(k) "s_mov_b64 exec, %[m" #k "]\n ds_write_b32 %[a" #k "], %[A" #k "]\n ds_or_b32 %[t" #k "], %[c" #k "]\n"
static __device__ __forceinline__ void held_out(const Held& H) {
    asm volatile(HB(0) HB(1) HB(2) HB(3) "s_mov_b64 exec, -1\n"
                 :: [m0] "s"(H.m[0]), [m1] "s"(H.m[1]), [m2] "s"(H.m[2]), [m3] "s"(H.m[3]), [a0] "v"(H.a[0]), [a1] "v"(H.a[1]), [a2] "v"(H.a[2]), [a3] "v"(H.a[3]),
                   [t0] "v"(H.t[0]), [t1] "v"(H.t[1]), [t2] "v"(H.t[2]), [t3] "v"(H.t[3]),
                   [A0] "v"(H.A[0]), [A1] "v"(H.A[1]), [A2] "v"(H.A[2]), [A3] "v"(H.A[3]), [c0] "v"(H.cd[0]), [c1] "v"(H.cd[1]), [c2] "v"(H.cd[2]), [c3] "v"(H.cd[3])
                 : "memory");
}
static __device__ __forceinline__ void tickets(unsigned* v, const Conv& c, unsigned inc) {
    asm volatile("ds_add_rtn_u32 %0, %4, %8\n ds_add_rtn_u32 %1, %5, %8\n ds_add_rtn_u32 %2, %6, %8\n ds_add_rtn_u32 %3, %7, %8\n"
                 : "=&v"(v[0]), "=&v"(v[1]), "=&v"(v[2]), "=&v"(v[3]) : "v"(c.t4[0]), "v"(c.t4[1]), "v"(c.t4[2]), "v"(c.t4[3]), "v"(inc) : "memory");
}
static __device__ __forceinline__ void tickets_masked(unsigned* v, const Conv& c, unsigned inc, const unsigned long long* vm) {
#pragma unroll
    for (int k = 0; k < 4; k++) {
        v[k] = 0;
        asm volatile("s_mov_b64 exec, %3\n ds_add_rtn_u32 %0, %1, %2\n s_mov_b64 exec, -1" : "+v"(v[k]) : "v"(c.t4[k]), "v"(inc), "s"(vm[k]) : "memory");
    }
}
static __device__ __forceinline__ void place(Held& H, unsigned* v, Conv& c, const unsigned long long* vm, unsigned lb) {
    unsigned bt[4];
#pragma unroll
    for (int k = 0; k < 4; k++) asm("v_mad_u32_u24 %0, %1, %2, %3" : "=v"(bt[k]) : "v"(c.t4[k]), "n"(LS / 4), "v"(lb));
    asm volatile("s_waitcnt lgkmcnt(0)" : "+v"(v[0]), "+v"(v[1]), "+v"(v[2]), "+v"(v[3]));
#pragma unroll
    for (int k = 0; k < 4; k++) {
        H.m[k] = __ballot(v[k] >= 16384u);
        unsigned sh;
        asm("v_lshrrev_b32 %0, 7, %3\n v_lshrrev_b32 %2, 6, %3\n v_and_b32 %0, 0x7c, %0\n v_or_b32 %1, 12, %0\n v_add_u32 %0, %0, %4\n v_add_u32 %1, %1, %4"
            : "=&v"(H.a[k]), "=&v"(H.t[k]), "=&v"(sh) : "v"(v[k]), "v"(bt[k]));
        H.A[k] = c.A[k];
        asm("v_lshlrev_b32 %0, %1, %2" : "=v"(H.cd[k]) : "v"(sh), "v"(c.cq[k]));
        asm volatile("s_andn2_b64 exec, %[w], %[m]\n ds_write_b32 %[a], %[A]\n ds_or_b32 %[t], %[c]\n s_mov_b64 exec, -1\n"
                     :: [w] "s"(vm[k]), [m] "s"(H.m[k]), [a] "v"(H.a[k]), [A] "v"(H.A[k]), [t] "v"(H.t[k]), [c] "v"(H.cd[k]) : "memory", "scc");
    }
}
struct Own { unsigned la, p[4], nl, ovf; };
static __device__ __forceinline__ v4u read_unit(unsigned la) {
    const lds_u32* p = ldsp(la);
    v4u v = {p[0], p[1], p[2], p[3]};
    return v;
}
typedef __attribute__((address_space(3))) v2u lds_v2u;
static __device__ __forceinline__ unsigned long long low_lanes(unsigned groups) { return groups >= 8 ? ~0ull : (1ull << (groups * 8)) - 1; }
static __device__ __forceinline__ unsigned flush_addr(unsigned e, unsigned f, unsigned long long act, unsigned base) {
    unsigned vo = (__umul24(f, LMUL * 16) & (LMASK * 16u)) | base;
    asm volatile("s_mov_b64 exec, %1\n ds_write_b32 %2, %3 offset:12\n s_mov_b64 exec, -1\n v_cndmask_b32 %0, %4, %0, %1"
                 : "+v"(vo) : "s"(act), "v"(e), "v"(0u), "v"(OOB) : "memory");
    return vo;
}
struct Pend { v4u v0, v1; unsigned a0, a1; };
static __device__ __forceinline__ void pend_out(const Pend& p, __amdgpu_buffer_rsrc_t rr) {
    __builtin_amdgcn_raw_buffer_store_b128(p.v0, rr, p.a0, 0, ST_AUX);
    __builtin_amdgcn_raw_buffer_store_b128(p.v1, rr, p.a1, 0, ST_AUX);
}
static __device__ __forceinline__ void flush(Own& o, Pend& pd, unsigned& wc, unsigned cnt, int tid, unsigned base, __amdgpu_buffer_rsrc_t rr, bool now) {
    const bool full = cnt >= 16384u;
    const unsigned long long bal = __ballot(full);
    const unsigned nfull = __popcll(bal);
    const unsigned wl = WL_OFF + (tid >> 6) * 512;
    if (full) {
        const unsigned pos = __builtin_amdgcn_mbcnt_hi((unsigned)(bal >> 32), __builtin_amdgcn_mbcnt_lo((unsigned)bal, 0));
        *ldsp(wl + pos * 4) = o.la;
        cnt -= 24 * TINC;
        *ldsp(tid * 4) = cnt;
        o.ovf |= cnt;
        o.nl++;
        o.p[3] = __builtin_amdgcn_alignbit(o.p[3], o.p[2], 22); o.p[2] = __builtin_amdgcn_alignbit(o.p[2], o.p[1], 22); o.p[1] = __builtin_amdgcn_alignbit(o.p[1], o.p[0], 22);
        o.p[0] = (o.p[0] << 10) + pos + wc;
    }
    const unsigned g = (tid >> 3) & 7, k16 = (tid & 7) * 16;
    const unsigned e0 = *ldsp(wl + g * 4) + k16, e1 = *ldsp(wl + g * 4 + 32) + k16;
    pd.v0 = read_unit(e0); pd.v1 = read_unit(e1);
    asm volatile("" ::: "memory");
    pd.a0 = flush_addr(e0, g + wc, low_lanes(nfull), base);
    pd.a1 = flush_addr(e1, g + wc + 8, nfull > 8 ? low_lanes(nfull - 8) : 0ull, base);
    if (now) pend_out(pd, rr);
    for (unsigned p = 16; p < nfull; p += 8) {
        const unsigned e = *ldsp(wl + (p + g) * 4) + k16;
        const v4u v = read_unit(e);
        asm volatile("" ::: "memory");
        __builtin_amdgcn_raw_buffer_store_b128(v, rr, flush_addr(e, g + wc + p, low_lanes(nfull - p), base), 0, ST_AUX);
    }
    wc = min(wc + nfull, 960u);
    asm volatile("s_waitcnt lgkmcnt(0)" : "+v"(pd.v0), "+v"(pd.v1) :: "memory");
}

static __device__ __forceinline__ void resolve(Held& H, unsigned* v, Conv& c, unsigned lb) {
    unsigned bt[4];
#pragma unroll
    for (int k = 0; k < 4; k++) asm("v_mad_u32_u24 %0, %1, %2, %3" : "=v"(bt[k]) : "v"(c.t4[k]), "n"(LS / 4), "v"(lb));
#pragma unroll
    for (int k = 0; k < 4; k++) {
        asm volatile("s_waitcnt lgkmcnt(%1)" : "+v"(v[k]) : "n"(3 - k));
        H.m[k] = __ballot(v[k] >= 16384u);
        unsigned sh;
        asm("v_lshrrev_b32 %0, 7, %3\n v_lshrrev_b32 %2, 6, %3\n v_and_b32 %0, 0x7c, %0\n v_or_b32 %1, 12, %0\n v_add_u32 %0, %0, %4\n v_add_u32 %1, %1, %4"
            : "=&v"(H.a[k]), "=&v"(H.t[k]), "=&v"(sh) : "v"(v[k]), "v"(bt[k]));
        H.A[k] = c.A[k];
        asm("v_lshlrev_b32 %0, %1, %2" : "=v"(H.cd[k]) : "v"(sh), "v"(c.cq[k]));
    }
#pragma unroll
    for (int k = 0; k < 4; k++) asm volatile("" : "+v"(H.a[k]), "+v"(H.t[k]), "+v"(H.cd[k]) : "s"(H.m[k]));
}
static __device__ __forceinline__ void put(const Held& H, int k) {
    asm volatile("s_not_b64 exec, %[m]\n ds_write_b32 %[a], %[A]\n ds_or_b32 %[t], %[c]\n s_mov_b64 exec, -1\n"
                 :: [m] "s"(H.m[k]), [a] "v"(H.a[k]), [A] "v"(H.A[k]), [t] "v"(H.t[k]), [c] "v"(H.cd[k]) : "memory", "scc");
}
static __device__ __forceinline__ void take_unit(v2u& x, v2u& y, unsigned e, unsigned long long act, unsigned zero) {
    asm volatile("s_mov_b64 exec, %3\n ds_read2_b32 %0, %2 offset1:1\n ds_read2_b32 %1, %2 offset0:2 offset1:3\n ds_write_b32 %2, %4 offset:12\n s_mov_b64 exec, -1"
                 : "=&v"(x), "=&v"(y) : "v"(e), "s"(act), "v"(zero) : "memory");
}

template <int V> static __device__ __forceinline__ void bin_body(unsigned* L, const int* __restrict__ idx_in, const int* __restrict__ idx_out,
                                                    const unsigned short* __restrict__ angle, const unsigned short* __restrict__ ang_freq,
                                                    const unsigned short* __restrict__ m, const unsigned short* __restrict__ w_down,
                                                    unsigned* __restrict__ mdp, int E, int T,
                                                    char* __restrict__ rec0, unsigned* __restrict__ tab, unsigned* __restrict__ flags,
                                                    int NR, int per, Lay ly, unsigned* __restrict__ wgmode) {
    const int tid = threadIdx.x;
    const unsigned run = blockIdx.x;
    char* rec = rec_align(rec0);
    unsigned tc0 = touch_code(tid);
    unsigned fbits;
    asm volatile("buffer_load_ushort %0, off, %1, 0" : "=v"(fbits) : "s"(mk_rsrc(ang_freq, 2)));
    float freq;
    const v4i_rs ro = mk_rsrc(idx_out, (unsigned)T * 4), ri = mk_rsrc(idx_in, (unsigned)T * 4), ra = mk_rsrc(angle, (unsigned)T * 2);
    const __amdgpu_buffer_rsrc_t rr = __builtin_amdgcn_make_buffer_rsrc((void*)rec, (short)0,
        (int)(ly.dumu * 16),
        0x00020000);
    const unsigned vo16 = ((tid >> P_LP) << P_LS) + (tid & ((1 << P_LP) - 1)) * 16, vo8 = vo16 >> 1;
    auto chunk = [&](unsigned c) {
        const unsigned nb = P_LS - 4 - P_LP, lo = 8 - P_LP;
        unsigned j = c & ((1 << nb) - 1);
        if (P_JM == 1) j = ((j & 7) << lo) | ((j >> 3) & ((1 << lo) - 1)) | (j >> (3 + lo) << (3 + lo));
        return ((c >> nb) << (nb + 6)) + (j << (P_LP - 4));
    };
#define NSH 4
    const unsigned shard = (run >> 3) & 3, member = (run & 7) | ((run >> 5) << 3);
    const bool leader = member == 0;
    const v4i_rs rw = mk_rsrc(wgmode + WM_CTR, (256 + 256) * 4);
    const int wave0 = __builtin_amdgcn_readfirstlane(tid >> 6) == 0;
    const unsigned ctr_o = shard * 128, box_o = (256 + shard * (256 / NSH) + member) * 4;
    if (wave0) asm volatile("buffer_store_dword %0, %1, %2, 0 offen sc1" :: "v"(0u), "v"(tid == 0 ? (leader ? ctr_o : box_o) : OOB), "s"(rw) : "memory");
    unsigned mail = NR < NSTAT ? 24u : 0u;
    if (leader && NR >= NSTAT) {
        const v4i_rs rp = mk_rsrc(rec, ly.dumu * 16);
        const unsigned pbase = __builtin_amdgcn_readfirstlane(logbase(tid >> 6, run, P_XO) + (((ly.F0 + (tid >> 6) * 24) * LMUL) & LMASK)) * 16;
        unsigned x = 0, xcc;
        asm volatile("buffer_atomic_add %0, %1, %2, %3 offen sc0" : "+v"(x) : "v"((tid & 63) < 2 * CALN ? (tid & 1) * 8192 + ((tid & 63) >> 1) * PSTR : OOB), "s"(rp), "s"(pbase));
        asm volatile("s_getreg_b32 %0, hwreg(HW_REG_XCC_ID)" : "=s"(xcc));
        unsigned mn[2] = {~0u, ~0u};
        unsigned long long ta, tb;
        asm volatile("s_waitcnt vmcnt(0)\n s_memtime %1" : "+v"(x), "=s"(ta));
#pragma unroll
        for (int i = 0; i < 2 * CALN; i++) {
            const int sd = (CORD ? i ^ (i >> 1) : i) & 1;
            asm volatile("v_mov_b32 %0, 0\n buffer_atomic_add %0, %5, %3, %4 offen sc0\n s_waitcnt vmcnt(0)\n s_memtime %1\n s_waitcnt lgkmcnt(0)"
                         : "+v"(x), "=s"(tb), "+s"(ta) : "s"(rp), "s"(pbase + sd * 8192 + (i >> 1) * PSTR), "v"((tid & 63) ? OOB : 0u));
            mn[sd] = min(mn[sd], (unsigned)(tb - ta));
            ta = tb;
        }
        *ldsp(WL_OFF + (tid >> 6) * 4) = (mn[1] < mn[0]) ^ ((pbase >> 21) & 1);
        __syncthreads();
        const unsigned votes = (unsigned)__ballot(*ldsp(WL_OFF + (tid & 15) * 4) != 0) & 0xFFFF, nv = __popc(votes);
        mail = 8 | (xcc & 3) | (nv >= 8 ? 4 : 0) | (nv > VTHR && nv < 16 - VTHR ? 16 : 0);
        if (wave0) asm volatile("buffer_store_dword %0, %1, %2, 0 offen sc1" :: "v"(mail), "v"((256 + shard * 64 + tid) * 4), "s"(rw) : "memory");
        __syncthreads();
    }
    v2u mv[4];
    const unsigned gp = run * 80 + (tid >> 5) * 4;
    {
        const v4i_rs rm = mk_rsrc(m, (unsigned)E * 8);
        const unsigned o = tid < 640 ? (((gp & ~255u) | ((gp >> 2) & 63)) * 32 + (tid & 31)) * 8 : OOB;
#pragma unroll
        for (int j = 0; j < 4; j++) asm volatile("buffer_load_dwordx2 %0, %1, %2, %3 offen " NTH : "=v"(mv[j]) : "v"(o), "s"(rm), "s"(j * 64 * 32 * 8));
    }
    Grp tg; bool tv[4];
    const unsigned tbase = (unsigned)NR * (P1_GRID * CH) + run * per;
    {
        const bool in = tid * 4 < per;
#pragma unroll
        for (int k = 0; k < 4; k++) tv[k] = in && tbase + tid * 4 + k < (unsigned)T;
        const unsigned o16 = in ? tid * 16 : OOB, o8 = in ? tid * 8 : OOB;
        asm volatile("buffer_load_dwordx4 %0, %1, %2, %3 offen nt" : "=v"(tg.io) : "v"(o16), "s"(ro), "s"(tbase * 4));
        asm volatile("buffer_load_dwordx4 %0, %1, %2, %3 offen nt" : "=v"(tg.ii) : "v"(o16), "s"(ri), "s"(tbase * 4));
        asm volatile("buffer_load_dwordx2 %0, %1, %2, %3 offen nt" : "=v"(tg.an) : "v"(o8), "s"(ra), "s"(tbase * 2));
    }
    const unsigned NFULL = (unsigned)NR * P1_GRID;
    Grp g0, cur[2];
    load_grp(g0, ro, ri, ra, NR > 0 ? chunk(run) : 0x3FFFFFu, vo16, vo8);
    load_grp(cur[0], ro, ri, ra, NR > 1 ? chunk(run + P1_GRID) : 0x3FFFFFu, vo16, vo8);
    Own own;
    own.la = LB + tid * LS; own.nl = 0; own.p[0] = own.p[1] = own.p[2] = own.p[3] = 0;
    unsigned wc = ly.F0;
    unsigned base = OOB;
    auto sided = [&]() {
        const unsigned lb0 = logbase(tid >> 6, run, P_XO), sw = (((run + mail) >> 2) ^ (mail & 16 ? tid >> 6 : lb0 >> 17)) & 1;
        base = ((lb0 | (sw << 9)) + (tid & 7)) * 16;
    };
    auto await = [&]() {
        for (int i = 0; i < 4096 && !mail; i++) {
            unsigned x = 0;
            asm volatile("buffer_atomic_add %0, %1, %2, 0 offen sc0\n s_waitcnt vmcnt(0)" : "+v"(x) : "v"((tid & 63) ? OOB : box_o), "s"(rw) : "memory");
            mail = __builtin_amdgcn_readfirstlane(x);
        }
        if (mail) sided(); else { mail = 1; own.ovf |= 16384u; }
    };
    if (mail) sided();
    own.ovf = (unsigned)(size_t)(lds_u32*)L << 14;
    *ldsp(tid * 4) = 64;
#pragma unroll
    for (int i = 0; i < 8; i++) *ldsp(LB + tid * LS + i * 16 + 12) = 0;
    { const v2u z = {0, 0}; *(lds_v2u*)(size_t)(WL_OFF + tid * 8) = z; }
    if (tid < 32) *ldsp(MS_OFF + tid * 4) = 0;
    if (P_GAP) { if (run == 0) wgmode[tid] = 0; }
    else if (run == 0 && tid < 64) wgmode[(tid >> 1) * 32 + (tid & 1)] = 0;
    if (run == 1 && tid < 64) {
        v4u v = {pad_rec(tid), pad_rec(tid + 64), pad_rec(tid + 128), CZERO};
        *(v4u*)(rec + ((size_t)ly.dumu + tid) * 16) = v;
    }
    Conv cv[2];
    unsigned seen = 0;
    auto convert = [&](Grp& gp, Conv& o) {
#pragma unroll
        for (int k = 0; k < 4; k++) {
            unsigned io = k == 0 ? gp.io.x : k == 1 ? gp.io.y : k == 2 ? gp.io.z : gp.io.w;
            unsigned ii = k == 0 ? gp.ii.x : k == 1 ? gp.ii.y : k == 2 ? gp.ii.z : gp.ii.w;
            unsigned aw = (k & 2) ? gp.an.y : gp.an.x;
            float ang = (k & 1) ? hi_bf(aw) : lo_bf(aw);
            conv_rec(io, ii, ang, freq, o.A[k], o.t4[k], o.cq[k], seen);
        }
#pragma unroll
        for (int k = 0; k < 4; k++) asm volatile("" : "+v"(o.A[k]), "+v"(o.t4[k]), "+v"(o.cq[k]));
    };
    const unsigned tinc = TINC, lb7 = LB;
    Held H;
    {
        asm volatile("s_waitcnt vmcnt(9)" : "+v"(mv[0]), "+v"(mv[1]), "+v"(mv[2]), "+v"(mv[3]));
        if (tid < 640) {
            unsigned mx[4] = {0, 0, 0, 0};
            float sq[4] = {0.f, 0.f, 0.f, 0.f};
            prep_md(mv, (tid & 31) * LMAX + gp, w_down, mdp, 32 * LMAX, mx, sq);
#pragma unroll
            for (int d = 0; d < 4; d++) {
                const unsigned v = wave_scan_max(mx[d]);
                const float s = wave_scan_addf(sq[d]);
                if ((tid & 63) == 63) { *ldsp(MS_OFF + 128 + (tid >> 6) * 32 + d * 4) = v; *ldsp(MS_OFF + 128 + (tid >> 6) * 32 + 16 + d * 4) = __float_as_uint(s); }
            }
        }
        asm volatile("s_waitcnt vmcnt(3)" : "+v"(tg.io), "+v"(tg.ii), "+v"(tg.an), "+v"(fbits),
                     "+v"(g0.io), "+v"(g0.ii), "+v"(g0.an));
        asm volatile("" :: "v"(tc0));
        freq = lo_bf(fbits) * 0.15915494309189535f;
        if (T & 1) {
            const unsigned rel = (unsigned)T - 1 - (tbase + tid * 4);
            if (tv[0] && rel < 4) { const unsigned av = angle[T - 1]; if (rel == 0) tg.an.x = av; else tg.an.y = av; }
        }
        __syncthreads();
        unsigned long long vm[4];
#pragma unroll
        for (int k = 0; k < 4; k++) vm[k] = __ballot(tv[k]);
        if (vm[0]) {
            unsigned v[4];
            convert(tg, cv[1]);
            tickets_masked(v, cv[1], tinc, vm);
            place(H, v, cv[1], vm, lb7);
            if ((H.m[0] & vm[0]) | (H.m[1] & vm[1]) | (H.m[2] & vm[2]) | (H.m[3] & vm[3])) own.ovf |= 16384u;
        }
        convert(g0, cv[0]);
#pragma unroll
        for (int k = 0; k < 4; k++) { H.m[k] = 0; H.a[k] = 0; H.t[k] = 0; H.A[k] = 0; H.cd[k] = 0; }
        asm volatile("s_waitcnt vmcnt(0)" ::: "memory");
        if (P_EA && !P_EP) load_grp(cur[1], ro, ri, ra, NR > 2 ? chunk(run + 2 * P1_GRID) : 0x3FFFFFu, vo16, vo8, P_EA);
    }
    Held Q[2];
    if (P_NEW) {
#pragma unroll
        for (int k = 0; k < 8; k++) { Held& q = Q[k >> 2]; q.m[k & 3] = 0; q.a[k & 3] = 0; q.t[k & 3] = 0; q.A[k & 3] = 0; q.cd[k & 3] = 0; }
        if (NR > 0) { unsigned v[4]; tickets(v, cv[0], tinc); resolve(Q[0], v, cv[0], lb7); __syncthreads(); }
    }
    const unsigned long long vm[4] = {~0ull, ~0ull, ~0ull, ~0ull};
    unsigned ca = run + P1_GRID, cb = run + 2 * P1_GRID;
    Pend pd; pd.a0 = pd.a1 = OOB; pd.v0 = pd.v1 = v4u{0, 0, 0, 0};
    unsigned got = 0;
    int st = leader ? 1 : 0;
    if (NR == 0) { asm volatile("s_waitcnt lgkmcnt(0)" ::: "memory"); __syncthreads(); }
    const unsigned wl = WL_OFF + (tid >> 6) * 512, g8 = (tid >> 3) & 7, k16 = (tid & 7) * 16, zero = 0;
    for (int rd = 0; P_NEW && NR > 0; rd += 2) {
        bool done = false;
#pragma unroll
        for (int r = 0; r < 2; r++) {
            unsigned cnt, nfull; v2u e01; unsigned e2;
            auto rdcnt = [&]() { asm volatile("ds_read_b32 %0, %1" : "=v"(cnt) : "v"(tid * 4) : "memory"); };
            auto list = [&]() {
                asm volatile("s_waitcnt lgkmcnt(0)" : "+v"(cnt));
                const bool full = cnt >= 16384u;
                const unsigned long long bal = __ballot(full);
                nfull = __popcll(bal);
                if (full) {
                    const unsigned pos = __builtin_amdgcn_mbcnt_hi((unsigned)(bal >> 32), __builtin_amdgcn_mbcnt_lo((unsigned)bal, 0));
                    *ldsp(wl + pos * 4) = own.la;
                    cnt -= 24 * TINC;
                    *ldsp(tid * 4) = cnt;
                    own.ovf |= cnt;
                    own.nl++;
                    own.p[3] = __builtin_amdgcn_alignbit(own.p[3], own.p[2], 22); own.p[2] = __builtin_amdgcn_alignbit(own.p[2], own.p[1], 22); own.p[1] = __builtin_amdgcn_alignbit(own.p[1], own.p[0], 22);
                    own.p[0] = (own.p[0] << 10) + pos + wc;
                }
                asm volatile("ds_read2_b32 %0, %2 offset1:8\n ds_read_b32 %1, %2 offset:64" : "=&v"(e01), "=&v"(e2) : "v"(wl + g8 * 4) : "memory");
            };
            auto conv = [&](int k) {
                Conv& o = cv[0];
                const Grp& gp = cur[r];
                unsigned io = k == 0 ? gp.io.x : k == 1 ? gp.io.y : k == 2 ? gp.io.z : gp.io.w;
                unsigned ii = k == 0 ? gp.ii.x : k == 1 ? gp.ii.y : k == 2 ? gp.ii.z : gp.ii.w;
                unsigned aw = (k & 2) ? gp.an.y : gp.an.x;
                float ang = (k & 1) ? hi_bf(aw) : lo_bf(aw);
                asm volatile("" : "+v"(ang));
                conv_rec2(io, ii, ang, freq, o.A[k], o.t4[k], o.cq[k], seen);
                asm volatile("" : "+v"(o.A[k]), "+v"(o.t4[k]), "+v"(o.cq[k]), "+v"(seen));
            };
            auto vmw = [&]() { asm volatile("s_waitcnt vmcnt(%4)" : "+v"(cur[r].io), "+v"(cur[r].ii), "+v"(cur[r].an), "+v"(got) : "n"(2 + N_EA)); };
            if (P_X == 0) { held_out(Q[r ^ 1]); put(Q[r], 0); rdcnt(); vmw(); conv(0); list(); put(Q[r], 1); conv(1); put(Q[r], 2); conv(2); put(Q[r], 3); conv(3); }
            if (P_X == 1) { rdcnt(); list(); held_out(Q[r ^ 1]); put(Q[r], 0); vmw(); conv(0); put(Q[r], 1); conv(1); put(Q[r], 2); conv(2); put(Q[r], 3); conv(3); }
            if (P_X == 2) { held_out(Q[r ^ 1]); put(Q[r], 0); put(Q[r], 1); put(Q[r], 2); put(Q[r], 3); rdcnt(); vmw(); conv(0); conv(1); list(); conv(2); conv(3); }
            if (P_X == 3) { held_out(Q[r ^ 1]); rdcnt(); vmw(); conv(0); list(); put(Q[r], 0); conv(1); put(Q[r], 1); conv(2); put(Q[r], 2); conv(3); put(Q[r], 3); }
            if (wave0) {
                const unsigned g = __builtin_amdgcn_readfirstlane(got);
                unsigned nx = cb + P1_GRID;
                if (rd + r + 3 >= NSTAT) nx = st == 2 ? NSTAT * P1_GRID + g * NSH + shard : NFULL;
                if (st == 0 && g != 0) { st = 1; if (!mail) { mail = g; sided(); } }
                if (st == 0 && rd + r + 4 == NSTAT) {
                    for (int i = 0; i < 128 && st == 0; i++) {
                        unsigned x = 0;
                        asm volatile("buffer_atomic_add %0, %1, %2, 0 offen sc0\n s_waitcnt vmcnt(0)" : "+v"(x) : "v"(tid == 0 ? box_o : OOB), "s"(rw) : "memory");
                        if (__builtin_amdgcn_readfirstlane(x) != 0) st = 1;
                    }
                }
                if (st == 1 && rd + r + 4 >= NSTAT) st = 2;
                nx = cb < NFULL ? min(nx, NFULL) : NFULL;
                if ((tid & 63) == 0) { const v2u w2 = {nx, mail}; *(lds_v2u*)(size_t)(MS_OFF + 104) = w2; }
                got = st == 2 ? 1u : 0u;
                if (st != 1 && nx < NFULL)
                    asm volatile("buffer_atomic_add %0, %1, %2, 0 offen sc0" : "+v"(got) : "v"(tid == 0 ? (st == 2 ? ctr_o : box_o) : OOB), "s"(rw) : "memory");
                if (leader) asm volatile("buffer_store_dword %0, %1, %2, 0 offen sc1" :: "v"(mail), "v"(tid < 256 / NSH ? (256 + shard * (256 / NSH) + tid) * 4 : OOB), "s"(rw) : "memory");
            }
            load_grp(cur[r ^ 1], ro, ri, ra, cb < NFULL ? chunk(cb) : 0x3FFFFFu, vo16, vo8, 7 ^ P_EA);
            if (!mail) {
                mail = __builtin_amdgcn_readfirstlane(*ldsp(MS_OFF + 108));
                if (mail) sided(); else if (nfull) await();
            }
            const unsigned long long act0 = __ballot(g8 < nfull), act1 = __ballot(g8 + 8 < nfull);
            const unsigned vo = (__umul24(g8 + wc, LMUL * 16) & (LMASK * 16u)) | base;
            unsigned a0 = g8 < nfull ? vo : OOB, a1 = g8 + 8 < nfull ? vo : OOB;
            asm volatile("s_waitcnt lgkmcnt(0)" : "+v"(e01), "+v"(e2), "+v"(a0), "+v"(a1) :: "memory");
            e01.x += k16; e01.y += k16;
            asm volatile("" : "+v"(e01));
            __syncthreads();
            v2u x0, y0, x1, y1; unsigned nx, v[4];
            take_unit(x0, y0, e01.x, act0, zero);
            take_unit(x1, y1, e01.y, act1, zero);
            asm volatile("ds_read_b32 %0, %1" : "=v"(nx) : "v"(MS_OFF + 104) : "memory");
            if (nfull > 16) {
                for (unsigned p = 16; p < nfull; p += 8) {
                    if (p > 16) e2 = *ldsp(wl + (p + g8) * 4);
                    v2u x, y;
                    take_unit(x, y, e2 + k16, __ballot(g8 + p < nfull), zero);
                    asm volatile("s_waitcnt lgkmcnt(0)" : "+v"(x), "+v"(y));
                    const v4u u = {x.x, x.y, y.x, y.y};
                    __builtin_amdgcn_raw_buffer_store_b128(u, rr, g8 + p < nfull ? vo : OOB, p << (15 + 4), ST_AUX);
                }
            }
            auto lines_out = [&]() {
                const v4u u0 = {x0.x, x0.y, y0.x, y0.y}, u1 = {x1.x, x1.y, y1.x, y1.y};
                __builtin_amdgcn_raw_buffer_store_b128(u0, rr, a0, 0, ST_AUX);
                __builtin_amdgcn_raw_buffer_store_b128(u1, rr, a1, 8 << (15 + 4), ST_AUX);
                asm volatile("" ::: "memory");
            };
            wc = min(wc + nfull, 960u);
            if (ca < NFULL) tickets(v, cv[0], tinc);
            else asm volatile("s_waitcnt lgkmcnt(0)" : "=v"(v[0]), "=v"(v[1]), "=v"(v[2]), "=v"(v[3]));
            asm volatile("s_waitcnt lgkmcnt(4)" : "+v"(x0), "+v"(y0), "+v"(x1), "+v"(y1), "+v"(nx));
            lines_out();
            if (ca >= NFULL) {
                __syncthreads();
                held_out(Q[r]);
                done = true; break;
            }
            const unsigned cn = __builtin_amdgcn_readfirstlane(nx);
            if (P_EA) load_grp(cur[r], ro, ri, ra, cn < NFULL ? chunk(cn) : 0x3FFFFFu, vo16, vo8, P_EA);
            resolve(Q[r ^ 1], v, cv[0], lb7);
            __syncthreads();
            ca = cb;
            cb = ca < NFULL ? cn : NFULL;
        }
        if (done) break;
    }
    for (int rd = 0; !P_NEW && NR > 0; rd += 2) {
        bool done = false;
#pragma unroll
        for (int r = 0; r < 2; r++) {
            unsigned v[4];
            if (P_EP == 2) load_grp(cur[r ^ 1], ro, ri, ra, cb < NFULL ? chunk(cb) : 0x3FFFFFu, vo16, vo8, P_EA);
            tickets(v, cv[r & 1], tinc);
            held_out(H);
            if (P_EP == 1) load_grp(cur[r ^ 1], ro, ri, ra, cb < NFULL ? chunk(cb) : 0x3FFFFFu, vo16, vo8, P_EA);
            if (P_DF == 1) pend_out(pd, rr);
            asm volatile("s_waitcnt vmcnt(%4)" : "+v"(cur[r].io), "+v"(cur[r].ii), "+v"(cur[r].an), "+v"(got) : "n"(2 + N_EA));
            convert(cur[r], cv[(r + 1) & 1]);
            if (wave0) {
                const unsigned g = __builtin_amdgcn_readfirstlane(got);
                unsigned nx = cb + P1_GRID;
                if (rd + r + 3 >= NSTAT) nx = st == 2 ? NSTAT * P1_GRID + g * NSH + shard : NFULL;
                if (st == 0 && g != 0) { st = 1; if (!mail) { mail = g; sided(); } }
                if (st == 0 && rd + r + 4 == NSTAT) {
                    for (int i = 0; i < 128 && st == 0; i++) {
                        unsigned x = 0;
                        asm volatile("buffer_atomic_add %0, %1, %2, 0 offen sc0\n s_waitcnt vmcnt(0)" : "+v"(x) : "v"(tid == 0 ? box_o : OOB), "s"(rw) : "memory");
                        if (__builtin_amdgcn_readfirstlane(x) != 0) st = 1;
                    }
                }
                if (st == 1 && rd + r + 4 >= NSTAT) st = 2;
                nx = cb < NFULL ? min(nx, NFULL) : NFULL;
                if ((tid & 63) == 0) { const v2u w2 = {nx, mail}; *(lds_v2u*)(size_t)(MS_OFF + 104) = w2; }
                got = st == 2 ? 1u : 0u;
                if (st != 1 && nx < NFULL)
                    asm volatile("buffer_atomic_add %0, %1, %2, 0 offen sc0" : "+v"(got) : "v"(tid == 0 ? (st == 2 ? ctr_o : box_o) : OOB), "s"(rw) : "memory");
                if (leader) asm volatile("buffer_store_dword %0, %1, %2, 0 offen sc1" :: "v"(mail), "v"(tid < 256 / NSH ? (256 + shard * (256 / NSH) + tid) * 4 : OOB), "s"(rw) : "memory");
            }
            load_grp(cur[r ^ 1], ro, ri, ra, cb < NFULL ? chunk(cb) : 0x3FFFFFu, vo16, vo8, 7 ^ P_EA);
            if (P_DF == 2) { asm volatile("" ::: "memory"); pend_out(pd, rr); asm volatile("" ::: "memory"); }
            place(H, v, cv[r & 1], vm, lb7);
            if (P_DF == 3) pend_out(pd, rr);
            asm volatile("s_waitcnt lgkmcnt(0)" ::: "memory");
            __syncthreads();
            const unsigned cnt = *ldsp(tid * 4);
            const unsigned nx = *ldsp(MS_OFF + 104);
            if (!mail) {
                mail = __builtin_amdgcn_readfirstlane(*ldsp(MS_OFF + 108));
                if (mail) sided(); else if (__ballot(cnt >= 16384u)) await();
            }
            flush(own, pd, wc, cnt, tid, base, rr, P_DF == 0);
            if (P_EA && !P_EP) {
                const unsigned cn = __builtin_amdgcn_readfirstlane(nx);
                load_grp(cur[r], ro, ri, ra, cn < NFULL ? chunk(cn) : 0x3FFFFFu, vo16, vo8, P_EA);
            }
            __syncthreads();
            if (ca >= NFULL) { done = true; break; }
            ca = cb;
            cb = ca < NFULL ? __builtin_amdgcn_readfirstlane(nx) : NFULL;
        }
        if (done) break;
    }
    asm volatile("" : "+v"(got));
    if (P_DF) pend_out(pd, rr);
    unsigned tc1 = touch_code(tid);
    if (!mail) await();
    if (!P_NEW) held_out(H);
    asm volatile("s_waitcnt lgkmcnt(0)" ::: "memory");
    {
        v2u fl;
        if (tid < 64) {
            const unsigned sa = MS_OFF + 128 + (tid & 15) * 32 + (tid >> 4) * 4;
            unsigned mx = (tid & 15) < 10 ? *ldsp(sa) : 0u;
            float sq = (tid & 15) < 10 ? __uint_as_float(*ldsp(sa + 16)) : 0.f;
#pragma unroll
            for (int i = 0; i < 4; i++) {
                mx = max(mx, (unsigned)(i == 0 ? __builtin_amdgcn_update_dpp(0, (int)mx, 0x111, 0xF, 0xF, false) : i == 1 ? __builtin_amdgcn_update_dpp(0, (int)mx, 0x112, 0xF, 0xF, false)
                                        : i == 2 ? __builtin_amdgcn_update_dpp(0, (int)mx, 0x114, 0xF, 0xF, false) : __builtin_amdgcn_update_dpp(0, (int)mx, 0x118, 0xF, 0xF, false)));
                const int si = __float_as_int(sq);
                sq += __int_as_float(i == 0 ? __builtin_amdgcn_update_dpp(0, si, 0x111, 0xF, 0xF, false) : i == 1 ? __builtin_amdgcn_update_dpp(0, si, 0x112, 0xF, 0xF, false)
                                     : i == 2 ? __builtin_amdgcn_update_dpp(0, si, 0x114, 0xF, 0xF, false) : __builtin_amdgcn_update_dpp(0, si, 0x118, 0xF, 0xF, false));
            }
            mx = min(mx ? (mx >> 16) + 2 : 0u, 0x7F80u);
            fl.x = (mx << 16) | (unsigned)__builtin_amdgcn_update_dpp(0, (int)mx, 0x142, 0xA, 0xF, false);
            fl.y = (__float_as_uint(sq) & 0xFFFF0000u) | ((unsigned)__builtin_amdgcn_update_dpp(0, __float_as_int(sq), 0x142, 0xA, 0xF, false) >> 16);
        }
        if (P_GAP && tid < 20) *ldsp(MS_OFF + 128 + tid * 4) = 0;
        if (own.ovf >= 16384u || own.nl > NPOS || wc > ly.cap || (seen & 0x30000000u)) *ldsp(MS_OFF + 96) = 1;
        const unsigned tt = ((tid & 31) << 5) | (tid >> 5);
        const unsigned d = *ldsp(tt * 4) - 64;
        unsigned P = (d * 3) >> 11;
        if (P >= 24) { *ldsp(MS_OFF + 96) = 1; P = 0; }
        const unsigned nu = (P + 2) / 3;
        const unsigned inc = wave_scan_add(nu);
        if ((tid & 63) == 63) *ldsp(MS_OFF + (tid >> 6) * 4) = inc;
        __syncthreads();
        unsigned wb = (tid & 63) < (tid >> 6) ? *ldsp(MS_OFF + (tid & 15) * 4) : 0;
        wb = __builtin_amdgcn_readlane(row_scan_add(wb), 15);
        unsigned fo = wb + inc - nu;
        if (fo + nu > ly.fcap) *ldsp(MS_OFF + 96) = 1;
        const unsigned fbase = pairidx(run) * (ly.F0 * 256);
        auto piece = [&](unsigned n) { return n * 1024 + (((((run + mail) >> 2) ^ (mail & 16 ? n : (fbase + n * 1024) >> 17)) & 1) << 9); };
        {
            const unsigned n = (fo * 33223) >> 24;
            fo -= n * 505;
            if (P_GAP && (fo + nu >= 505 || tid == P1_THREADS - 1)) *ldsp(MS_OFF + 128 + n * 4) = fo + nu;
            fo = (fo + piece(n)) & 0xFFFF;
        }
        *ldsp(WL_OFF + tt * 4) = (nu << 8) | (fo << 16);
        *ldsp(tid * 4) = (fo << 8) | P;
        __syncthreads();
        {
            const v4u te = {own.p[0], own.p[1], own.p[2], (own.p[3] & 15) | (min(own.nl, 15u) << 4) | ((base >> 1) & 4096) | *ldsp(WL_OFF + tid * 4)};
            __builtin_amdgcn_raw_buffer_store_b128(te, __builtin_amdgcn_make_buffer_rsrc((void*)(tab + run * NT * 4), (short)0, NT * 16, 0x00020000), tid * 16, 0, 16);
        }
        if (tid < 64) {
            if (*ldsp(MS_OFF + 96)) fl.x = 0x7F807F80u;
            if ((tid & 31) == 31) __hip_atomic_store((unsigned long long*)(flags + ((tid >> 5) * P1_GRID + run) * 2), ((unsigned long long)fl.y << 32) | fl.x, __ATOMIC_RELAXED, __HIP_MEMORY_SCOPE_AGENT);
        }
        const unsigned k = tid & 7;
        const unsigned pa = (run & 15) * 24 + k * 3;
        const __amdgpu_buffer_rsrc_t rf = __builtin_amdgcn_make_buffer_rsrc((void*)(rec + (size_t)fbase * 16), (short)0, (int)(ly.F0 * 4096), 0x00020000);
        if (P_GAP && tid >= 768) {
            const unsigned n = (tid - 768) >> 3, x = *ldsp(MS_OFF + 128 + min(n, 19u) * 4) + k;
            const v4u v = {0, 0, 0, 0};
            const bool on = n < 20 && x > k && (x - k >= 505 ? k < 7 : ((x ^ (x - k - 1)) & 8) == 0);
            __builtin_amdgcn_raw_buffer_store_b128(v, rf, on ? (x < 512 ? piece(n) + x : piece(n + 1) + x - 512) * 16 : OOB, 0, 16);
        }
#pragma unroll 4
        for (int it = 0; it < 8; it++) {
            const unsigned u = (tid & ~63) + it * 8 + ((tid >> 3) & 7), t = ((u & 31) << 5) | (u >> 5);
            const unsigned w = *ldsp(u * 4), Pt = w & 255;
            v4u v = read_unit(LB + t * LS + k * 16);
            if (k * 3 + 1 >= Pt) { v.y = pad_rec(pa + 1); v.w |= 512u << 11; }
            if (k * 3 + 2 >= Pt) { v.z = pad_rec(pa + 2); v.w |= 512u << 22; }
            __builtin_amdgcn_raw_buffer_store_b128(v, rf, k * 3 < Pt ? ((w >> 8) + k) * 16 : OOB, 0, 16);
        }
        asm volatile("s_waitcnt vmcnt(8)" :: "v"(tc1));
    }
    WAIT_GRP(0, cur[0]); WAIT_GRP(0, cur[1]);
}

#define BIN_ARGS const int* __restrict__ idx_in, const int* __restrict__ idx_out, const unsigned short* __restrict__ angle, const unsigned short* __restrict__ ang_freq, \
    const unsigned short* __restrict__ m, const unsigned short* __restrict__ w_down, unsigned* __restrict__ mdp, int E, int T, \
    char* __restrict__ rec0, unsigned* __restrict__ tab, unsigned* __restrict__ flags, int NR, int per, Lay ly, unsigned* __restrict__ wgmode
#define BIN_PASS idx_in, idx_out, angle, ang_freq, m, w_down, mdp, E, T, rec0, tab, flags, NR, per, ly, wgmode
extern "C" __global__ __launch_bounds__(P1_THREADS) void k_bin(BIN_ARGS) {
    __shared__ __attribute__((aligned(16))) unsigned L[BIN_LDS / 4];
    bin_body<VDEF>(L, BIN_PASS);
}

struct AccArgs {
    const char* rec; const unsigned* tab; const unsigned* flags; const unsigned* mdp; float* part; unsigned* praw; unsigned* wgmode;
    const int* idx_in; const int* idx_out; const unsigned short* angle; const unsigned short* ang_freq; const unsigned short* m; const unsigned short* w_down;
    const unsigned short* dist; const unsigned short* centers; const unsigned short* w_rbf; const unsigned short* w_sbf; const unsigned short* w_up;
    unsigned short* out;
    int EP; unsigned O; int T; float c4; int E; int unused;
    Lay ly;
};
typedef char AccArgs_has_no_padding[sizeof(AccArgs) == 19 * 8 + 10 * 4 ? 1 : -1];

typedef const __attribute__((address_space(4))) unsigned* cptr;
static __device__ __forceinline__ void edge_gh(float d, const unsigned short* centers, const unsigned short* w_sbf, const unsigned short* w_rbf, float* g, float* h) {
#pragma unroll
    for (int k = 0; k < 4; k++) g[k] = h[k] = 0.f;
#pragma unroll
    for (int r = 0; r < 8; r++) {
        const unsigned cu = ((cptr)centers)[r >> 1];
        const float df = d - ((r & 1) ? hi_bf(cu) : lo_bf(cu));
        const float rb = __builtin_amdgcn_exp2f(df * df * -14.426950408889634f);
#pragma unroll
        for (int k = 0; k < 2; k++) {
            const unsigned u = ((cptr)w_sbf)[r * 2 + k], v = ((cptr)w_rbf)[r * 2 + k];
            g[2 * k] += rb * lo_bf(u); g[2 * k + 1] += rb * hi_bf(u); h[2 * k] += rb * lo_bf(v); h[2 * k + 1] += rb * hi_bf(v);
        }
    }
}
static __device__ __forceinline__ v2u edge_out(const float* agg, const float* g, const float* h, unsigned m0, unsigned m1, const unsigned* wu) {
    float a[4];
#pragma unroll
    for (int k = 0; k < 4; k++) a[k] = agg[k] * g[k];
    const float mm[4] = {lo_bf(m0), hi_bf(m0), lo_bf(m1), hi_bf(m1)};
    float o[4];
#pragma unroll
    for (int dd = 0; dd < 4; dd++) {
        float s = 0.f;
#pragma unroll
        for (int k = 0; k < 4; k++) { const unsigned u = wu[k * 2 + (dd >> 1)]; s += a[k] * ((dd & 1) ? hi_bf(u) : lo_bf(u)); }
        o[dd] = mm[dd] + s * h[dd];
    }
    v2u r;
    asm("v_cvt_pk_bf16_f32 %0, %1, %2" : "=v"(r.x) : "v"(o[0]), "v"(o[1]));
    asm("v_cvt_pk_bf16_f32 %0, %1, %2" : "=v"(r.y) : "v"(o[2]), "v"(o[3]));
    return r;
}

typedef const __attribute__((address_space(4))) AccArgs* kaptr;
static __device__ __forceinline__ void acc_fallback(unsigned* lds, unsigned mode, const __attribute__((address_space(4))) AccArgs& a_, int tid, int b, int qt, int cp) {
    const char* rec = rec_align(a_.rec); const unsigned* flags = a_.flags; const unsigned* mdp = a_.mdp;
    const int EP = a_.EP, T = a_.T;
    const Lay ly = {a_.ly.F0, a_.ly.cap, a_.ly.dumu, a_.ly.fcap};
    const int lane = tid & 63;
    const unsigned O = a_.O;
    float* pout0 = a_.part + (size_t)(qt * 4 + cp * 2) * EP + (size_t)b * O;
    const unsigned G = tid >> 3, k = tid & 7;
    {
        unsigned* praw = a_.praw + (size_t)(qt * 2 + cp) * EP + (size_t)b * O;
        for (unsigned i = tid; i < O; i += P2_THREADS) praw[i] = 0;
    }
    if (mode == 1) {
        unsigned* acc = lds + OBASE;
        const float Xm = 2139095040.0f / (float)(NWS * (NPOS * 24 + 24)) - 2.0f;
#pragma unroll 1
        for (int c = 0; c < 2; c++) {
            __syncthreads();
            if (tid == 0) lds[0] = 0;
            __syncthreads();
            if (tid < NWS) { unsigned v = wave_scan_max((flags[(cp * P1_GRID + tid) * 2] >> (16 * c)) & 0xFFFF); if (lane == 63) atomicMax(&lds[0], v); }
            __syncthreads();
            unsigned mbits = __builtin_amdgcn_readfirstlane(lds[0]);
            if (mbits == 0) mbits = 0x3f80;
            const float M = __uint_as_float(mbits << 16);
            const float S = Xm / (511.0f * M), inv = M / Xm;
            float racc[20];
#pragma unroll
            for (int r = 0; r < 20; r++) racc[r] = 0.f;
#pragma unroll 1
            for (int s = 0; s < 8; s++) {
                __syncthreads();
                const unsigned* src = mdp + (size_t)cp * (32 * LMAX) + (size_t)(qt * 8 + s) * LMAX;
                for (unsigned i = tid; i < LMAX; i += P2_THREADS) {
                    unsigned w = src[i];
                    lds[(i & ~255u) | ((i & 3) << 6) | ((i >> 2) & 63)] = __float_as_uint((c ? __uint_as_float(w) : lo_bf(w)) * S);
                }
                for (int i = tid; i < OBASE; i += P2_THREADS) acc[i] = 0;
                __syncthreads();
                const unsigned t = b * 32 + qt * 8 + s;
                for (unsigned r = G; r < NWS; r += 128) {
                    v4u te = ((const v4u*)a_.tab)[r * NT + t];
                    const unsigned tw = te.w, nl = (tw >> 4) & 15, nu = (tw >> 8) & 15;
                    for (unsigned l = 0; l <= nl; l++) {
                        unsigned u;
                        if (l < nl) {
                            u = (((te.x & 1023) * LMUL) & LMASK) + logbase(t >> 6, r, P_XO) + ((tw >> 3) & 512) + k;
                            te.x = __builtin_amdgcn_alignbit(te.y, te.x, 10); te.y = __builtin_amdgcn_alignbit(te.z, te.y, 10); te.z = __builtin_amdgcn_alignbit(te.w, te.z, 10);
                        }
                        else { if (k >= nu) break; u = pairidx(r) * (ly.F0 * 256) + (tw >> 16) + k; }
                        v4u q = *(const v4u*)(rec + (size_t)u * 16);
                        unsigned a[3] = {q.x, q.y, q.z};
#pragma unroll
                        for (int j = 0; j < 3; j++) {
                            const int cq = (int)((q.w >> (j == 0 ? 1 : j == 1 ? 11 : 22)) & 1023) - 512;
                            const int x = __float2int_rn(__uint_as_float(lds[(a[j] >> 2) & 0x7FFF]) * (float)cq);
                            if ((a[j] >> 17) - IDXB < OBASE) atomicAdd(&acc[(a[j] >> 17) - IDXB], (unsigned)x);
                        }
                    }
                }
                __syncthreads();
#pragma unroll
                for (int r = 0; r < 20; r++) racc[r] += (float)(int)acc[r * P2_THREADS + tid] * inv;
            }
#pragma unroll
            for (int r = 0; r < 20; r++) if ((unsigned)(r * P2_THREADS + tid) < O) pout0[(size_t)c * EP + r * P2_THREADS + tid] = racc[r];
        }
    }
    if (mode == 2) {
        float* facc = (float*)lds;
        const float freq = bf2f(a_.ang_freq[0]);
        float wd[2][4];
#pragma unroll
        for (int kk = 0; kk < 4; kk++) { wd[0][kk] = bf2f(a_.w_down[kk * 4 + cp * 2]); wd[1][kk] = bf2f(a_.w_down[kk * 4 + cp * 2 + 1]); }
        __syncthreads();
        for (int i = tid; i < 2 * OBASE; i += P2_THREADS) facc[i] = 0.f;
        __syncthreads();
        for (int t = tid; t < T; t += P2_THREADS) {
            const unsigned io = a_.idx_out[t], ii = a_.idx_in[t];
            if (((io >> 7) & 31) == (unsigned)b && ((ii >> 3) & 3) == (unsigned)qt) {
                const v2u mv = ((const v2u*)a_.m)[ii];
                const float a[4] = {lo_bf(mv.x), hi_bf(mv.x), lo_bf(mv.y), hi_bf(mv.y)};
                const float cs = __cosf(freq * bf2f(a_.angle[t]));
                const unsigned ol = (io & 127u) | ((io >> 5) & ~127u);
#pragma unroll
                for (int c = 0; c < 2; c++)
                    atomicAdd(&facc[c * OBASE + ol], (a[0] * wd[c][0] + a[1] * wd[c][1] + a[2] * wd[c][2] + a[3] * wd[c][3]) * cs);
            }
        }
        __syncthreads();
        for (unsigned idx = tid; idx < O; idx += P2_THREADS) { pout0[idx] = facc[idx]; pout0[(size_t)EP + idx] = facc[OBASE + idx]; }
    }
}

#define SWAPX 2
#define RAUX ""
#define J0 (wave >> 1)
#define IDXMASK "0x8fffffff"
#define STR2(x) #x
#define STR(x) STR2(x)
#define PLAN_IT 160

extern "C" __global__ __launch_bounds__(P2_THREADS) void k_acc(AccArgs a_) {
    constexpr int AV = 8;
    const Lay ly = a_.ly;
    __shared__ __attribute__((aligned(16))) unsigned lds[2 * OBASE];
    const int tid = threadIdx.x;
    const int wave = __builtin_amdgcn_readfirstlane(tid >> 6), lane = tid & 63;
    const int xcd = blockIdx.x & 7, jj = blockIdx.x >> 3;
    const int cp = jj & 1;
    const int qt = (AV & 3) == 0 ? xcd >> 1 : (AV & 3) == 1 ? (jj >> 1) & 3 : (xcd & 1) * 2 + ((jj >> 1) & 1);
    const int b = (AV & 3) == 0 ? (xcd & 1) * 16 + (jj >> 1) : (AV & 3) == 1 ? xcd * 4 + (jj >> 3) : (xcd >> 1) * 8 + (jj >> 2);
    const unsigned O = a_.O;
    unsigned stv = 0;
    unsigned tc2 = touch_code2(tid);
    unsigned* praw = a_.praw + (size_t)(qt * 2 + cp) * a_.EP + (size_t)b * O;
    const unsigned* mds = a_.mdp + (size_t)cp * (32 * LMAX) + (size_t)(qt * 8) * LMAX;
    unsigned mode = (unsigned)(size_t)lds ? 2u : 0u;
    unsigned exs = 0, bad = 0;
    v4u mm[2], dd;
    const int q = qt * 2 + cp;
    unsigned t16 = tid * 16;
    if (mode == 0) {
        const unsigned w = wave * 16 + ((lane & 15) ^ (cp ? SWAPX : 0)), t0 = b * 32 + qt * 8 + (lane >> 4) * 2;
        v4i_rs rs = mk_rsrc(rec_align(a_.rec), ly.dumu + 64);
        rs.y |= 16 << 16;
        const v4i_rs rm = mk_rsrc(mds, 8 * LMAX * 4);
        const v4i_rs rp0 = mk_rsrc(praw, O * 4);
        const v4i_rs rt = mk_rsrc(a_.tab, (NT * NWS * 4 + FL_SIZE) * 4);
        const v4i_rs rmm = mk_rsrc(a_.m, a_.E * 8), rd = mk_rsrc(a_.dist, a_.E * 2);
        asm volatile(
            "s_setprio 3\n"
            "buffer_load_dwordx4 v[32:35], %[tof], %[rt], 0 offen\n"
            "buffer_load_dwordx4 v[36:39], %[tof], %[rt], 0 offen offset:16\n"
            "v_and_b32 v10, 1023, %[tid16]\n"
            "s_load_dword s80, %[w0], 0\n s_load_dword s81, %[w1], 0\n s_load_dword s93, %[w2], 0\n s_load_dword s97, %[w3], 0\n"
            "s_movk_i32 s74, 16384\n s_mov_b32 s75, 32768\n s_mov_b32 s79, 49152\n s_mov_b32 s92, 65536\n"
            "v_bfrev_b32 v12, 1\n"
            "v_add_u32 v11, %[pb], v10\n"
            "v_bfrev_b32 v13, 1\n"
            "v_bfrev_b32 v14, 1\n"
            "v_bfrev_b32 v15, 1\n"
            "ds_write_b128 v11, v[12:15] offset:0\n"
            "ds_write_b128 v11, v[12:15] offset:1024\n"
            "ds_write_b128 v11, v[12:15] offset:2048\n"
            "ds_write_b128 v11, v[12:15] offset:3072\n"
            "ds_write_b128 v11, v[12:15] offset:4096\n"
            "v_bfrev_b32 v80, 1\n"
            "v_mov_b32 v84, 0xb9000000\n"
            "v_mov_b32 v88, 0\n"
            "v_add_u32 v89, 81920, %[tid16]\n"
            "v_add_u32 v90, 65536, v89\n"
            "v_lshrrev_b32 v16, 4, v10\n"
            "v_add_u32 v83, %[dumu], v16\n"
            "v_and_b32 v17, 7, v16\n"
            "v_lshl_or_b32 v82, v17, 28, v17\n"
            "v_lshrrev_b32 v18, 3, v16\n"
            "v_lshl_add_u32 v17, v17, 3, v18\n"
            "v_lshlrev_b32 v17, 2, v17\n"
            "v_add_u32 v17, %[pb], v17\n"
            "s_getpc_b64 s[70:71]\n"
            "Lpc%=:\n"
            "s_add_u32 s72, s70, Lchg2%=-Lpc%=\n s_addc_u32 s73, s71, 0\n"
            "s_add_u32 s66, s70, Lchg3%=-Lpc%=\n s_addc_u32 s67, s71, 0\n"
            "s_add_u32 s70, s70, Lchg1%=-Lpc%=\n s_addc_u32 s71, s71, 0\n"
            "s_waitcnt vmcnt(0)\n"
            "buffer_load_dwordx4 v[92:95], v10, %[rt], %[fof] offen\n"
            "buffer_load_dwordx4 v[96:99], v10, %[rt], %[fof] offen offset:1024\n"
            "s_mov_b32 s94, 0\n"
            "buffer_load_dwordx4 v[40:43], %[tid16], %[rm], s94 offen\n"
            "s_add_u32 s96, s94, 16384\n"
            "buffer_load_dwordx4 v[44:47], %[tid16], %[rm], s96 offen\n"
            "s_add_u32 s96, s94, 32768\n"
            "buffer_load_dwordx4 v[48:51], %[tid16], %[rm], s96 offen\n"
            "s_add_u32 s96, s94, 49152\n"
            "buffer_load_dwordx4 v[52:55], %[tid16], %[rm], s96 offen\n"
            "s_add_u32 s96, s94, 65536\n"
            "buffer_load_dwordx4 v[56:59], %[tid16], %[rm], s96 offen\n"
            "v_bfe_u32 v20, v35, 4, 4\n v_bfe_u32 v21, v39, 4, 4\n v_bfe_u32 v22, v35, 8, 4\n v_bfe_u32 v23, v39, 8, 4\n"
            "v_min_u32 v24, 1, v22\n v_min_u32 v25, 1, v23\n v_add_u32 v24, v24, v20\n v_add_u32 v25, v25, v21\n"
            "v_lshl_or_b32 v24, v25, 16, v24\n"
            "v_lshrrev_b32 v8, 16, v35\n v_lshrrev_b32 v9, 16, v39\n"
            "v_add_u32_dpp v25, v24, v24 row_shr:1 row_mask:0xf bank_mask:0xf bound_ctrl:1\n"
            "v_add_u32 v8, %[fb], v8\n v_add_u32 v9, %[fb], v9\n"
            "v_add_u32_dpp v25, v25, v25 row_shr:2 row_mask:0xf bank_mask:0xf bound_ctrl:1\n"
            "v_sub_u32 v26, 8, v22\n v_sub_u32 v27, 8, v23\n"
            "v_add_u32_dpp v25, v25, v25 row_shr:4 row_mask:0xf bank_mask:0xf bound_ctrl:1\n"
            "v_lshl_or_b32 v8, v26, 28, v8\n v_lshl_or_b32 v9, v27, 28, v9\n"
            "v_add_u32_dpp v25, v25, v25 row_shr:8 row_mask:0xf bank_mask:0xf bound_ctrl:1\n"
            "s_nop 1\n"
            "v_readlane_b32 s84, v25, 15\n"
            "v_readlane_b32 s85, v25, 31\n"
            "v_readlane_b32 s86, v25, 47\n"
            "v_readlane_b32 s87, v25, 63\n"
            "v_sub_u32 v25, v25, v24\n"
            "s_mov_b32 s96, 0\n s_mov_b64 s[76:77], 0\n"
            "s_add_u32 s84, s84, 0x70007\n"
            "s_bfe_u32 s88, s84, 0xd0003\n s_lshr_b32 s89, s84, 19\n"
            "s_max_u32 s88, s88, 8\n s_max_u32 s89, s89, 8\n"
            "s_add_u32 s90, s96, s88\n"
            "s_pack_ll_b32_b16 s91, s96, s90\n"
            "s_add_u32 s96, s90, s89\n"
            "s_lshl_b32 s89, s89, 8\n s_or_b32 s88, s88, s89\n s_and_b32 s88, s88, 0xffff\n"
            "s_or_b32 s76, s76, s88\n"
            "s_lshl_b32 s91, s91, 3\n"
            "v_mov_b32 v26, s91\n"
            "s_add_u32 s85, s85, 0x70007\n"
            "s_bfe_u32 s88, s85, 0xd0003\n s_lshr_b32 s89, s85, 19\n"
            "s_max_u32 s88, s88, 8\n s_max_u32 s89, s89, 8\n"
            "s_add_u32 s90, s96, s88\n"
            "s_pack_ll_b32_b16 s91, s96, s90\n"
            "s_add_u32 s96, s90, s89\n"
            "s_lshl_b32 s89, s89, 8\n s_or_b32 s88, s88, s89\n s_and_b32 s88, s88, 0xffff\n"
            "s_lshl_b32 s88, s88, 16\n"
            "s_or_b32 s76, s76, s88\n"
            "s_lshl_b32 s91, s91, 3\n"
            "s_mov_b32 exec_lo, 0xffff0000\n s_mov_b32 exec_hi, 0xffffffff\n"
            "v_mov_b32 v26, s91\n"
            "s_add_u32 s86, s86, 0x70007\n"
            "s_bfe_u32 s88, s86, 0xd0003\n s_lshr_b32 s89, s86, 19\n"
            "s_max_u32 s88, s88, 8\n s_max_u32 s89, s89, 8\n"
            "s_add_u32 s90, s96, s88\n"
            "s_pack_ll_b32_b16 s91, s96, s90\n"
            "s_add_u32 s96, s90, s89\n"
            "s_lshl_b32 s89, s89, 8\n s_or_b32 s88, s88, s89\n s_and_b32 s88, s88, 0xffff\n"
            "s_or_b32 s77, s77, s88\n"
            "s_lshl_b32 s91, s91, 3\n"
            "s_mov_b32 exec_lo, 0x0\n s_mov_b32 exec_hi, 0xffffffff\n"
            "v_mov_b32 v26, s91\n"
            "s_add_u32 s87, s87, 0x70007\n"
            "s_bfe_u32 s88, s87, 0xd0003\n s_lshr_b32 s89, s87, 19\n"
            "s_max_u32 s88, s88, 8\n s_max_u32 s89, s89, 8\n"
            "s_add_u32 s90, s96, s88\n"
            "s_pack_ll_b32_b16 s91, s96, s90\n"
            "s_add_u32 s96, s90, s89\n"
            "s_lshl_b32 s89, s89, 8\n s_or_b32 s88, s88, s89\n s_and_b32 s88, s88, 0xffff\n"
            "s_lshl_b32 s88, s88, 16\n"
            "s_or_b32 s77, s77, s88\n"
            "s_lshl_b32 s91, s91, 3\n"
            "s_mov_b32 exec_lo, 0x0\n s_mov_b32 exec_hi, 0xffff0000\n"
            "v_mov_b32 v26, s91\n"
            "s_mov_b64 exec, -1\n"
            "s_mov_b32 %[bad], 0\n"
            "s_cmp_gt_u32 s96, " STR(PLAN_IT) "\n"
            "s_cbranch_scc0 1f\n"
            "s_mov_b32 %[bad], 1\n s_mov_b32 s76, 0x08080808\n s_mov_b32 s77, 0x08080808\n"
            "s_branch Lnoplan%=\n"
            "1:\n"
            "v_add_u32 v25, v25, v26\n"
            "v_and_b32 v26, 0xffff, v25\n v_lshrrev_b32 v27, 16, v25\n"
            "v_lshlrev_b32 v26, 2, v26\n v_lshlrev_b32 v27, 2, v27\n"
            "v_add_u32 v26, %[pb], v26\n v_add_u32 v27, %[pb], v27\n"
            "v_lshl_add_u32 v28, v20, 2, v26\n v_lshl_add_u32 v29, v21, 2, v27\n"
            "v_cmp_ne_u32 vcc, 0, v22\n v_cmp_ne_u32 s[88:89], 0, v23\n"
            "v_lshrrev_b32 v30, 3, v35\n v_and_b32 v30, 0x200, v30\n v_or_b32 v30, %[xb], v30\n"
            "s_mov_b64 exec, vcc\n ds_write_b32 v28, v8\n s_mov_b64 exec, s[88:89]\n ds_write_b32 v29, v9\n s_mov_b64 exec, -1\n"
            "v_min_u32 v24, v20, v21\n v_mov_b32 v31, %[lmask]\n s_movk_i32 s91, 1023\n"
            "v_cmp_gt_u32 vcc, 4, v24\n"
            "s_cmp_eq_u64 vcc, 0\n s_cbranch_scc1 4f\n"
            "v_cmp_lt_u32 vcc, 0, v20\n v_cmp_lt_u32 s[88:89], 0, v21\n"
            "v_bfe_u32 v28, v32, 0, 10\n"
            "v_bfe_u32 v29, v36, 0, 10\n"
            "s_or_b64 s[84:85], vcc, s[88:89]\n"
            "s_cbranch_scc0 Lnoplan%=\n"
            "v_mul_u32_u24 v28, %[lmul], v28\n"
            "v_mul_u32_u24 v29, %[lmul], v29\n"
            "v_and_or_b32 v28, v28, v31, v30\n"
            "v_and_or_b32 v29, v29, v31, v30\n"
            "s_mov_b64 exec, vcc\n ds_write_b32 v26, v28 offset:0\n s_mov_b64 exec, s[88:89]\n ds_write_b32 v27, v29 offset:0\n s_mov_b64 exec, -1\n"
            "v_cmp_lt_u32 vcc, 1, v20\n v_cmp_lt_u32 s[88:89], 1, v21\n"
            "v_bfe_u32 v28, v32, 10, 10\n"
            "v_bfe_u32 v29, v36, 10, 10\n"
            "s_or_b64 s[84:85], vcc, s[88:89]\n"
            "s_cbranch_scc0 Lnoplan%=\n"
            "v_mul_u32_u24 v28, %[lmul], v28\n"
            "v_mul_u32_u24 v29, %[lmul], v29\n"
            "v_and_or_b32 v28, v28, v31, v30\n"
            "v_and_or_b32 v29, v29, v31, v30\n"
            "s_mov_b64 exec, vcc\n ds_write_b32 v26, v28 offset:4\n s_mov_b64 exec, s[88:89]\n ds_write_b32 v27, v29 offset:4\n s_mov_b64 exec, -1\n"
            "v_cmp_lt_u32 vcc, 2, v20\n v_cmp_lt_u32 s[88:89], 2, v21\n"
            "v_bfe_u32 v28, v32, 20, 10\n"
            "v_bfe_u32 v29, v36, 20, 10\n"
            "s_or_b64 s[84:85], vcc, s[88:89]\n"
            "s_cbranch_scc0 Lnoplan%=\n"
            "v_mul_u32_u24 v28, %[lmul], v28\n"
            "v_mul_u32_u24 v29, %[lmul], v29\n"
            "v_and_or_b32 v28, v28, v31, v30\n"
            "v_and_or_b32 v29, v29, v31, v30\n"
            "s_mov_b64 exec, vcc\n ds_write_b32 v26, v28 offset:8\n s_mov_b64 exec, s[88:89]\n ds_write_b32 v27, v29 offset:8\n s_mov_b64 exec, -1\n"
            "v_cmp_lt_u32 vcc, 3, v20\n v_cmp_lt_u32 s[88:89], 3, v21\n"
            "v_alignbit_b32 v28, v33, v32, 30\n"
            "v_and_b32 v28, s91, v28\n"
            "v_alignbit_b32 v29, v37, v36, 30\n"
            "v_and_b32 v29, s91, v29\n"
            "s_or_b64 s[84:85], vcc, s[88:89]\n"
            "s_cbranch_scc0 Lnoplan%=\n"
            "v_mul_u32_u24 v28, %[lmul], v28\n"
            "v_mul_u32_u24 v29, %[lmul], v29\n"
            "v_and_or_b32 v28, v28, v31, v30\n"
            "v_and_or_b32 v29, v29, v31, v30\n"
            "s_mov_b64 exec, vcc\n ds_write_b32 v26, v28 offset:12\n s_mov_b64 exec, s[88:89]\n ds_write_b32 v27, v29 offset:12\n s_mov_b64 exec, -1\n"
            "s_branch 5f\n"
            "4:\n"
            "v_bfe_u32 v12, v32, 0, 10\n"
            "v_bfe_u32 v13, v32, 10, 10\n"
            "v_bfe_u32 v14, v36, 0, 10\n"
            "v_bfe_u32 v15, v36, 10, 10\n"
            "v_mul_u32_u24 v12, %[lmul], v12\n"
            "v_mul_u32_u24 v13, %[lmul], v13\n"
            "v_mul_u32_u24 v14, %[lmul], v14\n"
            "v_mul_u32_u24 v15, %[lmul], v15\n"
            "v_and_or_b32 v12, v12, v31, v30\n"
            "v_and_or_b32 v13, v13, v31, v30\n"
            "v_and_or_b32 v14, v14, v31, v30\n"
            "v_and_or_b32 v15, v15, v31, v30\n"
            "ds_write2_b32 v26, v12, v13 offset0:0 offset1:1\n ds_write2_b32 v27, v14, v15 offset0:0 offset1:1\n"
            "v_bfe_u32 v12, v32, 20, 10\n"
            "v_alignbit_b32 v13, v33, v32, 30\n"
            "v_and_b32 v13, s91, v13\n"
            "v_bfe_u32 v14, v36, 20, 10\n"
            "v_alignbit_b32 v15, v37, v36, 30\n"
            "v_and_b32 v15, s91, v15\n"
            "v_mul_u32_u24 v12, %[lmul], v12\n"
            "v_mul_u32_u24 v13, %[lmul], v13\n"
            "v_mul_u32_u24 v14, %[lmul], v14\n"
            "v_mul_u32_u24 v15, %[lmul], v15\n"
            "v_and_or_b32 v12, v12, v31, v30\n"
            "v_and_or_b32 v13, v13, v31, v30\n"
            "v_and_or_b32 v14, v14, v31, v30\n"
            "v_and_or_b32 v15, v15, v31, v30\n"
            "ds_write2_b32 v26, v12, v13 offset0:2 offset1:3\n ds_write2_b32 v27, v14, v15 offset0:2 offset1:3\n"
            "5:\n"
            "v_cmp_lt_u32 vcc, 4, v20\n v_cmp_lt_u32 s[88:89], 4, v21\n"
            "v_bfe_u32 v28, v33, 8, 10\n"
            "v_bfe_u32 v29, v37, 8, 10\n"
            "s_or_b64 s[84:85], vcc, s[88:89]\n"
            "s_cbranch_scc0 Lnoplan%=\n"
            "v_mul_u32_u24 v28, %[lmul], v28\n"
            "v_mul_u32_u24 v29, %[lmul], v29\n"
            "v_and_or_b32 v28, v28, v31, v30\n"
            "v_and_or_b32 v29, v29, v31, v30\n"
            "s_mov_b64 exec, vcc\n ds_write_b32 v26, v28 offset:16\n s_mov_b64 exec, s[88:89]\n ds_write_b32 v27, v29 offset:16\n s_mov_b64 exec, -1\n"
            "v_cmp_lt_u32 vcc, 5, v20\n v_cmp_lt_u32 s[88:89], 5, v21\n"
            "v_bfe_u32 v28, v33, 18, 10\n"
            "v_bfe_u32 v29, v37, 18, 10\n"
            "s_or_b64 s[84:85], vcc, s[88:89]\n"
            "s_cbranch_scc0 Lnoplan%=\n"
            "v_mul_u32_u24 v28, %[lmul], v28\n"
            "v_mul_u32_u24 v29, %[lmul], v29\n"
            "v_and_or_b32 v28, v28, v31, v30\n"
            "v_and_or_b32 v29, v29, v31, v30\n"
            "s_mov_b64 exec, vcc\n ds_write_b32 v26, v28 offset:20\n s_mov_b64 exec, s[88:89]\n ds_write_b32 v27, v29 offset:20\n s_mov_b64 exec, -1\n"
            "v_cmp_lt_u32 vcc, 6, v20\n v_cmp_lt_u32 s[88:89], 6, v21\n"
            "v_alignbit_b32 v28, v34, v33, 28\n"
            "v_and_b32 v28, s91, v28\n"
            "v_alignbit_b32 v29, v38, v37, 28\n"
            "v_and_b32 v29, s91, v29\n"
            "s_or_b64 s[84:85], vcc, s[88:89]\n"
            "s_cbranch_scc0 Lnoplan%=\n"
            "v_mul_u32_u24 v28, %[lmul], v28\n"
            "v_mul_u32_u24 v29, %[lmul], v29\n"
            "v_and_or_b32 v28, v28, v31, v30\n"
            "v_and_or_b32 v29, v29, v31, v30\n"
            "s_mov_b64 exec, vcc\n ds_write_b32 v26, v28 offset:24\n s_mov_b64 exec, s[88:89]\n ds_write_b32 v27, v29 offset:24\n s_mov_b64 exec, -1\n"
            "v_cmp_lt_u32 vcc, 7, v20\n v_cmp_lt_u32 s[88:89], 7, v21\n"
            "v_bfe_u32 v28, v34, 6, 10\n"
            "v_bfe_u32 v29, v38, 6, 10\n"
            "s_or_b64 s[84:85], vcc, s[88:89]\n"
            "s_cbranch_scc0 Lnoplan%=\n"
            "v_mul_u32_u24 v28, %[lmul], v28\n"
            "v_mul_u32_u24 v29, %[lmul], v29\n"
            "v_and_or_b32 v28, v28, v31, v30\n"
            "v_and_or_b32 v29, v29, v31, v30\n"
            "s_mov_b64 exec, vcc\n ds_write_b32 v26, v28 offset:28\n s_mov_b64 exec, s[88:89]\n ds_write_b32 v27, v29 offset:28\n s_mov_b64 exec, -1\n"
            "v_cmp_lt_u32 vcc, 8, v20\n v_cmp_lt_u32 s[88:89], 8, v21\n"
            "v_bfe_u32 v28, v34, 16, 10\n"
            "v_bfe_u32 v29, v38, 16, 10\n"
            "s_or_b64 s[84:85], vcc, s[88:89]\n"
            "s_cbranch_scc0 Lnoplan%=\n"
            "v_mul_u32_u24 v28, %[lmul], v28\n"
            "v_mul_u32_u24 v29, %[lmul], v29\n"
            "v_and_or_b32 v28, v28, v31, v30\n"
            "v_and_or_b32 v29, v29, v31, v30\n"
            "s_mov_b64 exec, vcc\n ds_write_b32 v26, v28 offset:32\n s_mov_b64 exec, s[88:89]\n ds_write_b32 v27, v29 offset:32\n s_mov_b64 exec, -1\n"
            "v_cmp_lt_u32 vcc, 9, v20\n v_cmp_lt_u32 s[88:89], 9, v21\n"
            "v_alignbit_b32 v28, v35, v34, 26\n"
            "v_and_b32 v28, s91, v28\n"
            "v_alignbit_b32 v29, v39, v38, 26\n"
            "v_and_b32 v29, s91, v29\n"
            "s_or_b64 s[84:85], vcc, s[88:89]\n"
            "s_cbranch_scc0 Lnoplan%=\n"
            "v_mul_u32_u24 v28, %[lmul], v28\n"
            "v_mul_u32_u24 v29, %[lmul], v29\n"
            "v_and_or_b32 v28, v28, v31, v30\n"
            "v_and_or_b32 v29, v29, v31, v30\n"
            "s_mov_b64 exec, vcc\n ds_write_b32 v26, v28 offset:36\n s_mov_b64 exec, s[88:89]\n ds_write_b32 v27, v29 offset:36\n s_mov_b64 exec, -1\n"
            "Lnoplan%=:\n"
            "ds_read_b32 v60, v17 offset:0\n"
            "ds_read_b32 v61, v17 offset:256\n"
            "ds_read_b32 v62, v17 offset:512\n"
            "ds_read_b32 v63, v17 offset:768\n"
            "ds_read_b32 v64, v17 offset:1024\n"
            "ds_read_b32 v65, v17 offset:1280\n"
            "ds_read_b32 v66, v17 offset:1536\n"
            "ds_read_b32 v67, v17 offset:1792\n"
            "ds_read_b32 v68, v17 offset:2048\n"
            "ds_read_b32 v69, v17 offset:2304\n"
            "ds_read_b32 v70, v17 offset:2560\n"
            "ds_read_b32 v71, v17 offset:2816\n"
            "ds_read_b32 v72, v17 offset:3072\n"
            "ds_read_b32 v73, v17 offset:3328\n"
            "ds_read_b32 v74, v17 offset:3584\n"
            "ds_read_b32 v75, v17 offset:3840\n"
            "ds_read_b32 v76, v17 offset:4096\n"
            "ds_read_b32 v77, v17 offset:4352\n"
            "ds_read_b32 v78, v17 offset:4608\n"
            "ds_read_b32 v79, v17 offset:4864\n"
            "v_mov_b32 v100, 0\n"
            "v_mov_b32 v101, 0\n"
            "v_mov_b32 v102, 0\n"
            "v_mov_b32 v103, 0\n"
            "v_mov_b32 v107, v11\n"
            "s_mov_b32 s82, 0\n"
            "s_waitcnt lgkmcnt(15)\n"
            "v_mov_b32 v81, v60\n"
            "s_nop 1\n"
            "v_add_u32_dpp v111, v81, v82 row_newbcast:0 row_mask:0xf bank_mask:0x3\n"
            "v_add_u32_dpp v111, v81, v82 row_newbcast:8 row_mask:0xf bank_mask:0xc\n"
            "v_and_b32 v111, " IDXMASK ", v111\n"
            "v_min_u32 v111, v111, v83\n"
            "buffer_load_dwordx4 v[8:11], v111, %[rs], 0 idxen " RAUX "\n"
            "v_add_u32_dpp v111, v81, v82 row_newbcast:1 row_mask:0xf bank_mask:0x3\n"
            "v_add_u32_dpp v111, v81, v82 row_newbcast:9 row_mask:0xf bank_mask:0xc\n"
            "v_and_b32 v111, " IDXMASK ", v111\n"
            "v_min_u32 v111, v111, v83\n"
            "buffer_load_dwordx4 v[12:15], v111, %[rs], 0 idxen " RAUX "\n"
            "v_add_u32_dpp v111, v81, v82 row_newbcast:2 row_mask:0xf bank_mask:0x3\n"
            "v_add_u32_dpp v111, v81, v82 row_newbcast:10 row_mask:0xf bank_mask:0xc\n"
            "v_and_b32 v111, " IDXMASK ", v111\n"
            "v_min_u32 v111, v111, v83\n"
            "buffer_load_dwordx4 v[16:19], v111, %[rs], 0 idxen " RAUX "\n"
            "s_setprio 0\n"
            "ds_write_b128 v107, v[100:103] offset:0\n"
            "ds_write_b128 v107, v[100:103] offset:1024\n"
            "ds_write_b128 v107, v[100:103] offset:2048\n"
            "ds_write_b128 v107, v[100:103] offset:3072\n"
            "ds_write_b128 v107, v[100:103] offset:4096\n"
            "s_waitcnt vmcnt(8)\n"
            "v_pk_max_u16 v92, v92, v94\n v_pk_max_u16 v96, v96, v98\n"
            "v_lshlrev_b32 v104, 16, v93\n v_lshlrev_b32 v105, 16, v95\n v_lshlrev_b32 v106, 16, v97\n v_lshlrev_b32 v108, 16, v99\n"
            "v_pk_max_u16 v92, v92, v96\n"
            "v_add_f32 v104, v104, v105\n v_add_f32 v106, v106, v108\n v_add_f32 v93, v93, v95\n v_add_f32 v97, v97, v99\n"
            "v_and_b32 v94, 0xffff, v92\n v_lshrrev_b32 v92, 16, v92\n"
            "v_add_f32 v104, v104, v106\n v_add_f32 v93, v93, v97\n"
            "s_nop 0\n"
            "v_max_u32_dpp v94, v94, v94 row_shr:1 row_mask:0xf bank_mask:0xf bound_ctrl:1\n"
            "v_max_u32_dpp v92, v92, v92 row_shr:1 row_mask:0xf bank_mask:0xf bound_ctrl:1\n"
            "v_add_f32_dpp v104, v104, v104 row_shr:1 row_mask:0xf bank_mask:0xf bound_ctrl:1\n"
            "v_add_f32_dpp v93, v93, v93 row_shr:1 row_mask:0xf bank_mask:0xf bound_ctrl:1\n"
            "v_max_u32_dpp v94, v94, v94 row_shr:2 row_mask:0xf bank_mask:0xf bound_ctrl:1\n"
            "v_max_u32_dpp v92, v92, v92 row_shr:2 row_mask:0xf bank_mask:0xf bound_ctrl:1\n"
            "v_add_f32_dpp v104, v104, v104 row_shr:2 row_mask:0xf bank_mask:0xf bound_ctrl:1\n"
            "v_add_f32_dpp v93, v93, v93 row_shr:2 row_mask:0xf bank_mask:0xf bound_ctrl:1\n"
            "v_max_u32_dpp v94, v94, v94 row_shr:4 row_mask:0xf bank_mask:0xf bound_ctrl:1\n"
            "v_max_u32_dpp v92, v92, v92 row_shr:4 row_mask:0xf bank_mask:0xf bound_ctrl:1\n"
            "v_add_f32_dpp v104, v104, v104 row_shr:4 row_mask:0xf bank_mask:0xf bound_ctrl:1\n"
            "v_add_f32_dpp v93, v93, v93 row_shr:4 row_mask:0xf bank_mask:0xf bound_ctrl:1\n"
            "v_max_u32_dpp v94, v94, v94 row_shr:8 row_mask:0xf bank_mask:0xf bound_ctrl:1\n"
            "v_max_u32_dpp v92, v92, v92 row_shr:8 row_mask:0xf bank_mask:0xf bound_ctrl:1\n"
            "v_add_f32_dpp v104, v104, v104 row_shr:8 row_mask:0xf bank_mask:0xf bound_ctrl:1\n"
            "v_add_f32_dpp v93, v93, v93 row_shr:8 row_mask:0xf bank_mask:0xf bound_ctrl:1\n"
            "v_max_u32_dpp v94, v94, v94 row_bcast:15 row_mask:0xa bank_mask:0xf\n"
            "v_max_u32_dpp v92, v92, v92 row_bcast:15 row_mask:0xa bank_mask:0xf\n"
            "v_add_f32_dpp v104, v104, v104 row_bcast:15 row_mask:0xa bank_mask:0xf\n"
            "v_add_f32_dpp v93, v93, v93 row_bcast:15 row_mask:0xa bank_mask:0xf\n"
            "v_max_u32_dpp v94, v94, v94 row_bcast:31 row_mask:0xc bank_mask:0xf\n"
            "v_max_u32_dpp v92, v92, v92 row_bcast:31 row_mask:0xc bank_mask:0xf\n"
            "v_add_f32_dpp v104, v104, v104 row_bcast:31 row_mask:0xc bank_mask:0xf\n"
            "v_add_f32_dpp v93, v93, v93 row_bcast:31 row_mask:0xc bank_mask:0xf\n"
            "s_nop 1\n"
            "v_readlane_b32 s84, v94, 63\n v_readlane_b32 s85, v92, 63\n v_readlane_b32 s86, v104, 63\n v_readlane_b32 s87, v93, 63\n"
            "s_mov_b32 %[mode], 0\n"
            "s_max_u32 s96, s84, s85\n s_cmp_ge_u32 s96, 0x7f00\n s_cselect_b32 %[mode], 2, 0\n"
            "s_cmp_eq_u32 s84, 0\n s_cselect_b32 s84, 0x3f80, s84\n s_cselect_b32 s86, 0x7f000000, s86\n"
            "s_cmp_eq_u32 s85, 0\n s_cselect_b32 s85, 0x3f80, s85\n s_cselect_b32 s87, 0x7f000000, s87\n"
            "s_min_u32 s96, s84, s85\n s_cmp_lt_u32 s96, 0x800\n s_cselect_b32 %[mode], 2, %[mode]\n"
            "s_cmp_lg_u32 %[mode], 0\n"
            "s_cbranch_scc1 Lend%=\n"
            "s_lshl_b32 s84, s84, 16\n s_lshl_b32 s85, s85, 16\n"
            "v_mov_b32 v104, s84\n v_mov_b32 v105, s85\n"
            "v_mul_f32 v106, 0x3e82d0e5, v104\n v_mul_f32 v108, 0x3e82d0e5, v105\n"
            "v_mul_f32 v104, v104, v104\n v_mul_f32 v105, v105, v105\n"
            "v_add_u32 v106, 0x7fffff, v106\n v_add_u32 v108, 0x7fffff, v108\n"
            "v_mul_f32 v104, %[c4], v104\n v_mul_f32 v105, %[c4], v105\n"
            "v_and_b32 v106, 0x7f800000, v106\n v_and_b32 v108, 0x7f800000, v108\n"
            "v_cmp_lt_f32 vcc, s86, v104\n v_cmp_lt_f32 s[88:89], s87, v105\n"
            "v_add_u32 v86, 0xc00000, v106\n v_add_u32 v87, 0xc00000, v108\n"
            "v_lshrrev_b32 v106, 23, v106\n v_lshrrev_b32 v108, 15, v108\n"
            "v_or_b32 %[exs], v106, v108\n"
            "s_or_b64 vcc, vcc, s[88:89]\n"
            "s_cselect_b32 %[mode], 1, 0\n"
            "s_cbranch_scc1 Lend%=\n"
            "s_waitcnt vmcnt(3)\n"
            "s_mov_b32 m0, %[wb]\n"
            "s_nop 0\n"
            "ds_write_addtid_b32 v40 offset:0\n"
            "ds_write_addtid_b32 v41 offset:256\n"
            "ds_write_addtid_b32 v42 offset:512\n"
            "ds_write_addtid_b32 v43 offset:768\n"
            "ds_write_addtid_b32 v44 offset:16384\n"
            "ds_write_addtid_b32 v45 offset:16640\n"
            "ds_write_addtid_b32 v46 offset:16896\n"
            "ds_write_addtid_b32 v47 offset:17152\n"
            "ds_write_addtid_b32 v48 offset:32768\n"
            "ds_write_addtid_b32 v49 offset:33024\n"
            "ds_write_addtid_b32 v50 offset:33280\n"
            "ds_write_addtid_b32 v51 offset:33536\n"
            "ds_write_addtid_b32 v52 offset:49152\n"
            "ds_write_addtid_b32 v53 offset:49408\n"
            "ds_write_addtid_b32 v54 offset:49664\n"
            "ds_write_addtid_b32 v55 offset:49920\n"
            "s_add_u32 m0, %[wb], 16384\n"
            "s_nop 0\n"
            "ds_write_addtid_b32 v56 offset:49152\n"
            "ds_write_addtid_b32 v57 offset:49408\n"
            "ds_write_addtid_b32 v58 offset:49664\n"
            "ds_write_addtid_b32 v59 offset:49920\n"
            "s_mov_b32 s92, 0\n"
            "s_and_b32 s75, s76, 255\n"
            "s_movk_i32 s74, 1000\n"
            "s_sub_i32 s79, s75, %[j0]\n"
            "s_max_i32 s79, s79, 8\n"
            "s_cmp_eq_u32 s92, 7\n s_cselect_b32 s79, 8, s79\n"
            "s_setprio 3\n"
            "v_add_u32_dpp v111, v81, v82 row_newbcast:3 row_mask:0xf bank_mask:0x3\n"
            "v_add_u32_dpp v111, v81, v82 row_newbcast:11 row_mask:0xf bank_mask:0xc\n"
            "v_and_b32 v111, " IDXMASK ", v111\n"
            "v_min_u32 v111, v111, v83\n"
            "buffer_load_dwordx4 v[20:23], v111, %[rs], 0 idxen " RAUX "\n"
            "v_add_u32_dpp v111, v81, v82 row_newbcast:4 row_mask:0xf bank_mask:0x3\n"
            "v_add_u32_dpp v111, v81, v82 row_newbcast:12 row_mask:0xf bank_mask:0xc\n"
            "v_and_b32 v111, " IDXMASK ", v111\n"
            "v_min_u32 v111, v111, v83\n"
            "buffer_load_dwordx4 v[24:27], v111, %[rs], 0 idxen " RAUX "\n"
            "v_add_u32_dpp v111, v81, v82 row_newbcast:5 row_mask:0xf bank_mask:0x3\n"
            "v_add_u32_dpp v111, v81, v82 row_newbcast:13 row_mask:0xf bank_mask:0xc\n"
            "v_and_b32 v111, " IDXMASK ", v111\n"
            "v_min_u32 v111, v111, v83\n"
            "buffer_load_dwordx4 v[28:31], v111, %[rs], 0 idxen " RAUX "\n"
            "v_add_u32_dpp v111, v81, v82 row_newbcast:6 row_mask:0xf bank_mask:0x3\n"
            "v_add_u32_dpp v111, v81, v82 row_newbcast:14 row_mask:0xf bank_mask:0xc\n"
            "v_and_b32 v111, " IDXMASK ", v111\n"
            "v_min_u32 v111, v111, v83\n"
            "buffer_load_dwordx4 v[32:35], v111, %[rs], 0 idxen " RAUX "\n"
            "v_add_u32_dpp v111, v81, v82 row_newbcast:7 row_mask:0xf bank_mask:0x3\n"
            "v_add_u32_dpp v111, v81, v82 row_newbcast:15 row_mask:0xf bank_mask:0xc\n"
            "v_and_b32 v111, " IDXMASK ", v111\n"
            "v_min_u32 v111, v111, v83\n"
            "buffer_load_dwordx4 v[36:39], v111, %[rs], 0 idxen " RAUX "\n"
            "s_min_u32 s82, s82, 19\n"
            "s_add_u32 s82, s82, 1\n"
            "s_set_gpr_idx_on s82, gpr_idx(SRC0)\n"
            "s_nop 0\n"
            "v_mov_b32 v81, v60\n"
            "s_set_gpr_idx_off\n"
            "s_waitcnt lgkmcnt(0)\n"
            "s_barrier\n"
            "s_waitcnt vmcnt(7)\n"
            "v_and_b32 v104, 0x1fffc, v8\n"
            "v_and_b32 v105, 0x1fffc, v9\n"
            "v_and_b32 v106, 0x1fffc, v10\n"
            "ds_read_b32 v93, v104\n"
            "ds_read_b32 v95, v105\n"
            "ds_read_b32 v97, v106\n"
            "s_waitcnt lgkmcnt(0)\n"
            "Ls0_%=:\n"
            "s_cmp_gt_i32 s75, s74\n"
            "s_cbranch_scc1 1f\n"
            "s_waitcnt vmcnt(6)\n"
            "1:\n"
            "s_waitcnt vmcnt(11)\n"
            "s_cmp_eq_u32 s75, s79\n"
            "s_cbranch_scc1 Le0_%=\n"
            "Lc0_%=:\n"
            "v_and_b32 v104, 0x1fffc, v12\n"
            "v_and_b32 v105, 0x1fffc, v13\n"
            "v_and_b32 v106, 0x1fffc, v14\n"
            "ds_read_b32 v99, v104\n"
            "ds_read_b32 v101, v105\n"
            "ds_read_b32 v103, v106\n"
            "v_add_u32_dpp v111, v81, v82 row_newbcast:0 row_mask:0xf bank_mask:0x3\n"
            "v_add_u32_dpp v111, v81, v82 row_newbcast:8 row_mask:0xf bank_mask:0xc\n"
            "v_and_b32 v108, 0x7fe, v11\n"
            "v_and_b32 v109, 0x1ff800, v11\n"
            "v_lshrrev_b32 v110, 22, v11\n"
            "v_lshrrev_b32 v112, 15, v8\n"
            "v_lshrrev_b32 v113, 15, v9\n"
            "v_lshrrev_b32 v114, 15, v10\n"
            "v_and_b32 v111, " IDXMASK ", v111\n"
            "v_min_u32 v111, v111, v83\n"
            "buffer_load_dwordx4 v[8:11], v111, %[rs], 0 idxen " RAUX "\n"
            "v_fmamk_f32 v108, v108, 0x7e800000, v84\n"
            "v_fmamk_f32 v109, v109, 0x79800000, v84\n"
            "v_fmamk_f32 v110, v110, 0x7f000000, v84\n"
            "v_or_b32 v112, 3, v112\n"
            "v_or_b32 v113, 3, v113\n"
            "v_or_b32 v114, 3, v114\n"
            "s_sub_u32 s75, s75, 1\n"
            "s_waitcnt lgkmcnt(6)\n"
            "v_lshlrev_b32 v92, 16, v93\n"
            "v_lshlrev_b32 v94, 16, v95\n"
            "v_lshlrev_b32 v96, 16, v97\n"
            "v_pk_fma_f32 v[92:93], v[92:93], v[108:109], v[86:87] op_sel:[0,0,0] op_sel_hi:[1,0,1]\n"
            "v_pk_fma_f32 v[94:95], v[94:95], v[108:109], v[86:87] op_sel:[0,1,0] op_sel_hi:[1,1,1]\n"
            "v_pk_fma_f32 v[96:97], v[96:97], v[110:111], v[86:87] op_sel:[0,0,0] op_sel_hi:[1,0,1]\n"
            "v_lshl_add_u32 v92, v93, 16, v92\n"
            "v_lshl_add_u32 v94, v95, 16, v94\n"
            "v_lshl_add_u32 v96, v97, 16, v96\n"
            "v_sub_u32 v92, v92, v86\n"
            "v_mad_i32_i16 v88, v93, v112, v88\n"
            "ds_add_u32 v112, v92 offset:65533\n"
            "v_sub_u32 v94, v94, v86\n"
            "v_mad_i32_i16 v88, v95, v113, v88\n"
            "ds_add_u32 v113, v94 offset:65533\n"
            "v_sub_u32 v96, v96, v86\n"
            "v_mad_i32_i16 v88, v97, v114, v88\n"
            "ds_add_u32 v114, v96 offset:65533\n"
            "Ls1_%=:\n"
            "s_cmp_gt_i32 s75, s74\n"
            "s_cbranch_scc1 1f\n"
            "s_waitcnt vmcnt(6)\n"
            "1:\n"
            "s_waitcnt vmcnt(11)\n"
            "s_cmp_eq_u32 s75, s79\n"
            "s_cbranch_scc1 Le1_%=\n"
            "Lc1_%=:\n"
            "v_and_b32 v104, 0x1fffc, v16\n"
            "v_and_b32 v105, 0x1fffc, v17\n"
            "v_and_b32 v106, 0x1fffc, v18\n"
            "ds_read_b32 v93, v104\n"
            "ds_read_b32 v95, v105\n"
            "ds_read_b32 v97, v106\n"
            "v_add_u32_dpp v111, v81, v82 row_newbcast:1 row_mask:0xf bank_mask:0x3\n"
            "v_add_u32_dpp v111, v81, v82 row_newbcast:9 row_mask:0xf bank_mask:0xc\n"
            "v_and_b32 v108, 0x7fe, v15\n"
            "v_and_b32 v109, 0x1ff800, v15\n"
            "v_lshrrev_b32 v110, 22, v15\n"
            "v_lshrrev_b32 v112, 15, v12\n"
            "v_lshrrev_b32 v113, 15, v13\n"
            "v_lshrrev_b32 v114, 15, v14\n"
            "v_and_b32 v111, " IDXMASK ", v111\n"
            "v_min_u32 v111, v111, v83\n"
            "buffer_load_dwordx4 v[12:15], v111, %[rs], 0 idxen " RAUX "\n"
            "v_fmamk_f32 v108, v108, 0x7e800000, v84\n"
            "v_fmamk_f32 v109, v109, 0x79800000, v84\n"
            "v_fmamk_f32 v110, v110, 0x7f000000, v84\n"
            "v_or_b32 v112, 3, v112\n"
            "v_or_b32 v113, 3, v113\n"
            "v_or_b32 v114, 3, v114\n"
            "s_sub_u32 s75, s75, 1\n"
            "s_waitcnt lgkmcnt(6)\n"
            "v_lshlrev_b32 v98, 16, v99\n"
            "v_lshlrev_b32 v100, 16, v101\n"
            "v_lshlrev_b32 v102, 16, v103\n"
            "v_pk_fma_f32 v[98:99], v[98:99], v[108:109], v[86:87] op_sel:[0,0,0] op_sel_hi:[1,0,1]\n"
            "v_pk_fma_f32 v[100:101], v[100:101], v[108:109], v[86:87] op_sel:[0,1,0] op_sel_hi:[1,1,1]\n"
            "v_pk_fma_f32 v[102:103], v[102:103], v[110:111], v[86:87] op_sel:[0,0,0] op_sel_hi:[1,0,1]\n"
            "v_lshl_add_u32 v98, v99, 16, v98\n"
            "v_lshl_add_u32 v100, v101, 16, v100\n"
            "v_lshl_add_u32 v102, v103, 16, v102\n"
            "v_sub_u32 v98, v98, v86\n"
            "v_mad_i32_i16 v88, v99, v112, v88\n"
            "ds_add_u32 v112, v98 offset:65533\n"
            "v_sub_u32 v100, v100, v86\n"
            "v_mad_i32_i16 v88, v101, v113, v88\n"
            "ds_add_u32 v113, v100 offset:65533\n"
            "v_sub_u32 v102, v102, v86\n"
            "v_mad_i32_i16 v88, v103, v114, v88\n"
            "ds_add_u32 v114, v102 offset:65533\n"
            "Ls2_%=:\n"
            "s_cmp_gt_i32 s75, s74\n"
            "s_cbranch_scc1 1f\n"
            "s_waitcnt vmcnt(6)\n"
            "1:\n"
            "s_waitcnt vmcnt(11)\n"
            "s_cmp_eq_u32 s75, s79\n"
            "s_cbranch_scc1 Le2_%=\n"
            "Lc2_%=:\n"
            "v_and_b32 v104, 0x1fffc, v20\n"
            "v_and_b32 v105, 0x1fffc, v21\n"
            "v_and_b32 v106, 0x1fffc, v22\n"
            "ds_read_b32 v99, v104\n"
            "ds_read_b32 v101, v105\n"
            "ds_read_b32 v103, v106\n"
            "v_add_u32_dpp v111, v81, v82 row_newbcast:2 row_mask:0xf bank_mask:0x3\n"
            "v_add_u32_dpp v111, v81, v82 row_newbcast:10 row_mask:0xf bank_mask:0xc\n"
            "v_and_b32 v108, 0x7fe, v19\n"
            "v_and_b32 v109, 0x1ff800, v19\n"
            "v_lshrrev_b32 v110, 22, v19\n"
            "v_lshrrev_b32 v112, 15, v16\n"
            "v_lshrrev_b32 v113, 15, v17\n"
            "v_lshrrev_b32 v114, 15, v18\n"
            "v_and_b32 v111, " IDXMASK ", v111\n"
            "v_min_u32 v111, v111, v83\n"
            "buffer_load_dwordx4 v[16:19], v111, %[rs], 0 idxen " RAUX "\n"
            "v_fmamk_f32 v108, v108, 0x7e800000, v84\n"
            "v_fmamk_f32 v109, v109, 0x79800000, v84\n"
            "v_fmamk_f32 v110, v110, 0x7f000000, v84\n"
            "v_or_b32 v112, 3, v112\n"
            "v_or_b32 v113, 3, v113\n"
            "v_or_b32 v114, 3, v114\n"
            "s_sub_u32 s75, s75, 1\n"
            "s_waitcnt lgkmcnt(6)\n"
            "v_lshlrev_b32 v92, 16, v93\n"
            "v_lshlrev_b32 v94, 16, v95\n"
            "v_lshlrev_b32 v96, 16, v97\n"
            "v_pk_fma_f32 v[92:93], v[92:93], v[108:109], v[86:87] op_sel:[0,0,0] op_sel_hi:[1,0,1]\n"
            "v_pk_fma_f32 v[94:95], v[94:95], v[108:109], v[86:87] op_sel:[0,1,0] op_sel_hi:[1,1,1]\n"
            "v_pk_fma_f32 v[96:97], v[96:97], v[110:111], v[86:87] op_sel:[0,0,0] op_sel_hi:[1,0,1]\n"
            "v_lshl_add_u32 v92, v93, 16, v92\n"
            "v_lshl_add_u32 v94, v95, 16, v94\n"
            "v_lshl_add_u32 v96, v97, 16, v96\n"
            "v_sub_u32 v92, v92, v86\n"
            "v_mad_i32_i16 v88, v93, v112, v88\n"
            "ds_add_u32 v112, v92 offset:65533\n"
            "v_sub_u32 v94, v94, v86\n"
            "v_mad_i32_i16 v88, v95, v113, v88\n"
            "ds_add_u32 v113, v94 offset:65533\n"
            "v_sub_u32 v96, v96, v86\n"
            "v_mad_i32_i16 v88, v97, v114, v88\n"
            "ds_add_u32 v114, v96 offset:65533\n"
            "Ls3_%=:\n"
            "s_cmp_gt_i32 s75, s74\n"
            "s_cbranch_scc1 1f\n"
            "s_waitcnt vmcnt(6)\n"
            "1:\n"
            "s_waitcnt vmcnt(11)\n"
            "s_cmp_eq_u32 s75, s79\n"
            "s_cbranch_scc1 Le3_%=\n"
            "Lc3_%=:\n"
            "v_and_b32 v104, 0x1fffc, v24\n"
            "v_and_b32 v105, 0x1fffc, v25\n"
            "v_and_b32 v106, 0x1fffc, v26\n"
            "ds_read_b32 v93, v104\n"
            "ds_read_b32 v95, v105\n"
            "ds_read_b32 v97, v106\n"
            "v_add_u32_dpp v111, v81, v82 row_newbcast:3 row_mask:0xf bank_mask:0x3\n"
            "v_add_u32_dpp v111, v81, v82 row_newbcast:11 row_mask:0xf bank_mask:0xc\n"
            "v_and_b32 v108, 0x7fe, v23\n"
            "v_and_b32 v109, 0x1ff800, v23\n"
            "v_lshrrev_b32 v110, 22, v23\n"
            "v_lshrrev_b32 v112, 15, v20\n"
            "v_lshrrev_b32 v113, 15, v21\n"
            "v_lshrrev_b32 v114, 15, v22\n"
            "v_and_b32 v111, " IDXMASK ", v111\n"
            "v_min_u32 v111, v111, v83\n"
            "buffer_load_dwordx4 v[20:23], v111, %[rs], 0 idxen " RAUX "\n"
            "v_fmamk_f32 v108, v108, 0x7e800000, v84\n"
            "v_fmamk_f32 v109, v109, 0x79800000, v84\n"
            "v_fmamk_f32 v110, v110, 0x7f000000, v84\n"
            "v_or_b32 v112, 3, v112\n"
            "v_or_b32 v113, 3, v113\n"
            "v_or_b32 v114, 3, v114\n"
            "s_sub_u32 s75, s75, 1\n"
            "s_waitcnt lgkmcnt(6)\n"
            "v_lshlrev_b32 v98, 16, v99\n"
            "v_lshlrev_b32 v100, 16, v101\n"
            "v_lshlrev_b32 v102, 16, v103\n"
            "v_pk_fma_f32 v[98:99], v[98:99], v[108:109], v[86:87] op_sel:[0,0,0] op_sel_hi:[1,0,1]\n"
            "v_pk_fma_f32 v[100:101], v[100:101], v[108:109], v[86:87] op_sel:[0,1,0] op_sel_hi:[1,1,1]\n"
            "v_pk_fma_f32 v[102:103], v[102:103], v[110:111], v[86:87] op_sel:[0,0,0] op_sel_hi:[1,0,1]\n"
            "v_lshl_add_u32 v98, v99, 16, v98\n"
            "v_lshl_add_u32 v100, v101, 16, v100\n"
            "v_lshl_add_u32 v102, v103, 16, v102\n"
            "v_sub_u32 v98, v98, v86\n"
            "v_mad_i32_i16 v88, v99, v112, v88\n"
            "ds_add_u32 v112, v98 offset:65533\n"
            "v_sub_u32 v100, v100, v86\n"
            "v_mad_i32_i16 v88, v101, v113, v88\n"
            "ds_add_u32 v113, v100 offset:65533\n"
            "v_sub_u32 v102, v102, v86\n"
            "v_mad_i32_i16 v88, v103, v114, v88\n"
            "ds_add_u32 v114, v102 offset:65533\n"
            "Ls4_%=:\n"
            "s_cmp_gt_i32 s75, s74\n"
            "s_cbranch_scc1 1f\n"
            "s_waitcnt vmcnt(6)\n"
            "1:\n"
            "s_waitcnt vmcnt(11)\n"
            "s_cmp_eq_u32 s75, s79\n"
            "s_cbranch_scc1 Le4_%=\n"
            "Lc4_%=:\n"
            "v_and_b32 v104, 0x1fffc, v28\n"
            "v_and_b32 v105, 0x1fffc, v29\n"
            "v_and_b32 v106, 0x1fffc, v30\n"
            "ds_read_b32 v99, v104\n"
            "ds_read_b32 v101, v105\n"
            "ds_read_b32 v103, v106\n"
            "v_add_u32_dpp v111, v81, v82 row_newbcast:4 row_mask:0xf bank_mask:0x3\n"
            "v_add_u32_dpp v111, v81, v82 row_newbcast:12 row_mask:0xf bank_mask:0xc\n"
            "v_and_b32 v108, 0x7fe, v27\n"
            "v_and_b32 v109, 0x1ff800, v27\n"
            "v_lshrrev_b32 v110, 22, v27\n"
            "v_lshrrev_b32 v112, 15, v24\n"
            "v_lshrrev_b32 v113, 15, v25\n"
            "v_lshrrev_b32 v114, 15, v26\n"
            "v_and_b32 v111, " IDXMASK ", v111\n"
            "v_min_u32 v111, v111, v83\n"
            "buffer_load_dwordx4 v[24:27], v111, %[rs], 0 idxen " RAUX "\n"
            "v_fmamk_f32 v108, v108, 0x7e800000, v84\n"
            "v_fmamk_f32 v109, v109, 0x79800000, v84\n"
            "v_fmamk_f32 v110, v110, 0x7f000000, v84\n"
            "v_or_b32 v112, 3, v112\n"
            "v_or_b32 v113, 3, v113\n"
            "v_or_b32 v114, 3, v114\n"
            "s_sub_u32 s75, s75, 1\n"
            "s_waitcnt lgkmcnt(6)\n"
            "v_lshlrev_b32 v92, 16, v93\n"
            "v_lshlrev_b32 v94, 16, v95\n"
            "v_lshlrev_b32 v96, 16, v97\n"
            "v_pk_fma_f32 v[92:93], v[92:93], v[108:109], v[86:87] op_sel:[0,0,0] op_sel_hi:[1,0,1]\n"
            "v_pk_fma_f32 v[94:95], v[94:95], v[108:109], v[86:87] op_sel:[0,1,0] op_sel_hi:[1,1,1]\n"
            "v_pk_fma_f32 v[96:97], v[96:97], v[110:111], v[86:87] op_sel:[0,0,0] op_sel_hi:[1,0,1]\n"
            "v_lshl_add_u32 v92, v93, 16, v92\n"
            "v_lshl_add_u32 v94, v95, 16, v94\n"
            "v_lshl_add_u32 v96, v97, 16, v96\n"
            "v_sub_u32 v92, v92, v86\n"
            "v_mad_i32_i16 v88, v93, v112, v88\n"
            "ds_add_u32 v112, v92 offset:65533\n"
            "v_sub_u32 v94, v94, v86\n"
            "v_mad_i32_i16 v88, v95, v113, v88\n"
            "ds_add_u32 v113, v94 offset:65533\n"
            "v_sub_u32 v96, v96, v86\n"
            "v_mad_i32_i16 v88, v97, v114, v88\n"
            "ds_add_u32 v114, v96 offset:65533\n"
            "Ls5_%=:\n"
            "s_cmp_gt_i32 s75, s74\n"
            "s_cbranch_scc1 1f\n"
            "s_waitcnt vmcnt(6)\n"
            "1:\n"
            "s_waitcnt vmcnt(11)\n"
            "s_cmp_eq_u32 s75, s79\n"
            "s_cbranch_scc1 Le5_%=\n"
            "Lc5_%=:\n"
            "v_and_b32 v104, 0x1fffc, v32\n"
            "v_and_b32 v105, 0x1fffc, v33\n"
            "v_and_b32 v106, 0x1fffc, v34\n"
            "ds_read_b32 v93, v104\n"
            "ds_read_b32 v95, v105\n"
            "ds_read_b32 v97, v106\n"
            "v_add_u32_dpp v111, v81, v82 row_newbcast:5 row_mask:0xf bank_mask:0x3\n"
            "v_add_u32_dpp v111, v81, v82 row_newbcast:13 row_mask:0xf bank_mask:0xc\n"
            "v_and_b32 v108, 0x7fe, v31\n"
            "v_and_b32 v109, 0x1ff800, v31\n"
            "v_lshrrev_b32 v110, 22, v31\n"
            "v_lshrrev_b32 v112, 15, v28\n"
            "v_lshrrev_b32 v113, 15, v29\n"
            "v_lshrrev_b32 v114, 15, v30\n"
            "v_and_b32 v111, " IDXMASK ", v111\n"
            "v_min_u32 v111, v111, v83\n"
            "buffer_load_dwordx4 v[28:31], v111, %[rs], 0 idxen " RAUX "\n"
            "v_fmamk_f32 v108, v108, 0x7e800000, v84\n"
            "v_fmamk_f32 v109, v109, 0x79800000, v84\n"
            "v_fmamk_f32 v110, v110, 0x7f000000, v84\n"
            "v_or_b32 v112, 3, v112\n"
            "v_or_b32 v113, 3, v113\n"
            "v_or_b32 v114, 3, v114\n"
            "s_sub_u32 s75, s75, 1\n"
            "s_waitcnt lgkmcnt(6)\n"
            "v_lshlrev_b32 v98, 16, v99\n"
            "v_lshlrev_b32 v100, 16, v101\n"
            "v_lshlrev_b32 v102, 16, v103\n"
            "v_pk_fma_f32 v[98:99], v[98:99], v[108:109], v[86:87] op_sel:[0,0,0] op_sel_hi:[1,0,1]\n"
            "v_pk_fma_f32 v[100:101], v[100:101], v[108:109], v[86:87] op_sel:[0,1,0] op_sel_hi:[1,1,1]\n"
            "v_pk_fma_f32 v[102:103], v[102:103], v[110:111], v[86:87] op_sel:[0,0,0] op_sel_hi:[1,0,1]\n"
            "v_lshl_add_u32 v98, v99, 16, v98\n"
            "v_lshl_add_u32 v100, v101, 16, v100\n"
            "v_lshl_add_u32 v102, v103, 16, v102\n"
            "v_sub_u32 v98, v98, v86\n"
            "v_mad_i32_i16 v88, v99, v112, v88\n"
            "ds_add_u32 v112, v98 offset:65533\n"
            "v_sub_u32 v100, v100, v86\n"
            "v_mad_i32_i16 v88, v101, v113, v88\n"
            "ds_add_u32 v113, v100 offset:65533\n"
            "v_sub_u32 v102, v102, v86\n"
            "v_mad_i32_i16 v88, v103, v114, v88\n"
            "ds_add_u32 v114, v102 offset:65533\n"
            "Ls6_%=:\n"
            "s_cmp_gt_i32 s75, s74\n"
            "s_cbranch_scc1 1f\n"
            "s_waitcnt vmcnt(6)\n"
            "1:\n"
            "s_waitcnt vmcnt(11)\n"
            "s_cmp_eq_u32 s75, s79\n"
            "s_cbranch_scc1 Le6_%=\n"
            "Lc6_%=:\n"
            "v_and_b32 v104, 0x1fffc, v36\n"
            "v_and_b32 v105, 0x1fffc, v37\n"
            "v_and_b32 v106, 0x1fffc, v38\n"
            "ds_read_b32 v99, v104\n"
            "ds_read_b32 v101, v105\n"
            "ds_read_b32 v103, v106\n"
            "v_add_u32_dpp v111, v81, v82 row_newbcast:6 row_mask:0xf bank_mask:0x3\n"
            "v_add_u32_dpp v111, v81, v82 row_newbcast:14 row_mask:0xf bank_mask:0xc\n"
            "v_and_b32 v108, 0x7fe, v35\n"
            "v_and_b32 v109, 0x1ff800, v35\n"
            "v_lshrrev_b32 v110, 22, v35\n"
            "v_lshrrev_b32 v112, 15, v32\n"
            "v_lshrrev_b32 v113, 15, v33\n"
            "v_lshrrev_b32 v114, 15, v34\n"
            "v_and_b32 v111, " IDXMASK ", v111\n"
            "v_min_u32 v111, v111, v83\n"
            "buffer_load_dwordx4 v[32:35], v111, %[rs], 0 idxen " RAUX "\n"
            "v_fmamk_f32 v108, v108, 0x7e800000, v84\n"
            "v_fmamk_f32 v109, v109, 0x79800000, v84\n"
            "v_fmamk_f32 v110, v110, 0x7f000000, v84\n"
            "v_or_b32 v112, 3, v112\n"
            "v_or_b32 v113, 3, v113\n"
            "v_or_b32 v114, 3, v114\n"
            "s_sub_u32 s75, s75, 1\n"
            "s_waitcnt lgkmcnt(6)\n"
            "v_lshlrev_b32 v92, 16, v93\n"
            "v_lshlrev_b32 v94, 16, v95\n"
            "v_lshlrev_b32 v96, 16, v97\n"
            "v_pk_fma_f32 v[92:93], v[92:93], v[108:109], v[86:87] op_sel:[0,0,0] op_sel_hi:[1,0,1]\n"
            "v_pk_fma_f32 v[94:95], v[94:95], v[108:109], v[86:87] op_sel:[0,1,0] op_sel_hi:[1,1,1]\n"
            "v_pk_fma_f32 v[96:97], v[96:97], v[110:111], v[86:87] op_sel:[0,0,0] op_sel_hi:[1,0,1]\n"
            "v_lshl_add_u32 v92, v93, 16, v92\n"
            "v_lshl_add_u32 v94, v95, 16, v94\n"
            "v_lshl_add_u32 v96, v97, 16, v96\n"
            "v_sub_u32 v92, v92, v86\n"
            "v_mad_i32_i16 v88, v93, v112, v88\n"
            "ds_add_u32 v112, v92 offset:65533\n"
            "v_sub_u32 v94, v94, v86\n"
            "v_mad_i32_i16 v88, v95, v113, v88\n"
            "ds_add_u32 v113, v94 offset:65533\n"
            "v_sub_u32 v96, v96, v86\n"
            "v_mad_i32_i16 v88, v97, v114, v88\n"
            "ds_add_u32 v114, v96 offset:65533\n"
            "Ls7_%=:\n"
            "s_cmp_gt_i32 s75, s74\n"
            "s_cbranch_scc1 1f\n"
            "s_waitcnt vmcnt(6)\n"
            "1:\n"
            "s_waitcnt vmcnt(11)\n"
            "s_cmp_eq_u32 s75, s79\n"
            "s_cbranch_scc1 Le7_%=\n"
            "Lc7_%=:\n"
            "v_and_b32 v104, 0x1fffc, v8\n"
            "v_and_b32 v105, 0x1fffc, v9\n"
            "v_and_b32 v106, 0x1fffc, v10\n"
            "ds_read_b32 v93, v104\n"
            "ds_read_b32 v95, v105\n"
            "ds_read_b32 v97, v106\n"
            "v_add_u32_dpp v111, v81, v82 row_newbcast:7 row_mask:0xf bank_mask:0x3\n"
            "v_add_u32_dpp v111, v81, v82 row_newbcast:15 row_mask:0xf bank_mask:0xc\n"
            "v_and_b32 v108, 0x7fe, v39\n"
            "v_and_b32 v109, 0x1ff800, v39\n"
            "v_lshrrev_b32 v110, 22, v39\n"
            "v_lshrrev_b32 v112, 15, v36\n"
            "v_lshrrev_b32 v113, 15, v37\n"
            "v_lshrrev_b32 v114, 15, v38\n"
            "v_and_b32 v111, " IDXMASK ", v111\n"
            "v_min_u32 v111, v111, v83\n"
            "buffer_load_dwordx4 v[36:39], v111, %[rs], 0 idxen " RAUX "\n"
            "s_min_u32 s82, s82, 19\n"
            "s_add_u32 s82, s82, 1\n"
            "s_set_gpr_idx_on s82, gpr_idx(SRC0)\n"
            "s_nop 0\n"
            "v_mov_b32 v81, v60\n"
            "s_set_gpr_idx_off\n"
            "v_fmamk_f32 v108, v108, 0x7e800000, v84\n"
            "v_fmamk_f32 v109, v109, 0x79800000, v84\n"
            "v_fmamk_f32 v110, v110, 0x7f000000, v84\n"
            "v_or_b32 v112, 3, v112\n"
            "v_or_b32 v113, 3, v113\n"
            "v_or_b32 v114, 3, v114\n"
            "s_sub_u32 s75, s75, 1\n"
            "s_waitcnt lgkmcnt(6)\n"
            "v_lshlrev_b32 v98, 16, v99\n"
            "v_lshlrev_b32 v100, 16, v101\n"
            "v_lshlrev_b32 v102, 16, v103\n"
            "v_pk_fma_f32 v[98:99], v[98:99], v[108:109], v[86:87] op_sel:[0,0,0] op_sel_hi:[1,0,1]\n"
            "v_pk_fma_f32 v[100:101], v[100:101], v[108:109], v[86:87] op_sel:[0,1,0] op_sel_hi:[1,1,1]\n"
            "v_pk_fma_f32 v[102:103], v[102:103], v[110:111], v[86:87] op_sel:[0,0,0] op_sel_hi:[1,0,1]\n"
            "v_lshl_add_u32 v98, v99, 16, v98\n"
            "v_lshl_add_u32 v100, v101, 16, v100\n"
            "v_lshl_add_u32 v102, v103, 16, v102\n"
            "v_sub_u32 v98, v98, v86\n"
            "v_mad_i32_i16 v88, v99, v112, v88\n"
            "ds_add_u32 v112, v98 offset:65533\n"
            "v_sub_u32 v100, v100, v86\n"
            "v_mad_i32_i16 v88, v101, v113, v88\n"
            "ds_add_u32 v113, v100 offset:65533\n"
            "v_sub_u32 v102, v102, v86\n"
            "v_mad_i32_i16 v88, v103, v114, v88\n"
            "ds_add_u32 v114, v102 offset:65533\n"
            "s_branch Ls0_%=\n"
            "Le0_%=:\n"
            "s_cmp_lt_u32 s75, 8\n"
            "s_cbranch_scc1 1f\n"
            "s_swappc_b64 s[80:81], s[66:67]\n"
            "s_branch Lc0_%=\n"
            "1:\n"
            "s_cmp_lg_u32 s75, 7\n"
            "s_cbranch_scc1 1f\n"
            "s_setprio 2\n"
            "s_mov_b32 s79, 3\n"
            "s_branch Lc0_%=\n"
            "1:\n"
            "s_cmp_lg_u32 s75, 3\n"
            "s_cbranch_scc1 1f\n"
            "s_setprio 1\n"
            "s_mov_b32 s79, 1\n"
            "s_branch Lc0_%=\n"
            "1:\n"
            "s_setprio 0\n"
            "v_add_u32_dpp v111, v81, v82 row_newbcast:0 row_mask:0xf bank_mask:0x3\n"
            "v_add_u32_dpp v111, v81, v82 row_newbcast:8 row_mask:0xf bank_mask:0xc\n"
            "v_and_b32 v108, 0x7fe, v11\n"
            "v_and_b32 v109, 0x1ff800, v11\n"
            "v_lshrrev_b32 v110, 22, v11\n"
            "v_lshrrev_b32 v112, 15, v8\n"
            "v_lshrrev_b32 v113, 15, v9\n"
            "v_lshrrev_b32 v114, 15, v10\n"
            "v_and_b32 v111, " IDXMASK ", v111\n"
            "v_min_u32 v111, v111, v83\n"
            "buffer_load_dwordx4 v[8:11], v111, %[rs], 0 idxen " RAUX "\n"
            "v_fmamk_f32 v108, v108, 0x7e800000, v84\n"
            "v_fmamk_f32 v109, v109, 0x79800000, v84\n"
            "v_fmamk_f32 v110, v110, 0x7f000000, v84\n"
            "v_or_b32 v112, 3, v112\n"
            "v_or_b32 v113, 3, v113\n"
            "v_or_b32 v114, 3, v114\n"
            "s_waitcnt lgkmcnt(3)\n"
            "s_swappc_b64 s[80:81], s[70:71]\n"
            "v_lshlrev_b32 v92, 16, v93\n"
            "v_lshlrev_b32 v94, 16, v95\n"
            "v_lshlrev_b32 v96, 16, v97\n"
            "v_pk_fma_f32 v[92:93], v[92:93], v[108:109], v[86:87] op_sel:[0,0,0] op_sel_hi:[1,0,1]\n"
            "v_pk_fma_f32 v[94:95], v[94:95], v[108:109], v[86:87] op_sel:[0,1,0] op_sel_hi:[1,1,1]\n"
            "v_pk_fma_f32 v[96:97], v[96:97], v[110:111], v[86:87] op_sel:[0,0,0] op_sel_hi:[1,0,1]\n"
            "v_lshl_add_u32 v92, v93, 16, v92\n"
            "v_lshl_add_u32 v94, v95, 16, v94\n"
            "v_lshl_add_u32 v96, v97, 16, v96\n"
            "v_sub_u32 v92, v92, v86\n"
            "v_mad_i32_i16 v88, v93, v112, v88\n"
            "ds_add_u32 v112, v92 offset:65533\n"
            "v_sub_u32 v94, v94, v86\n"
            "v_mad_i32_i16 v88, v95, v113, v88\n"
            "ds_add_u32 v113, v94 offset:65533\n"
            "v_sub_u32 v96, v96, v86\n"
            "v_mad_i32_i16 v88, v97, v114, v88\n"
            "ds_add_u32 v114, v96 offset:65533\n"
            "v_and_b32 v104, 0x1fffc, v12\n"
            "v_and_b32 v105, 0x1fffc, v13\n"
            "v_and_b32 v106, 0x1fffc, v14\n"
            "s_swappc_b64 s[80:81], s[72:73]\n"
            "ds_read_b32 v99, v104\n"
            "ds_read_b32 v101, v105\n"
            "ds_read_b32 v103, v106\n"
            "s_waitcnt lgkmcnt(0)\n"
            "s_branch Ls1_%=\n"
            "Le1_%=:\n"
            "s_cmp_lt_u32 s75, 8\n"
            "s_cbranch_scc1 1f\n"
            "s_swappc_b64 s[80:81], s[66:67]\n"
            "s_branch Lc1_%=\n"
            "1:\n"
            "s_cmp_lg_u32 s75, 7\n"
            "s_cbranch_scc1 1f\n"
            "s_setprio 2\n"
            "s_mov_b32 s79, 3\n"
            "s_branch Lc1_%=\n"
            "1:\n"
            "s_cmp_lg_u32 s75, 3\n"
            "s_cbranch_scc1 1f\n"
            "s_setprio 1\n"
            "s_mov_b32 s79, 1\n"
            "s_branch Lc1_%=\n"
            "1:\n"
            "s_setprio 0\n"
            "v_add_u32_dpp v111, v81, v82 row_newbcast:1 row_mask:0xf bank_mask:0x3\n"
            "v_add_u32_dpp v111, v81, v82 row_newbcast:9 row_mask:0xf bank_mask:0xc\n"
            "v_and_b32 v108, 0x7fe, v15\n"
            "v_and_b32 v109, 0x1ff800, v15\n"
            "v_lshrrev_b32 v110, 22, v15\n"
            "v_lshrrev_b32 v112, 15, v12\n"
            "v_lshrrev_b32 v113, 15, v13\n"
            "v_lshrrev_b32 v114, 15, v14\n"
            "v_and_b32 v111, " IDXMASK ", v111\n"
            "v_min_u32 v111, v111, v83\n"
            "buffer_load_dwordx4 v[12:15], v111, %[rs], 0 idxen " RAUX "\n"
            "v_fmamk_f32 v108, v108, 0x7e800000, v84\n"
            "v_fmamk_f32 v109, v109, 0x79800000, v84\n"
            "v_fmamk_f32 v110, v110, 0x7f000000, v84\n"
            "v_or_b32 v112, 3, v112\n"
            "v_or_b32 v113, 3, v113\n"
            "v_or_b32 v114, 3, v114\n"
            "s_waitcnt lgkmcnt(3)\n"
            "s_swappc_b64 s[80:81], s[70:71]\n"
            "v_lshlrev_b32 v98, 16, v99\n"
            "v_lshlrev_b32 v100, 16, v101\n"
            "v_lshlrev_b32 v102, 16, v103\n"
            "v_pk_fma_f32 v[98:99], v[98:99], v[108:109], v[86:87] op_sel:[0,0,0] op_sel_hi:[1,0,1]\n"
            "v_pk_fma_f32 v[100:101], v[100:101], v[108:109], v[86:87] op_sel:[0,1,0] op_sel_hi:[1,1,1]\n"
            "v_pk_fma_f32 v[102:103], v[102:103], v[110:111], v[86:87] op_sel:[0,0,0] op_sel_hi:[1,0,1]\n"
            "v_lshl_add_u32 v98, v99, 16, v98\n"
            "v_lshl_add_u32 v100, v101, 16, v100\n"
            "v_lshl_add_u32 v102, v103, 16, v102\n"
            "v_sub_u32 v98, v98, v86\n"
            "v_mad_i32_i16 v88, v99, v112, v88\n"
            "ds_add_u32 v112, v98 offset:65533\n"
            "v_sub_u32 v100, v100, v86\n"
            "v_mad_i32_i16 v88, v101, v113, v88\n"
            "ds_add_u32 v113, v100 offset:65533\n"
            "v_sub_u32 v102, v102, v86\n"
            "v_mad_i32_i16 v88, v103, v114, v88\n"
            "ds_add_u32 v114, v102 offset:65533\n"
            "v_and_b32 v104, 0x1fffc, v16\n"
            "v_and_b32 v105, 0x1fffc, v17\n"
            "v_and_b32 v106, 0x1fffc, v18\n"
            "s_swappc_b64 s[80:81], s[72:73]\n"
            "ds_read_b32 v93, v104\n"
            "ds_read_b32 v95, v105\n"
            "ds_read_b32 v97, v106\n"
            "s_waitcnt lgkmcnt(0)\n"
            "s_branch Ls2_%=\n"
            "Le2_%=:\n"
            "s_cmp_lt_u32 s75, 8\n"
            "s_cbranch_scc1 1f\n"
            "s_swappc_b64 s[80:81], s[66:67]\n"
            "s_branch Lc2_%=\n"
            "1:\n"
            "s_cmp_lg_u32 s75, 7\n"
            "s_cbranch_scc1 1f\n"
            "s_setprio 2\n"
            "s_mov_b32 s79, 3\n"
            "s_branch Lc2_%=\n"
            "1:\n"
            "s_cmp_lg_u32 s75, 3\n"
            "s_cbranch_scc1 1f\n"
            "s_setprio 1\n"
            "s_mov_b32 s79, 1\n"
            "s_branch Lc2_%=\n"
            "1:\n"
            "s_setprio 0\n"
            "v_add_u32_dpp v111, v81, v82 row_newbcast:2 row_mask:0xf bank_mask:0x3\n"
            "v_add_u32_dpp v111, v81, v82 row_newbcast:10 row_mask:0xf bank_mask:0xc\n"
            "v_and_b32 v108, 0x7fe, v19\n"
            "v_and_b32 v109, 0x1ff800, v19\n"
            "v_lshrrev_b32 v110, 22, v19\n"
            "v_lshrrev_b32 v112, 15, v16\n"
            "v_lshrrev_b32 v113, 15, v17\n"
            "v_lshrrev_b32 v114, 15, v18\n"
            "v_and_b32 v111, " IDXMASK ", v111\n"
            "v_min_u32 v111, v111, v83\n"
            "buffer_load_dwordx4 v[16:19], v111, %[rs], 0 idxen " RAUX "\n"
            "v_fmamk_f32 v108, v108, 0x7e800000, v84\n"
            "v_fmamk_f32 v109, v109, 0x79800000, v84\n"
            "v_fmamk_f32 v110, v110, 0x7f000000, v84\n"
            "v_or_b32 v112, 3, v112\n"
            "v_or_b32 v113, 3, v113\n"
            "v_or_b32 v114, 3, v114\n"
            "s_waitcnt lgkmcnt(3)\n"
            "s_swappc_b64 s[80:81], s[70:71]\n"
            "v_lshlrev_b32 v92, 16, v93\n"
            "v_lshlrev_b32 v94, 16, v95\n"
            "v_lshlrev_b32 v96, 16, v97\n"
            "v_pk_fma_f32 v[92:93], v[92:93], v[108:109], v[86:87] op_sel:[0,0,0] op_sel_hi:[1,0,1]\n"
            "v_pk_fma_f32 v[94:95], v[94:95], v[108:109], v[86:87] op_sel:[0,1,0] op_sel_hi:[1,1,1]\n"
            "v_pk_fma_f32 v[96:97], v[96:97], v[110:111], v[86:87] op_sel:[0,0,0] op_sel_hi:[1,0,1]\n"
            "v_lshl_add_u32 v92, v93, 16, v92\n"
            "v_lshl_add_u32 v94, v95, 16, v94\n"
            "v_lshl_add_u32 v96, v97, 16, v96\n"
            "v_sub_u32 v92, v92, v86\n"
            "v_mad_i32_i16 v88, v93, v112, v88\n"
            "ds_add_u32 v112, v92 offset:65533\n"
            "v_sub_u32 v94, v94, v86\n"
            "v_mad_i32_i16 v88, v95, v113, v88\n"
            "ds_add_u32 v113, v94 offset:65533\n"
            "v_sub_u32 v96, v96, v86\n"
            "v_mad_i32_i16 v88, v97, v114, v88\n"
            "ds_add_u32 v114, v96 offset:65533\n"
            "v_and_b32 v104, 0x1fffc, v20\n"
            "v_and_b32 v105, 0x1fffc, v21\n"
            "v_and_b32 v106, 0x1fffc, v22\n"
            "s_swappc_b64 s[80:81], s[72:73]\n"
            "ds_read_b32 v99, v104\n"
            "ds_read_b32 v101, v105\n"
            "ds_read_b32 v103, v106\n"
            "s_waitcnt lgkmcnt(0)\n"
            "s_branch Ls3_%=\n"
            "Le3_%=:\n"
            "s_cmp_lt_u32 s75, 8\n"
            "s_cbranch_scc1 1f\n"
            "s_swappc_b64 s[80:81], s[66:67]\n"
            "s_branch Lc3_%=\n"
            "1:\n"
            "s_cmp_lg_u32 s75, 7\n"
            "s_cbranch_scc1 1f\n"
            "s_setprio 2\n"
            "s_mov_b32 s79, 3\n"
            "s_branch Lc3_%=\n"
            "1:\n"
            "s_cmp_lg_u32 s75, 3\n"
            "s_cbranch_scc1 1f\n"
            "s_setprio 1\n"
            "s_mov_b32 s79, 1\n"
            "s_branch Lc3_%=\n"
            "1:\n"
            "s_setprio 0\n"
            "v_add_u32_dpp v111, v81, v82 row_newbcast:3 row_mask:0xf bank_mask:0x3\n"
            "v_add_u32_dpp v111, v81, v82 row_newbcast:11 row_mask:0xf bank_mask:0xc\n"
            "v_and_b32 v108, 0x7fe, v23\n"
            "v_and_b32 v109, 0x1ff800, v23\n"
            "v_lshrrev_b32 v110, 22, v23\n"
            "v_lshrrev_b32 v112, 15, v20\n"
            "v_lshrrev_b32 v113, 15, v21\n"
            "v_lshrrev_b32 v114, 15, v22\n"
            "v_and_b32 v111, " IDXMASK ", v111\n"
            "v_min_u32 v111, v111, v83\n"
            "buffer_load_dwordx4 v[20:23], v111, %[rs], 0 idxen " RAUX "\n"
            "v_fmamk_f32 v108, v108, 0x7e800000, v84\n"
            "v_fmamk_f32 v109, v109, 0x79800000, v84\n"
            "v_fmamk_f32 v110, v110, 0x7f000000, v84\n"
            "v_or_b32 v112, 3, v112\n"
            "v_or_b32 v113, 3, v113\n"
            "v_or_b32 v114, 3, v114\n"
            "s_waitcnt lgkmcnt(3)\n"
            "s_swappc_b64 s[80:81], s[70:71]\n"
            "v_lshlrev_b32 v98, 16, v99\n"
            "v_lshlrev_b32 v100, 16, v101\n"
            "v_lshlrev_b32 v102, 16, v103\n"
            "v_pk_fma_f32 v[98:99], v[98:99], v[108:109], v[86:87] op_sel:[0,0,0] op_sel_hi:[1,0,1]\n"
            "v_pk_fma_f32 v[100:101], v[100:101], v[108:109], v[86:87] op_sel:[0,1,0] op_sel_hi:[1,1,1]\n"
            "v_pk_fma_f32 v[102:103], v[102:103], v[110:111], v[86:87] op_sel:[0,0,0] op_sel_hi:[1,0,1]\n"
            "v_lshl_add_u32 v98, v99, 16, v98\n"
            "v_lshl_add_u32 v100, v101, 16, v100\n"
            "v_lshl_add_u32 v102, v103, 16, v102\n"
            "v_sub_u32 v98, v98, v86\n"
            "v_mad_i32_i16 v88, v99, v112, v88\n"
            "ds_add_u32 v112, v98 offset:65533\n"
            "v_sub_u32 v100, v100, v86\n"
            "v_mad_i32_i16 v88, v101, v113, v88\n"
            "ds_add_u32 v113, v100 offset:65533\n"
            "v_sub_u32 v102, v102, v86\n"
            "v_mad_i32_i16 v88, v103, v114, v88\n"
            "ds_add_u32 v114, v102 offset:65533\n"
            "v_and_b32 v104, 0x1fffc, v24\n"
            "v_and_b32 v105, 0x1fffc, v25\n"
            "v_and_b32 v106, 0x1fffc, v26\n"
            "s_swappc_b64 s[80:81], s[72:73]\n"
            "ds_read_b32 v93, v104\n"
            "ds_read_b32 v95, v105\n"
            "ds_read_b32 v97, v106\n"
            "s_waitcnt lgkmcnt(0)\n"
            "s_branch Ls4_%=\n"
            "Le4_%=:\n"
            "s_cmp_lt_u32 s75, 8\n"
            "s_cbranch_scc1 1f\n"
            "s_swappc_b64 s[80:81], s[66:67]\n"
            "s_branch Lc4_%=\n"
            "1:\n"
            "s_cmp_lg_u32 s75, 7\n"
            "s_cbranch_scc1 1f\n"
            "s_setprio 2\n"
            "s_mov_b32 s79, 3\n"
            "s_branch Lc4_%=\n"
            "1:\n"
            "s_cmp_lg_u32 s75, 3\n"
            "s_cbranch_scc1 1f\n"
            "s_setprio 1\n"
            "s_mov_b32 s79, 1\n"
            "s_branch Lc4_%=\n"
            "1:\n"
            "s_setprio 0\n"
            "v_add_u32_dpp v111, v81, v82 row_newbcast:4 row_mask:0xf bank_mask:0x3\n"
            "v_add_u32_dpp v111, v81, v82 row_newbcast:12 row_mask:0xf bank_mask:0xc\n"
            "v_and_b32 v108, 0x7fe, v27\n"
            "v_and_b32 v109, 0x1ff800, v27\n"
            "v_lshrrev_b32 v110, 22, v27\n"
            "v_lshrrev_b32 v112, 15, v24\n"
            "v_lshrrev_b32 v113, 15, v25\n"
            "v_lshrrev_b32 v114, 15, v26\n"
            "v_and_b32 v111, " IDXMASK ", v111\n"
            "v_min_u32 v111, v111, v83\n"
            "buffer_load_dwordx4 v[24:27], v111, %[rs], 0 idxen " RAUX "\n"
            "v_fmamk_f32 v108, v108, 0x7e800000, v84\n"
            "v_fmamk_f32 v109, v109, 0x79800000, v84\n"
            "v_fmamk_f32 v110, v110, 0x7f000000, v84\n"
            "v_or_b32 v112, 3, v112\n"
            "v_or_b32 v113, 3, v113\n"
            "v_or_b32 v114, 3, v114\n"
            "s_waitcnt lgkmcnt(3)\n"
            "s_swappc_b64 s[80:81], s[70:71]\n"
            "v_lshlrev_b32 v92, 16, v93\n"
            "v_lshlrev_b32 v94, 16, v95\n"
            "v_lshlrev_b32 v96, 16, v97\n"
            "v_pk_fma_f32 v[92:93], v[92:93], v[108:109], v[86:87] op_sel:[0,0,0] op_sel_hi:[1,0,1]\n"
            "v_pk_fma_f32 v[94:95], v[94:95], v[108:109], v[86:87] op_sel:[0,1,0] op_sel_hi:[1,1,1]\n"
            "v_pk_fma_f32 v[96:97], v[96:97], v[110:111], v[86:87] op_sel:[0,0,0] op_sel_hi:[1,0,1]\n"
            "v_lshl_add_u32 v92, v93, 16, v92\n"
            "v_lshl_add_u32 v94, v95, 16, v94\n"
            "v_lshl_add_u32 v96, v97, 16, v96\n"
            "v_sub_u32 v92, v92, v86\n"
            "v_mad_i32_i16 v88, v93, v112, v88\n"
            "ds_add_u32 v112, v92 offset:65533\n"
            "v_sub_u32 v94, v94, v86\n"
            "v_mad_i32_i16 v88, v95, v113, v88\n"
            "ds_add_u32 v113, v94 offset:65533\n"
            "v_sub_u32 v96, v96, v86\n"
            "v_mad_i32_i16 v88, v97, v114, v88\n"
            "ds_add_u32 v114, v96 offset:65533\n"
            "v_and_b32 v104, 0x1fffc, v28\n"
            "v_and_b32 v105, 0x1fffc, v29\n"
            "v_and_b32 v106, 0x1fffc, v30\n"
            "s_swappc_b64 s[80:81], s[72:73]\n"
            "ds_read_b32 v99, v104\n"
            "ds_read_b32 v101, v105\n"
            "ds_read_b32 v103, v106\n"
            "s_waitcnt lgkmcnt(0)\n"
            "s_branch Ls5_%=\n"
            "Le5_%=:\n"
            "s_cmp_lt_u32 s75, 8\n"
            "s_cbranch_scc1 1f\n"
            "s_swappc_b64 s[80:81], s[66:67]\n"
            "s_branch Lc5_%=\n"
            "1:\n"
            "s_cmp_lg_u32 s75, 7\n"
            "s_cbranch_scc1 1f\n"
            "s_setprio 2\n"
            "s_mov_b32 s79, 3\n"
            "s_branch Lc5_%=\n"
            "1:\n"
            "s_cmp_lg_u32 s75, 3\n"
            "s_cbranch_scc1 1f\n"
            "s_setprio 1\n"
            "s_mov_b32 s79, 1\n"
            "s_branch Lc5_%=\n"
            "1:\n"
            "s_setprio 0\n"
            "v_add_u32_dpp v111, v81, v82 row_newbcast:5 row_mask:0xf bank_mask:0x3\n"
            "v_add_u32_dpp v111, v81, v82 row_newbcast:13 row_mask:0xf bank_mask:0xc\n"
            "v_and_b32 v108, 0x7fe, v31\n"
            "v_and_b32 v109, 0x1ff800, v31\n"
            "v_lshrrev_b32 v110, 22, v31\n"
            "v_lshrrev_b32 v112, 15, v28\n"
            "v_lshrrev_b32 v113, 15, v29\n"
            "v_lshrrev_b32 v114, 15, v30\n"
            "v_and_b32 v111, " IDXMASK ", v111\n"
            "v_min_u32 v111, v111, v83\n"
            "buffer_load_dwordx4 v[28:31], v111, %[rs], 0 idxen " RAUX "\n"
            "v_fmamk_f32 v108, v108, 0x7e800000, v84\n"
            "v_fmamk_f32 v109, v109, 0x79800000, v84\n"
            "v_fmamk_f32 v110, v110, 0x7f000000, v84\n"
            "v_or_b32 v112, 3, v112\n"
            "v_or_b32 v113, 3, v113\n"
            "v_or_b32 v114, 3, v114\n"
            "s_waitcnt lgkmcnt(3)\n"
            "s_swappc_b64 s[80:81], s[70:71]\n"
            "v_lshlrev_b32 v98, 16, v99\n"
            "v_lshlrev_b32 v100, 16, v101\n"
            "v_lshlrev_b32 v102, 16, v103\n"
            "v_pk_fma_f32 v[98:99], v[98:99], v[108:109], v[86:87] op_sel:[0,0,0] op_sel_hi:[1,0,1]\n"
            "v_pk_fma_f32 v[100:101], v[100:101], v[108:109], v[86:87] op_sel:[0,1,0] op_sel_hi:[1,1,1]\n"
            "v_pk_fma_f32 v[102:103], v[102:103], v[110:111], v[86:87] op_sel:[0,0,0] op_sel_hi:[1,0,1]\n"
            "v_lshl_add_u32 v98, v99, 16, v98\n"
            "v_lshl_add_u32 v100, v101, 16, v100\n"
            "v_lshl_add_u32 v102, v103, 16, v102\n"
            "v_sub_u32 v98, v98, v86\n"
            "v_mad_i32_i16 v88, v99, v112, v88\n"
            "ds_add_u32 v112, v98 offset:65533\n"
            "v_sub_u32 v100, v100, v86\n"
            "v_mad_i32_i16 v88, v101, v113, v88\n"
            "ds_add_u32 v113, v100 offset:65533\n"
            "v_sub_u32 v102, v102, v86\n"
            "v_mad_i32_i16 v88, v103, v114, v88\n"
            "ds_add_u32 v114, v102 offset:65533\n"
            "v_and_b32 v104, 0x1fffc, v32\n"
            "v_and_b32 v105, 0x1fffc, v33\n"
            "v_and_b32 v106, 0x1fffc, v34\n"
            "s_swappc_b64 s[80:81], s[72:73]\n"
            "ds_read_b32 v93, v104\n"
            "ds_read_b32 v95, v105\n"
            "ds_read_b32 v97, v106\n"
            "s_waitcnt lgkmcnt(0)\n"
            "s_branch Ls6_%=\n"
            "Le6_%=:\n"
            "s_cmp_lt_u32 s75, 8\n"
            "s_cbranch_scc1 1f\n"
            "s_swappc_b64 s[80:81], s[66:67]\n"
            "s_branch Lc6_%=\n"
            "1:\n"
            "s_cmp_lg_u32 s75, 7\n"
            "s_cbranch_scc1 1f\n"
            "s_setprio 2\n"
            "s_mov_b32 s79, 3\n"
            "s_branch Lc6_%=\n"
            "1:\n"
            "s_cmp_lg_u32 s75, 3\n"
            "s_cbranch_scc1 1f\n"
            "s_setprio 1\n"
            "s_mov_b32 s79, 1\n"
            "s_branch Lc6_%=\n"
            "1:\n"
            "s_setprio 0\n"
            "v_add_u32_dpp v111, v81, v82 row_newbcast:6 row_mask:0xf bank_mask:0x3\n"
            "v_add_u32_dpp v111, v81, v82 row_newbcast:14 row_mask:0xf bank_mask:0xc\n"
            "v_and_b32 v108, 0x7fe, v35\n"
            "v_and_b32 v109, 0x1ff800, v35\n"
            "v_lshrrev_b32 v110, 22, v35\n"
            "v_lshrrev_b32 v112, 15, v32\n"
            "v_lshrrev_b32 v113, 15, v33\n"
            "v_lshrrev_b32 v114, 15, v34\n"
            "v_and_b32 v111, " IDXMASK ", v111\n"
            "v_min_u32 v111, v111, v83\n"
            "buffer_load_dwordx4 v[32:35], v111, %[rs], 0 idxen " RAUX "\n"
            "v_fmamk_f32 v108, v108, 0x7e800000, v84\n"
            "v_fmamk_f32 v109, v109, 0x79800000, v84\n"
            "v_fmamk_f32 v110, v110, 0x7f000000, v84\n"
            "v_or_b32 v112, 3, v112\n"
            "v_or_b32 v113, 3, v113\n"
            "v_or_b32 v114, 3, v114\n"
            "s_waitcnt lgkmcnt(3)\n"
            "s_swappc_b64 s[80:81], s[70:71]\n"
            "v_lshlrev_b32 v92, 16, v93\n"
            "v_lshlrev_b32 v94, 16, v95\n"
            "v_lshlrev_b32 v96, 16, v97\n"
            "v_pk_fma_f32 v[92:93], v[92:93], v[108:109], v[86:87] op_sel:[0,0,0] op_sel_hi:[1,0,1]\n"
            "v_pk_fma_f32 v[94:95], v[94:95], v[108:109], v[86:87] op_sel:[0,1,0] op_sel_hi:[1,1,1]\n"
            "v_pk_fma_f32 v[96:97], v[96:97], v[110:111], v[86:87] op_sel:[0,0,0] op_sel_hi:[1,0,1]\n"
            "v_lshl_add_u32 v92, v93, 16, v92\n"
            "v_lshl_add_u32 v94, v95, 16, v94\n"
            "v_lshl_add_u32 v96, v97, 16, v96\n"
            "v_sub_u32 v92, v92, v86\n"
            "v_mad_i32_i16 v88, v93, v112, v88\n"
            "ds_add_u32 v112, v92 offset:65533\n"
            "v_sub_u32 v94, v94, v86\n"
            "v_mad_i32_i16 v88, v95, v113, v88\n"
            "ds_add_u32 v113, v94 offset:65533\n"
            "v_sub_u32 v96, v96, v86\n"
            "v_mad_i32_i16 v88, v97, v114, v88\n"
            "ds_add_u32 v114, v96 offset:65533\n"
            "v_and_b32 v104, 0x1fffc, v36\n"
            "v_and_b32 v105, 0x1fffc, v37\n"
            "v_and_b32 v106, 0x1fffc, v38\n"
            "s_swappc_b64 s[80:81], s[72:73]\n"
            "ds_read_b32 v99, v104\n"
            "ds_read_b32 v101, v105\n"
            "ds_read_b32 v103, v106\n"
            "s_waitcnt lgkmcnt(0)\n"
            "s_branch Ls7_%=\n"
            "Le7_%=:\n"
            "s_cmp_lt_u32 s75, 8\n"
            "s_cbranch_scc1 1f\n"
            "s_swappc_b64 s[80:81], s[66:67]\n"
            "s_branch Lc7_%=\n"
            "1:\n"
            "s_cmp_lg_u32 s75, 7\n"
            "s_cbranch_scc1 1f\n"
            "s_setprio 2\n"
            "s_mov_b32 s79, 3\n"
            "s_branch Lc7_%=\n"
            "1:\n"
            "s_cmp_lg_u32 s75, 3\n"
            "s_cbranch_scc1 1f\n"
            "s_setprio 1\n"
            "s_mov_b32 s79, 1\n"
            "s_branch Lc7_%=\n"
            "1:\n"
            "s_setprio 0\n"
            "v_add_u32_dpp v111, v81, v82 row_newbcast:7 row_mask:0xf bank_mask:0x3\n"
            "v_add_u32_dpp v111, v81, v82 row_newbcast:15 row_mask:0xf bank_mask:0xc\n"
            "v_and_b32 v108, 0x7fe, v39\n"
            "v_and_b32 v109, 0x1ff800, v39\n"
            "v_lshrrev_b32 v110, 22, v39\n"
            "v_lshrrev_b32 v112, 15, v36\n"
            "v_lshrrev_b32 v113, 15, v37\n"
            "v_lshrrev_b32 v114, 15, v38\n"
            "v_and_b32 v111, " IDXMASK ", v111\n"
            "v_min_u32 v111, v111, v83\n"
            "buffer_load_dwordx4 v[36:39], v111, %[rs], 0 idxen " RAUX "\n"
            "s_min_u32 s82, s82, 19\n"
            "s_add_u32 s82, s82, 1\n"
            "s_set_gpr_idx_on s82, gpr_idx(SRC0)\n"
            "s_nop 0\n"
            "v_mov_b32 v81, v60\n"
            "s_set_gpr_idx_off\n"
            "v_fmamk_f32 v108, v108, 0x7e800000, v84\n"
            "v_fmamk_f32 v109, v109, 0x79800000, v84\n"
            "v_fmamk_f32 v110, v110, 0x7f000000, v84\n"
            "v_or_b32 v112, 3, v112\n"
            "v_or_b32 v113, 3, v113\n"
            "v_or_b32 v114, 3, v114\n"
            "s_waitcnt lgkmcnt(3)\n"
            "s_swappc_b64 s[80:81], s[70:71]\n"
            "v_lshlrev_b32 v98, 16, v99\n"
            "v_lshlrev_b32 v100, 16, v101\n"
            "v_lshlrev_b32 v102, 16, v103\n"
            "v_pk_fma_f32 v[98:99], v[98:99], v[108:109], v[86:87] op_sel:[0,0,0] op_sel_hi:[1,0,1]\n"
            "v_pk_fma_f32 v[100:101], v[100:101], v[108:109], v[86:87] op_sel:[0,1,0] op_sel_hi:[1,1,1]\n"
            "v_pk_fma_f32 v[102:103], v[102:103], v[110:111], v[86:87] op_sel:[0,0,0] op_sel_hi:[1,0,1]\n"
            "v_lshl_add_u32 v98, v99, 16, v98\n"
            "v_lshl_add_u32 v100, v101, 16, v100\n"
            "v_lshl_add_u32 v102, v103, 16, v102\n"
            "v_sub_u32 v98, v98, v86\n"
            "v_mad_i32_i16 v88, v99, v112, v88\n"
            "ds_add_u32 v112, v98 offset:65533\n"
            "v_sub_u32 v100, v100, v86\n"
            "v_mad_i32_i16 v88, v101, v113, v88\n"
            "ds_add_u32 v113, v100 offset:65533\n"
            "v_sub_u32 v102, v102, v86\n"
            "v_mad_i32_i16 v88, v103, v114, v88\n"
            "ds_add_u32 v114, v102 offset:65533\n"
            "v_and_b32 v104, 0x1fffc, v8\n"
            "v_and_b32 v105, 0x1fffc, v9\n"
            "v_and_b32 v106, 0x1fffc, v10\n"
            "s_swappc_b64 s[80:81], s[72:73]\n"
            "ds_read_b32 v93, v104\n"
            "ds_read_b32 v95, v105\n"
            "ds_read_b32 v97, v106\n"
            "s_waitcnt lgkmcnt(0)\n"
            "s_branch Ls0_%=\n"
            "Lchg1%=:\n"
            "s_add_u32 s92, s92, 1\n"
            "s_cmp_eq_u32 s92, 8\n"
            "s_cbranch_scc1 1f\n"
            "s_barrier\n"
            "s_mov_b32 m0, %[wb]\n"
            "s_nop 0\n"
            "ds_write_addtid_b32 v40 offset:0\n"
            "ds_write_addtid_b32 v41 offset:256\n"
            "ds_write_addtid_b32 v42 offset:512\n"
            "ds_write_addtid_b32 v43 offset:768\n"
            "ds_write_addtid_b32 v44 offset:16384\n"
            "ds_write_addtid_b32 v45 offset:16640\n"
            "ds_write_addtid_b32 v46 offset:16896\n"
            "ds_write_addtid_b32 v47 offset:17152\n"
            "ds_write_addtid_b32 v48 offset:32768\n"
            "ds_write_addtid_b32 v49 offset:33024\n"
            "ds_write_addtid_b32 v50 offset:33280\n"
            "ds_write_addtid_b32 v51 offset:33536\n"
            "ds_write_addtid_b32 v52 offset:49152\n"
            "ds_write_addtid_b32 v53 offset:49408\n"
            "ds_write_addtid_b32 v54 offset:49664\n"
            "ds_write_addtid_b32 v55 offset:49920\n"
            "s_add_u32 m0, %[wb], 16384\n"
            "s_nop 0\n"
            "ds_write_addtid_b32 v56 offset:49152\n"
            "ds_write_addtid_b32 v57 offset:49408\n"
            "ds_write_addtid_b32 v58 offset:49664\n"
            "ds_write_addtid_b32 v59 offset:49920\n"
            "1:\n"
            "s_setpc_b64 s[80:81]\n"
            "Lchg2%=:\n"
            "s_cmp_eq_u32 s92, 8\n"
            "s_cbranch_scc1 Lfin%=\n"
            "s_lshr_b64 s[76:77], s[76:77], 8\n"
            "s_and_b32 s75, s76, 255\n"
            "s_movk_i32 s74, 1000\n"
            "s_sub_i32 s79, s75, %[j0]\n"
            "s_max_i32 s79, s79, 8\n"
            "s_cmp_eq_u32 s92, 7\n s_cselect_b32 s79, 8, s79\n"
            "s_setprio 3\n"
            "s_waitcnt lgkmcnt(3)\n"
            "s_barrier\n"
            "s_setpc_b64 s[80:81]\n"
            "Lchg3%=:\n"
            "s_sub_u32 s74, s75, 7\n"
            "s_mov_b32 s79, 7\n"
            "s_cmp_eq_u32 s92, 7\n"
            "s_cbranch_scc1 1f\n"
            "s_add_u32 s94, s94, %[i4]\n"
            "buffer_load_dwordx4 v[40:43], %[tid16], %[rm], s94 offen\n"
            "s_add_u32 s96, s94, 16384\n"
            "buffer_load_dwordx4 v[44:47], %[tid16], %[rm], s96 offen\n"
            "s_add_u32 s96, s94, 32768\n"
            "buffer_load_dwordx4 v[48:51], %[tid16], %[rm], s96 offen\n"
            "s_add_u32 s96, s94, 49152\n"
            "buffer_load_dwordx4 v[52:55], %[tid16], %[rm], s96 offen\n"
            "s_add_u32 s96, s94, 65536\n"
            "buffer_load_dwordx4 v[56:59], %[tid16], %[rm], s96 offen\n"
            "s_branch 2f\n"
            "1:\n"
            "buffer_load_dword v49, v80, %[rd], 0 offen\n"
            "s_mov_b32 s74, 0\n"
            "buffer_load_dword v48, %[ed], %[rd], 0 offen " NTH "\n"
            "buffer_load_dword v50, %[ed], %[rd], %[e1] offen " NTH "\n"
            "v_lshlrev_b32 v52, 2, %[ed]\n"
            "s_lshl_b32 s96, %[e1], 2\n"
            "buffer_load_dwordx4 v[40:43], v52, %[rmm], 0 offen " NTH "\n"
            "buffer_load_dwordx4 v[44:47], v52, %[rmm], s96 offen " NTH "\n"
            "2:\n"
            "s_setpc_b64 s[80:81]\n"
            "Lfin%=:\n"
            "s_waitcnt lgkmcnt(0)\n"
            "s_barrier\n"
            "ds_read_b128 v[60:63], v89 offset:0\n"
            "ds_read_b128 v[64:67], v89 offset:16384\n"
            "ds_read_b128 v[68:71], v89 offset:32768\n"
            "ds_read_b128 v[72:75], v89 offset:49152\n"
            "ds_read_b128 v[76:79], v90\n"
            "v_subrev_u32 v107, 65533, v89\n"
            "v_mov_b32 v108, 0\n"
            "v_bfe_u32 v106, %[tid16], 9, 3\n"
            "v_cmp_eq_u32 vcc, %[q], v106\n"
            "v_bfrev_b32 v105, 1\n"
            "s_nop 0\n"
            "v_cndmask_b32 v105, %[tid16], v105, vcc\n"
            "s_mov_b32 s96, 16384\n s_mov_b32 s97, 32768\n s_mov_b32 s92, 49152\n s_mov_b32 s93, 65536\n"
            "s_waitcnt lgkmcnt(4)\n"
            "buffer_store_dwordx4 v[60:63], v105, %[rp0], 0 offen sc1\n"
            "s_waitcnt lgkmcnt(3)\n"
            "buffer_store_dwordx4 v[64:67], v105, %[rp0], s96 offen sc1\n"
            "s_waitcnt lgkmcnt(2)\n"
            "buffer_store_dwordx4 v[68:71], v105, %[rp0], s97 offen sc1\n"
            "s_waitcnt lgkmcnt(1)\n"
            "buffer_store_dwordx4 v[72:75], v105, %[rp0], s92 offen sc1\n"
            "s_waitcnt lgkmcnt(0)\n"
            "buffer_store_dwordx4 v[76:79], v105, %[rp0], s93 offen sc1\n"
            "v_add_u32 v112, 0x8000, v60\n"
            "v_add_u32 v106, 0, v107\n"
            "v_mad_i32_i16 v108, v112, v106, v108 op_sel:[1,0,0,0]\n"
            "v_add_u32 v112, 0x8000, v61\n"
            "v_add_u32 v106, 4, v107\n"
            "v_mad_i32_i16 v108, v112, v106, v108 op_sel:[1,0,0,0]\n"
            "v_add_u32 v112, 0x8000, v62\n"
            "v_add_u32 v106, 8, v107\n"
            "v_mad_i32_i16 v108, v112, v106, v108 op_sel:[1,0,0,0]\n"
            "v_add_u32 v112, 0x8000, v63\n"
            "v_add_u32 v106, 12, v107\n"
            "v_mad_i32_i16 v108, v112, v106, v108 op_sel:[1,0,0,0]\n"
            "v_add_u32 v107, 16384, v107\n"
            "v_add_u32 v112, 0x8000, v64\n"
            "v_add_u32 v106, 0, v107\n"
            "v_mad_i32_i16 v108, v112, v106, v108 op_sel:[1,0,0,0]\n"
            "v_add_u32 v112, 0x8000, v65\n"
            "v_add_u32 v106, 4, v107\n"
            "v_mad_i32_i16 v108, v112, v106, v108 op_sel:[1,0,0,0]\n"
            "v_add_u32 v112, 0x8000, v66\n"
            "v_add_u32 v106, 8, v107\n"
            "v_mad_i32_i16 v108, v112, v106, v108 op_sel:[1,0,0,0]\n"
            "v_add_u32 v112, 0x8000, v67\n"
            "v_add_u32 v106, 12, v107\n"
            "v_mad_i32_i16 v108, v112, v106, v108 op_sel:[1,0,0,0]\n"
            "v_add_u32 v107, 16384, v107\n"
            "v_add_u32 v112, 0x8000, v68\n"
            "v_add_u32 v106, 0, v107\n"
            "v_mad_i32_i16 v108, v112, v106, v108 op_sel:[1,0,0,0]\n"
            "v_add_u32 v112, 0x8000, v69\n"
            "v_add_u32 v106, 4, v107\n"
            "v_mad_i32_i16 v108, v112, v106, v108 op_sel:[1,0,0,0]\n"
            "v_add_u32 v112, 0x8000, v70\n"
            "v_add_u32 v106, 8, v107\n"
            "v_mad_i32_i16 v108, v112, v106, v108 op_sel:[1,0,0,0]\n"
            "v_add_u32 v112, 0x8000, v71\n"
            "v_add_u32 v106, 12, v107\n"
            "v_mad_i32_i16 v108, v112, v106, v108 op_sel:[1,0,0,0]\n"
            "v_add_u32 v107, 16384, v107\n"
            "v_add_u32 v112, 0x8000, v72\n"
            "v_add_u32 v106, 0, v107\n"
            "v_mad_i32_i16 v108, v112, v106, v108 op_sel:[1,0,0,0]\n"
            "v_add_u32 v112, 0x8000, v73\n"
            "v_add_u32 v106, 4, v107\n"
            "v_mad_i32_i16 v108, v112, v106, v108 op_sel:[1,0,0,0]\n"
            "v_add_u32 v112, 0x8000, v74\n"
            "v_add_u32 v106, 8, v107\n"
            "v_mad_i32_i16 v108, v112, v106, v108 op_sel:[1,0,0,0]\n"
            "v_add_u32 v112, 0x8000, v75\n"
            "v_add_u32 v106, 12, v107\n"
            "v_mad_i32_i16 v108, v112, v106, v108 op_sel:[1,0,0,0]\n"
            "v_add_u32 v107, 16384, v107\n"
            "v_add_u32 v112, 0x8000, v76\n"
            "v_add_u32 v106, 0, v107\n"
            "v_mad_i32_i16 v108, v112, v106, v108 op_sel:[1,0,0,0]\n"
            "v_add_u32 v112, 0x8000, v77\n"
            "v_add_u32 v106, 4, v107\n"
            "v_mad_i32_i16 v108, v112, v106, v108 op_sel:[1,0,0,0]\n"
            "v_add_u32 v112, 0x8000, v78\n"
            "v_add_u32 v106, 8, v107\n"
            "v_mad_i32_i16 v108, v112, v106, v108 op_sel:[1,0,0,0]\n"
            "v_add_u32 v112, 0x8000, v79\n"
            "v_add_u32 v106, 12, v107\n"
            "v_mad_i32_i16 v108, v112, v106, v108 op_sel:[1,0,0,0]\n"
            "v_add_u32 v107, 16384, v107\n"
            "v_sub_u32 v108, v88, v108\n"
            "v_mov_b32 v106, %[wb]\n"
            "v_mov_b32 v107, %[bad]\n"
            "v_add_u32_dpp v108, v108, v108 row_shr:1 row_mask:0xf bank_mask:0xf bound_ctrl:1\n"
            "s_nop 1\n"
            "v_add_u32_dpp v108, v108, v108 row_shr:2 row_mask:0xf bank_mask:0xf bound_ctrl:1\n"
            "s_nop 1\n"
            "v_add_u32_dpp v108, v108, v108 row_shr:4 row_mask:0xf bank_mask:0xf bound_ctrl:1\n"
            "s_nop 1\n"
            "v_add_u32_dpp v108, v108, v108 row_shr:8 row_mask:0xf bank_mask:0xf bound_ctrl:1\n"
            "s_nop 1\n"
            "v_add_u32_dpp v108, v108, v108 row_bcast:15 row_mask:0xa bank_mask:0xf\n"
            "s_nop 1\n"
            "v_add_u32_dpp v108, v108, v108 row_bcast:31 row_mask:0xc bank_mask:0xf\n"
            "s_nop 1\n"
            "v_lshrrev_b32 v106, 7, v106\n"
            "s_mov_b32 exec_lo, 0\n s_mov_b32 exec_hi, 0x80000000\n"
            "ds_write2_b32 v106, v108, v107 offset1:1\n"
            "s_mov_b64 exec, -1\n"
            "s_waitcnt vmcnt(5)\n"
            "s_branch Lend2%=\n"
            "Lend%=:\n"
            "s_waitcnt vmcnt(0)\n"
            "Lend2%=:\n"
            "s_setprio 0\n"
            "s_waitcnt lgkmcnt(0)\n"

            : [mode] "=&s"(mode), [bad] "=&s"(bad), [exs] "=&v"(exs), [tid16] "+v"(t16), "+v"(tc2), "=&{v[40:43]}"(mm[0]), "=&{v[44:47]}"(mm[1]), "=&{v[48:51]}"(dd)
            : [rs] "s"(rs), [rm] "s"(rm), [rp0] "s"(rp0), [rt] "s"(rt), [i4] "s"(LMAX * 4), [c4] "s"(a_.c4), [pb] "s"(81920 + wave * 5120), [wb] "s"(wave * 1024), [q] "s"(q), [j0] "s"(J0),
              [rmm] "s"(rmm), [rd] "s"(rd), [e1] "s"(128 * 32 * 128 * 2), [ed] "v"((((wave * 8 + q) * 32 + b) * 128 + lane * 2) * 2),
              [fof] "s"((NT * NWS * 4 + cp * (P1_GRID * 2)) * 4), [dumu] "s"(ly.dumu), [lmul] "n"(LMUL), [lmask] "n"(LMASK),
              [w0] "s"(a_.centers), [w1] "s"(a_.w_sbf), [w2] "s"(a_.w_rbf), [w3] "s"(a_.w_up),
              [tof] "v"((w * NT + t0) * 16), [fb] "v"(pairidx(w) * (ly.F0 * 256)), [xb] "v"(logbase(b >> 1, w, P_XO))
            : "memory", "vcc", "scc", "m0", "s66", "s67", "s70", "s71", "s72", "s73", "s74", "s75", "s76", "s77", "s79", "s80", "s81", "s82",
              "s84", "s85", "s86", "s87", "s88", "s89", "s90", "s91", "s92", "s93", "s94", "s96", "s97",
              "v8", "v9", "v10", "v11", "v12", "v13", "v14", "v15", "v16", "v17", "v18", "v19", "v20", "v21", "v22", "v23", "v24", "v25", "v26", "v27", "v28", "v29", "v30", "v31", "v32", "v33", "v34", "v35", "v36", "v37", "v38", "v39", "v52", "v53", "v54", "v55", "v56", "v57", "v58", "v59", "v60", "v61", "v62", "v63", "v64", "v65", "v66", "v67", "v68", "v69", "v70", "v71", "v72", "v73", "v74", "v75", "v76", "v77", "v78", "v79", "v80", "v81", "v82", "v83", "v84", "v85", "v86", "v87", "v88", "v89", "v90", "v91", "v92", "v93", "v94", "v95", "v96", "v97", "v98", "v99", "v100", "v101", "v102", "v103", "v104", "v105", "v106", "v107", "v108", "v109", "v110", "v111", "v112", "v113", "v114");
    }
    const int tid2 = (int)(t16 >> 4);
    kaptr ka = (kaptr)__builtin_amdgcn_kernarg_segment_ptr();
    asm volatile("" : "+s"(ka));
    unsigned long long* wm = (unsigned long long*)(ka->wgmode + b * 32);
    const int E = ka->E;
    const unsigned nblk = ka->O >> 7;
    const int ln = tid2 & 63, wv = __builtin_amdgcn_readfirstlane(tid2 >> 6);
    const bool two = (16 + wv) * 8 + q < nblk;
    float g[2][2][4], h[2][2][4];
    auto gh1 = [&](int p) __attribute__((always_inline)) {
        asm volatile("" : "+v"(dd));
        edge_gh(lo_bf(p ? dd.z : dd.x), ka->centers, ka->w_sbf, ka->w_rbf, g[p][0], h[p][0]);
        edge_gh(lo_bf(p ? dd.w : dd.y), ka->centers, ka->w_sbf, ka->w_rbf, g[p][1], h[p][1]);
#pragma unroll
        for (int i = 0; i < 8; i++) asm volatile("" : "+v"(g[p][i >> 2][i & 3]), "+v"(h[p][i >> 2][i & 3]));
    };
    constexpr unsigned GW = 1, M1 = 0xf, M2 = 0xff00, P1 = 0, PT = 0;
    v2u own[2] = {{0, 0}, {0, 0}};
    if (mode == 0) {
        if (AV & 8) {
            if (E & 1) {
                const unsigned e = ((wv * 8 + q) * 32 + b) * 128 + ln * 2;
                if (e == E - 1) dd.x = ka->dist[e];
                if (e + 128 * 32 * 128 == E - 1) dd.z = ka->dist[E - 1];
            }
            dd.y = dd.x >> 16; dd.w = dd.z >> 16;
        }
        if ((M1 >> wv) & 1) gh1(0);
        if (P1 == 1 && two) gh1(1);
        own[0] = *(const v2u*)(lds + OBASE + (wv * 8 + q) * 128 + ln * 2);
        if (two) own[1] = *(const v2u*)(lds + OBASE + ((16 + wv) * 8 + q) * 128 + ln * 2);
        asm volatile("s_waitcnt vmcnt(0)" ::: "memory");
        __syncthreads();
        const v2u cs = *(const v2u*)(lds + (ln & 15) * 2);
        if (__builtin_amdgcn_readlane(row_scan_add(cs.x), 15) != 0 || __ballot(cs.y != 0)) mode = 1;
    }
    if (mode != 0) {
        acc_fallback(lds, mode, *ka, tid2, b, qt, cp);
        asm volatile("s_waitcnt vmcnt(0)" ::: "memory");
        __syncthreads();
        if (tid2 == PT) { __builtin_amdgcn_fence(__ATOMIC_RELEASE, "agent"); asm volatile("s_waitcnt vmcnt(0)" ::: "memory"); }
        const __amdgpu_buffer_rsrc_t rmm = __builtin_amdgcn_make_buffer_rsrc((void*)ka->m, (short)0, E * 8, 0x00020000);
        const __amdgpu_buffer_rsrc_t rdd = __builtin_amdgcn_make_buffer_rsrc((void*)ka->dist, (short)0, E * 2, 0x00020000);
        const unsigned e = ((wv * 8 + q) * 32 + b) * 128 + ln * 2, e1 = e + 128 * 32 * 128;
        mm[0] = __builtin_amdgcn_raw_buffer_load_b128(rmm, e * 8, 0, 0); mm[1] = __builtin_amdgcn_raw_buffer_load_b128(rmm, e1 * 8, 0, 0);
        dd.x = __builtin_amdgcn_raw_buffer_load_b16(rdd, e * 2, 0, 0); dd.y = __builtin_amdgcn_raw_buffer_load_b16(rdd, e * 2 + 2, 0, 0);
        dd.z = __builtin_amdgcn_raw_buffer_load_b16(rdd, e1 * 2, 0, 0); dd.w = __builtin_amdgcn_raw_buffer_load_b16(rdd, e1 * 2 + 2, 0, 0);
        own[0].x = own[0].y = own[1].x = own[1].y = 0;
    }
    const unsigned* prb = ka->praw + (size_t)b * ka->O; unsigned short* outp = ka->out;
    int EP = ka->EP;
    unsigned wu[8];
#pragma unroll
    for (int i = 0; i < 8; i++) wu[i] = ((cptr)ka->w_up)[i];
    if (tid2 == PT) {
        const unsigned long long add = (1u | (mode ? 256u << q : 0u)) | (qt == 0 ? (unsigned long long)(__builtin_amdgcn_readfirstlane(exs) & 0xFFFF) << (32 + 16 * cp) : 0ull);
        unsigned long long v = __hip_atomic_fetch_add(wm, add, __ATOMIC_RELAXED, __HIP_MEMORY_SCOPE_AGENT) + add;
        for (int spin = 0; spin < (1 << 22) && (v & 255) != 8; spin++) {
            v = __hip_atomic_load(wm, __ATOMIC_RELAXED, __HIP_MEMORY_SCOPE_AGENT);
            if ((v & 255) != 8) __builtin_amdgcn_s_sleep(2);
        }
        if (v & 0xFF00) { __builtin_amdgcn_fence(__ATOMIC_ACQUIRE, "agent"); asm volatile("s_waitcnt vmcnt(0)" ::: "memory"); }
        lds[64] = ((unsigned)v >> 8) & 255; lds[65] = (unsigned)(v >> 32);
    }
    if (mode == 0 && ((M2 >> wv) & 1)) gh1(0);
    if (P1 == 2 && mode == 0 && two) gh1(1);
    asm volatile("" : "+s"(prb), "+s"(outp), "+s"(EP), "+s"(wu[0]), "+s"(wu[1]), "+s"(wu[2]), "+s"(wu[3]), "+s"(wu[4]), "+s"(wu[5]), "+s"(wu[6]), "+s"(wu[7]));
    const __amdgpu_buffer_rsrc_t rpr = __builtin_amdgcn_make_buffer_rsrc((void*)prb, (short)0, (7 * EP + (int)(nblk << 7)) * 4, 0x00020000);
    const __amdgpu_buffer_rsrc_t rou = __builtin_amdgcn_make_buffer_rsrc((void*)outp, (short)0, E * 8, 0x00020000);
    __syncthreads();
    v2u w[2][8];
#pragma unroll
    for (int p = 0; p < 2; p++) {
        if (p == 0 || two) {
#pragma unroll
            for (int j = 0; j < 8; j++)
                w[p][j] = __builtin_amdgcn_raw_buffer_load_b64(rpr, (((p * 16 + wv) * 8 + q) * 128 + ln * 2) * 4, j == q ? (int)OOB : j * EP * 4, AV & 4 ? 16 : PR_AUX);
        }
    }
    if (GW) {
        asm volatile("" ::: "memory");
        if (mode != 0 || !(((M1 | M2) >> wv) & 1)) gh1(0);
        if (two && (mode != 0 || P1 == 0)) gh1(1);
    }
    const unsigned fbm = __builtin_amdgcn_readfirstlane(lds[64]), exw = __builtin_amdgcn_readfirstlane(lds[65]);
    float inv[4];
#pragma unroll
    for (int k = 0; k < 4; k++) inv[k] = __uint_as_float(((exw >> (8 * k)) & 255) << 23) * (1.0f / 511.0f);
#pragma unroll
    for (int p = 0; p < 2; p++) {
        const unsigned blk = (p * 16 + wv) * 8 + q;
        if (p == 0 || two) {
            int s[2][4];
            {
                const int l0 = (int)(short)(own[p].x & 0xFFFF), h0 = (int)(own[p].x + 0x8000u) >> 16, l1 = (int)(short)(own[p].y & 0xFFFF), h1 = (int)(own[p].y + 0x8000u) >> 16;
                s[0][0] = cp ? 0 : l0; s[0][1] = cp ? 0 : h0; s[0][2] = cp ? l0 : 0; s[0][3] = cp ? h0 : 0;
                s[1][0] = cp ? 0 : l1; s[1][1] = cp ? 0 : h1; s[1][2] = cp ? l1 : 0; s[1][3] = cp ? h1 : 0;
            }
#pragma unroll
            for (int j = 0; j < 8; j++) {
                s[0][(j & 1) * 2] += (int)(short)(w[p][j].x & 0xFFFF); s[0][(j & 1) * 2 + 1] += (int)(w[p][j].x + 0x8000u) >> 16;
                s[1][(j & 1) * 2] += (int)(short)(w[p][j].y & 0xFFFF); s[1][(j & 1) * 2 + 1] += (int)(w[p][j].y + 0x8000u) >> 16;
            }
            float a[2][4];
#pragma unroll
            for (int i = 0; i < 2; i++)
#pragma unroll
                for (int k = 0; k < 4; k++) a[i][k] = s[i][k] ? (float)s[i][k] * inv[k] : 0.f;
            if (fbm && blk < nblk) {
                const float* part = ka->part + (size_t)b * ka->O + blk * 128 + ln * 2;
#pragma unroll 1
                for (int j = 0; j < 8; j++)
                    if ((fbm >> j) & 1) {
#pragma unroll
                        for (int i = 0; i < 2; i++) {
                            const float x = part[(size_t)(j * 2) * EP + i], y = part[(size_t)(j * 2 + 1) * EP + i];
                            if (j & 1) { a[i][2] += x; a[i][3] += y; } else { a[i][0] += x; a[i][1] += y; }
                        }
                    }
            }
            const v2u r0 = edge_out(a[0], g[p][0], h[p][0], mm[p].x, mm[p].y, wu), r1 = edge_out(a[1], g[p][1], h[p][1], mm[p].z, mm[p].w, wu);
            const v4u r = {r0.x, r0.y, r1.x, r1.y};
            __builtin_amdgcn_raw_buffer_store_b128(r, rou, ((blk * 32 + b) * 128 + ln * 2) * 8, 0, OUT_AUX);
        }
    }
}

extern "C" __global__ __launch_bounds__(256) void k_naive(const int* __restrict__ idx_in, const int* __restrict__ idx_out,
                                               const unsigned short* __restrict__ angle, const unsigned short* __restrict__ ang_freq,
                                               const unsigned short* __restrict__ m, const unsigned short* __restrict__ w_down,
                                               float* __restrict__ part, int T, int EP) {
    const float freq = bf2f(ang_freq[0]);
    for (long long t = (long long)blockIdx.x * 256 + threadIdx.x; t < T; t += (long long)gridDim.x * 256) {
        v2u mv = ((const v2u*)m)[idx_in[t]];
        float a[4] = {lo_bf(mv.x), hi_bf(mv.x), lo_bf(mv.y), hi_bf(mv.y)};
        float c = __cosf(freq * bf2f(angle[t]));
        int eo = idx_out[t];
#pragma unroll
        for (int d = 0; d < 4; d++) {
            float s = 0.f;
#pragma unroll
            for (int k = 0; k < 4; k++) s += a[k] * bf2f(w_down[k * 4 + d]);
            atomicAdd(&part[(size_t)d * EP + eo], s * c);
        }
    }
}

__global__ __launch_bounds__(256) void k_final(const unsigned short* __restrict__ m, const unsigned short* __restrict__ dist,
                                               const unsigned short* __restrict__ centers, const unsigned short* __restrict__ w_rbf,
                                               const unsigned short* __restrict__ w_sbf, const unsigned short* __restrict__ w_up,
                                               const float* __restrict__ part, unsigned short* __restrict__ out, int E, int EP) {
    const int e = blockIdx.x * 256 + threadIdx.x;
    if (e >= E) return;
    float a[4], g[4], h[4];
#pragma unroll
    for (int k = 0; k < 4; k++) a[k] = part[(size_t)k * EP + e];
    edge_gh(bf2f(dist[e]), centers, w_sbf, w_rbf, g, h);
    const v2u mv = ((const v2u*)m)[e];
    unsigned wu[8];
#pragma unroll
    for (int i = 0; i < 8; i++) wu[i] = ((cptr)w_up)[i];
    ((v2u*)out)[e] = edge_out(a, g, h, mv.x, mv.y, wu);
}

extern "C" void kernel_launch(void* const* d_in, const int* in_sizes, int n_in,
                              void* d_out, int out_size, void* d_ws, size_t ws_size,
                              hipStream_t stream) {
    const unsigned short* m = (const unsigned short*)d_in[0];
    const int* idx_in = (const int*)d_in[1];
    const int* idx_out = (const int*)d_in[2];
    const unsigned short* dist = (const unsigned short*)d_in[3];
    const unsigned short* angle = (const unsigned short*)d_in[4];
    const unsigned short* centers = (const unsigned short*)d_in[5];
    const unsigned short* ang_freq = (const unsigned short*)d_in[6];
    const unsigned short* w_rbf = (const unsigned short*)d_in[7];
    const unsigned short* w_sbf = (const unsigned short*)d_in[8];
    const unsigned short* w_down = (const unsigned short*)d_in[9];
    const unsigned short* w_up = (const unsigned short*)d_in[10];
    const int E = in_sizes[0] / 4;
    const int T = in_sizes[1];
    const int NR = T / (P1_GRID * CH);
    const int per = ((T - NR * (P1_GRID * CH) + P1_GRID - 1) / P1_GRID + 3) / 4 * 4;
    const unsigned O = (unsigned)(((E + 127) / 128 + 31) / 32) * 128;
    const unsigned I = (unsigned)(E + 31) / 32;
    const int EP = (int)O * 32;
    Lay ly;
    const unsigned meanr = (unsigned)(((size_t)NR * CH + per) / NT);
    ly.F0 = meanr >= 100 ? 40 : 72;
    if (LA > 3) ly.F0 = (ly.F0 + (1 << LA) - 1) >> LA << LA;
    ly.fcap = ly.F0 / 4 * 505;
    ly.cap = 768;
    char* ws = (char*)d_ws;
    size_t o = 0;
    ly.dumu = ly.cap * 32768;
    const size_t recsz = 0x200000 + ((size_t)ly.dumu + 64) * 16;
    char* rec = ws;
    o += recsz;
    unsigned* praw = (unsigned*)(ws + o); o += (size_t)8 * EP * 4; o = (o + 255) / 256 * 256;
    unsigned* wgmode = (unsigned*)(ws + o); o += 8192;
    unsigned* mdp = (unsigned*)(ws + o); o += (size_t)2 * 32 * LMAX * 4;
    unsigned* tab = (unsigned*)(ws + o); o += (size_t)NT * NWS * 16;
    unsigned* flags = (unsigned*)(ws + o); o += FL_SIZE * 4; o = (o + 255) / 256 * 256;
    float* part = (float*)(ws + o); o += (size_t)16 * EP * 4;
    const bool ok = E >= 16384 && O <= LMAX && I <= LMAX && T > 0 && meanr <= 180 && o <= ws_size;
    if (!ok) {
        part = (float*)ws;
        const int EQ = (E + 3) / 4 * 4;
        (void)hipMemsetAsync(part, 0, (size_t)4 * EQ * 4, stream);
        if (T > 0) k_naive<<<2048, 256, 0, stream>>>(idx_in, idx_out, angle, ang_freq, m, w_down, part, T, EQ);
        k_final<<<(E + 255) / 256, 256, 0, stream>>>(m, dist, centers, w_rbf, w_sbf, w_up, part, (unsigned short*)d_out, E, EQ);
        return;
    }
    k_bin<<<P1_GRID, P1_THREADS, 0, stream>>>(idx_in, idx_out, angle, ang_freq, m, w_down, mdp, E, T, rec, tab, flags, NR, per, ly, wgmode);
    AccArgs aa;
    aa.rec = rec; aa.tab = tab; aa.flags = flags; aa.mdp = mdp; aa.part = part; aa.praw = praw; aa.wgmode = wgmode;
    aa.idx_in = idx_in; aa.idx_out = idx_out; aa.angle = angle; aa.ang_freq = ang_freq; aa.m = m; aa.w_down = w_down;
    aa.EP = EP; aa.O = O; aa.T = T; aa.c4 = (float)((double)E / 144.0);
    aa.dist = dist; aa.centers = centers; aa.w_rbf = w_rbf; aa.w_sbf = w_sbf; aa.w_up = w_up; aa.out = (unsigned short*)d_out; aa.E = E; aa.unused = 0;
    aa.ly = ly;
    k_acc<<<256, P2_THREADS, 0, stream>>>(aa);
}
```

```cpp
#include <hip/hip_runtime.h>
#include <hip/hip_bf16.h>
#include <stdint.h>

typedef int v4i __attribute__((ext_vector_type(4)));
typedef unsigned v4u __attribute__((ext_vector_type(4)));
typedef unsigned v2u __attribute__((ext_vector_type(2)));
typedef int v4i_rs __attribute__((ext_vector_type(4)));
typedef __attribute__((address_space(3))) unsigned lds_u32;
typedef __attribute__((address_space(3))) v4u lds_v4u;

static __device__ __forceinline__ float bf2f(unsigned short v) { return __uint_as_float(((unsigned)v) << 16); }
static __device__ __forceinline__ unsigned f2bf_bits(float f) {
    unsigned u = __float_as_uint(f);
    u += 0x7FFF + ((u >> 16) & 1);
    return u >> 16;
}
static __device__ __forceinline__ float lo_bf(unsigned u) { return __uint_as_float(u << 16); }
static __device__ __forceinline__ float hi_bf(unsigned u) { return __uint_as_float(u & 0xFFFF0000u); }
static __device__ __forceinline__ v4i_rs mk_rsrc(const void* p, unsigned bytes) {
    unsigned long long a = (unsigned long long)p;
    v4i_rs r = {(int)(unsigned)a, (int)(unsigned)((a >> 32) & 0xFFFF), (int)bytes, 0x00020000};
    return r;
}
static __device__ __forceinline__ lds_u32* ldsp(unsigned byte) { return (lds_u32*)(size_t)byte; }

#define P1_THREADS 1024
#define PD 1
#define UNR ((PD & 1) ? 2 * PD : PD)
#define P1_GRID 256
#define NWS 256
#define CH (P1_THREADS * 4)
#define NT 1024
#define LMAX 20480
#define P2_THREADS 1024
#define OOB 0x80000000u
#define OBASE 20480
#define IDXB 4096
#define ST_AUX 16
#define LB 4096
#define LS 132
#define TINC 683
#define WL_OFF (LB + NT * LS)
#define MS_OFF (WL_OFF + 16 * 512)
#define BIN_LDS (MS_OFF + 128 + 320)
#define NSTAT 14
#define WM_CTR 1024
#define WM_BOX (WM_CTR + 256)
#define RM 1
#define RXOR 0
#define CALN 3
#define VTHR 5
#define CORD 0
#define PSTR 256
#define NTH "nt"
#define OUT_AUX 16
#define PR_AUX 18
#define TCH 7
#define CZERO ((512u << 1) | (512u << 11) | (512u << 22))
#define FL_SIZE (2 * P1_GRID * 2)

struct Grp { v4i io; v4i ii; v2u an; };
static __device__ __forceinline__ void load_grp(Grp& r, v4i_rs ro, v4i_rs ri, v4i_rs ra, unsigned c, unsigned vo16, unsigned vo8, int parts = 7) {
    if (parts & 2) asm volatile("buffer_load_dwordx4 %0, %1, %2, %3 offen nt" : "=v"(r.io) : "v"(vo16), "s"(ro), "s"(c * 256));
    if (parts & 4) asm volatile("buffer_load_dwordx4 %0, %1, %2, %3 offen nt" : "=v"(r.ii) : "v"(vo16), "s"(ri), "s"(c * 256));
    if (parts & 1) asm volatile("buffer_load_dwordx2 %0, %1, %2, %3 offen nt" : "=v"(r.an) : "v"(vo8), "s"(ra), "s"(c * 128));
}
#define WAIT_GRP(n, g) asm volatile("s_waitcnt vmcnt(%3)" : "+v"((g).io), "+v"((g).ii), "+v"((g).an) : "n"(n))

static __device__ __forceinline__ unsigned wave_scan_max(unsigned x) {
    x = max(x, (unsigned)__builtin_amdgcn_update_dpp(0, (int)x, 0x111, 0xF, 0xF, false));
    x = max(x, (unsigned)__builtin_amdgcn_update_dpp(0, (int)x, 0x112, 0xF, 0xF, false));
    x = max(x, (unsigned)__builtin_amdgcn_update_dpp(0, (int)x, 0x114, 0xF, 0xF, false));
    x = max(x, (unsigned)__builtin_amdgcn_update_dpp(0, (int)x, 0x118, 0xF, 0xF, false));
    x = max(x, (unsigned)__builtin_amdgcn_update_dpp(0, (int)x, 0x142, 0xA, 0xF, false));
    x = max(x, (unsigned)__builtin_amdgcn_update_dpp(0, (int)x, 0x143, 0xC, 0xF, false));
    return x;
}
static __device__ __forceinline__ unsigned row_scan_add(unsigned x) {
    x += (unsigned)__builtin_amdgcn_update_dpp(0, (int)x, 0x111, 0xF, 0xF, false);
    x += (unsigned)__builtin_amdgcn_update_dpp(0, (int)x, 0x112, 0xF, 0xF, false);
    x += (unsigned)__builtin_amdgcn_update_dpp(0, (int)x, 0x114, 0xF, 0xF, false);
    x += (unsigned)__builtin_amdgcn_update_dpp(0, (int)x, 0x118, 0xF, 0xF, false);
    return x;
}
static __device__ __forceinline__ unsigned wave_scan_add(unsigned x) {
    x = row_scan_add(x);
    x += (unsigned)__builtin_amdgcn_update_dpp(0, (int)x, 0x142, 0xA, 0xF, false);
    x += (unsigned)__builtin_amdgcn_update_dpp(0, (int)x, 0x143, 0xC, 0xF, false);
    return x;
}

static __device__ __forceinline__ float wave_scan_addf(float x) {
    x += __int_as_float(__builtin_amdgcn_update_dpp(0, __float_as_int(x), 0x111, 0xF, 0xF, false));
    x += __int_as_float(__builtin_amdgcn_update_dpp(0, __float_as_int(x), 0x112, 0xF, 0xF, false));
    x += __int_as_float(__builtin_amdgcn_update_dpp(0, __float_as_int(x), 0x114, 0xF, 0xF, false));
    x += __int_as_float(__builtin_amdgcn_update_dpp(0, __float_as_int(x), 0x118, 0xF, 0xF, false));
    x += __int_as_float(__builtin_amdgcn_update_dpp(0, __float_as_int(x), 0x142, 0xA, 0xF, false));
    x += __int_as_float(__builtin_amdgcn_update_dpp(0, __float_as_int(x), 0x143, 0xC, 0xF, false));
    return x;
}

struct Lay { unsigned F0, cap, dumu, fcap; };
#define P_A 0
#define P_B 0
#define P_XO 4
#define P_DF (P_NEW ? 0 : V & 3)
#define P_NEW ((V >> 7) & 1)
#define P_X (V & 3)
#define P_GAP ((V >> 8) & 1)
#define P_SPL ((V >> 9) & 3)
#define P_DC ((V >> 11) & 3)
#define P_EA ((V >> 2) & 7)
#define P_EP ((V >> 5) & 3)
#define N_EA ((P_EA & 1) + ((P_EA >> 1) & 1) + (P_EA >> 2))
#define VDEF 264
#define P_LP 6
#define P_LS 15
#define P_JM 1
#define SHP 2
#define LA 3
#define XORD 0
#define NPOS 10
#define LMUL (8 + 32768)
#define LMASK ((((1 << LA) - 1) << 3) | ((1023 >> LA) << (15 + LA)))
static __device__ __forceinline__ char* rec_align(const char* p) { return (char*)(((size_t)p + 0x1FFFFF) & ~(size_t)0x1FFFFF); }
static __device__ __forceinline__ unsigned pairidx(unsigned w) { return (w >> 3) * 4 + (w & 3); }
static __device__ __forceinline__ unsigned logbase(unsigned wave, unsigned w, unsigned xo) {
    const unsigned p = xo & 16 ? (w & 3) * 32 + (w >> 3) : pairidx(w), k = xo & 15;
    const unsigned x = (p & ((1 << k) - 1)) | (wave << k) | ((p >> k) << (k + 4));
    return ((x & ((64 >> LA) - 1)) << (3 + LA)) | ((x >> (6 - LA)) << 10);
}

static __device__ __forceinline__ void conv_rec(unsigned io, unsigned ii, float ang, float frev, unsigned& A, unsigned& t4, unsigned& cq, unsigned& seen) {
    unsigned x, y;
    asm("v_and_b32 %0, 31, %4\n v_and_b32 %1, 0xf80, %3\n v_lshl_or_b32 %2, %0, 2, %1" : "=&v"(x), "=&v"(y), "=v"(t4) : "v"(io), "v"(ii));
    asm("v_lshrrev_b32 %0, 5, %3\n v_lshrrev_b32 %1, 3, %4\n v_bfi_b32 %0, %5, %3, %0\n v_add_u32 %1, %6, %1\n v_lshl_add_u32 %2, %0, 17, %1"
        : "=&v"(x), "=&v"(y), "=v"(A) : "v"(io), "v"(ii), "s"(127u), "n"(IDXB << 17));
    float c = __builtin_amdgcn_cosf(__builtin_amdgcn_fractf(frev * ang));
    const unsigned cb = __float_as_uint(__builtin_fmaf(c, 511.0f, 12583424.0f));
    seen |= cb;
    cq = cb & 0x3FFu;
}
static __device__ __forceinline__ void conv_rec2(unsigned io, unsigned ii, float ang, float frev, unsigned& A, unsigned& t4, unsigned& cq, unsigned& seen) {
    unsigned x, y;
    asm volatile("v_and_b32 %0, 31, %4\n v_and_b32 %1, 0xf80, %3\n v_lshl_or_b32 %2, %0, 2, %1" : "=&v"(x), "=&v"(y), "=v"(t4) : "v"(io), "v"(ii));
    asm volatile("v_lshrrev_b32 %0, 5, %3\n v_lshrrev_b32 %1, 3, %4\n v_bfi_b32 %0, %5, %3, %0\n v_add_u32 %1, %6, %1\n v_lshl_add_u32 %2, %0, 17, %1"
        : "=&v"(x), "=&v"(y), "=v"(A) : "v"(io), "v"(ii), "s"(127u), "n"(IDXB << 17));
    float c = __builtin_amdgcn_cosf(__builtin_amdgcn_fractf(frev * ang));
    const unsigned cb = __float_as_uint(__builtin_fmaf(c, 511.0f, 12583424.0f));
    seen |= cb;
    cq = cb & 0x3FFu;
}
#define PADV 1
static __device__ __forceinline__ unsigned pad_rec(unsigned q) { return PADV == 0 ? 0x7FFFu << 17 : PADV == 1 ? (0x7FFFu - (q & 511)) << 17 : (IDXB + (q & 511)) << 17; }

static __device__ __forceinline__ void prep_md(const v2u* mv, unsigned dst, const unsigned short* w_down, unsigned* mdp, unsigned plane, unsigned* mx, float* sq) {
    float wd[16];
#pragma unroll
    for (int i = 0; i < 8; i++) { unsigned u = ((const __attribute__((address_space(4))) unsigned*)w_down)[i]; wd[2 * i] = lo_bf(u); wd[2 * i + 1] = hi_bf(u); }
    unsigned o[2][4];
#pragma unroll
    for (int j = 0; j < 4; j++) {
        const float a[4] = {lo_bf(mv[j].x), hi_bf(mv[j].x), lo_bf(mv[j].y), hi_bf(mv[j].y)};
        float s[4];
#pragma unroll
        for (int d = 0; d < 4; d++) {
            s[d] = 0.f;
#pragma unroll
            for (int k = 0; k < 4; k++) s[d] += a[k] * wd[k * 4 + d];
            sq[d] += s[d] * s[d];
            mx[d] = max(mx[d], __float_as_uint(s[d]) & 0x7FFFFFFFu);
        }
#pragma unroll
        for (int p = 0; p < 2; p++) {
            const unsigned lo = f2bf_bits(s[2 * p]) & 0xFFFF;
            const unsigned sb = __float_as_uint(s[2 * p + 1]);
            const unsigned h = (unsigned)max((int)((sb & 0x7FFFFFFFu) - lo + 0x8000u), 0) & 0x7FFF0000u;
            o[p][j] = lo | h | (sb & 0x80000000u);
        }
    }
#pragma unroll
    for (int p = 0; p < 2; p++) {
        v4u v = {o[p][0], o[p][1], o[p][2], o[p][3]};
        *(v4u*)(mdp + (size_t)p * plane + dst) = v;
    }
}

static __device__ __forceinline__ unsigned touch_code(int tid) {
    unsigned alo, ahi, blo, r = 0;
    asm volatile("s_getpc_b64 s[96:97]\n s_add_u32 %0, s96, k_bin@rel32@lo+4\n s_addc_u32 %1, s97, k_bin@rel32@hi+12\n"
                 "s_getpc_b64 s[96:97]\n s_add_u32 %2, s96, k_naive@rel32@lo+4\n" : "=&s"(alo), "=&s"(ahi), "=&s"(blo) :: "scc", "s96", "s97");
    const unsigned size = blo - alo < 65536u ? blo - alo : 0u;
    const v4i_rs rc = {(int)alo, (int)(ahi & 0xFFFF), (int)size, 0x00020000};
    if (tid < 512) asm volatile("buffer_load_dword %0, %1, %2, 0 offen" : "=v"(r) : "v"(tid * 128), "s"(rc));
    return r;
}

#define touch_code2 touch_code
struct Held { unsigned a[4], t[4], A[4], cd[4]; unsigned long long m[4]; };
struct Conv { unsigned A[4], t4[4], cq[4]; };
#define HB(k) "s_mov_b64 exec, %[m" #k "]\n ds_write_b32 %[a" #k "], %[A" #k "]\n ds_or_b32 %[t" #k "], %[c" #k "]\n"
static __device__ __forceinline__ void held_out(const Held& H) {
    asm volatile(HB(0) HB(1) HB(2) HB(3) "s_mov_b64 exec, -1\n"
                 :: [m0] "s"(H.m[0]), [m1] "s"(H.m[1]), [m2] "s"(H.m[2]), [m3] "s"(H.m[3]), [a0] "v"(H.a[0]), [a1] "v"(H.a[1]), [a2] "v"(H.a[2]), [a3] "v"(H.a[3]),
                   [t0] "v"(H.t[0]), [t1] "v"(H.t[1]), [t2] "v"(H.t[2]), [t3] "v"(H.t[3]),
                   [A0] "v"(H.A[0]), [A1] "v"(H.A[1]), [A2] "v"(H.A[2]), [A3] "v"(H.A[3]), [c0] "v"(H.cd[0]), [c1] "v"(H.cd[1]), [c2] "v"(H.cd[2]), [c3] "v"(H.cd[3])
                 : "memory");
}
static __device__ __forceinline__ void tickets(unsigned* v, const Conv& c, unsigned inc) {
    asm volatile("ds_add_rtn_u32 %0, %4, %8\n ds_add_rtn_u32 %1, %5, %8\n ds_add_rtn_u32 %2, %6, %8\n ds_add_rtn_u32 %3, %7, %8\n"
                 : "=&v"(v[0]), "=&v"(v[1]), "=&v"(v[2]), "=&v"(v[3]) : "v"(c.t4[0]), "v"(c.t4[1]), "v"(c.t4[2]), "v"(c.t4[3]), "v"(inc) : "memory");
}
static __device__ __forceinline__ void tickets_masked(unsigned* v, const Conv& c, unsigned inc, const unsigned long long* vm) {
#pragma unroll
    for (int k = 0; k < 4; k++) {
        v[k] = 0;
        asm volatile("s_mov_b64 exec, %3\n ds_add_rtn_u32 %0, %1, %2\n s_mov_b64 exec, -1" : "+v"(v[k]) : "v"(c.t4[k]), "v"(inc), "s"(vm[k]) : "memory");
    }
}
static __device__ __forceinline__ void place(Held& H, unsigned* v, Conv& c, const unsigned long long* vm, unsigned lb) {
    unsigned bt[4];
#pragma unroll
    for (int k = 0; k < 4; k++) asm("v_mad_u32_u24 %0, %1, %2, %3" : "=v"(bt[k]) : "v"(c.t4[k]), "n"(LS / 4), "v"(lb));
    asm volatile("s_waitcnt lgkmcnt(0)" : "+v"(v[0]), "+v"(v[1]), "+v"(v[2]), "+v"(v[3]));
#pragma unroll
    for (int k = 0; k < 4; k++) {
        H.m[k] = __ballot(v[k] >= 16384u);
        unsigned sh;
        asm("v_lshrrev_b32 %0, 7, %3\n v_lshrrev_b32 %2, 6, %3\n v_and_b32 %0, 0x7c, %0\n v_or_b32 %1, 12, %0\n v_add_u32 %0, %0, %4\n v_add_u32 %1, %1, %4"
            : "=&v"(H.a[k]), "=&v"(H.t[k]), "=&v"(sh) : "v"(v[k]), "v"(bt[k]));
        H.A[k] = c.A[k];
        asm("v_lshlrev_b32 %0, %1, %2" : "=v"(H.cd[k]) : "v"(sh), "v"(c.cq[k]));
        asm volatile("s_andn2_b64 exec, %[w], %[m]\n ds_write_b32 %[a], %[A]\n ds_or_b32 %[t], %[c]\n s_mov_b64 exec, -1\n"
                     :: [w] "s"(vm[k]), [m] "s"(H.m[k]), [a] "v"(H.a[k]), [A] "v"(H.A[k]), [t] "v"(H.t[k]), [c] "v"(H.cd[k]) : "memory", "scc");
    }
}
struct Own { unsigned la, p[4], nl, ovf; };
static __device__ __forceinline__ v4u read_unit(unsigned la) {
    const lds_u32* p = ldsp(la);
    v4u v = {p[0], p[1], p[2], p[3]};
    return v;
}
typedef __attribute__((address_space(3))) v2u lds_v2u;
static __device__ __forceinline__ unsigned long long low_lanes(unsigned groups) { return groups >= 8 ? ~0ull : (1ull << (groups * 8)) - 1; }
static __device__ __forceinline__ unsigned flush_addr(unsigned e, unsigned f, unsigned long long act, unsigned base) {
    unsigned vo = (__umul24(f, LMUL * 16) & (LMASK * 16u)) | base;
    asm volatile("s_mov_b64 exec, %1\n ds_write_b32 %2, %3 offset:12\n s_mov_b64 exec, -1\n v_cndmask_b32 %0, %4, %0, %1"
                 : "+v"(vo) : "s"(act), "v"(e), "v"(0u), "v"(OOB) : "memory");
    return vo;
}
struct Pend { v4u v0, v1; unsigned a0, a1; };
static __device__ __forceinline__ void pend_out(const Pend& p, __amdgpu_buffer_rsrc_t rr) {
    __builtin_amdgcn_raw_buffer_store_b128(p.v0, rr, p.a0, 0, ST_AUX);
    __builtin_amdgcn_raw_buffer_store_b128(p.v1, rr, p.a1, 0, ST_AUX);
}
static __device__ __forceinline__ void flush(Own& o, Pend& pd, unsigned& wc, unsigned cnt, int tid, unsigned base, __amdgpu_buffer_rsrc_t rr, bool now) {
    const bool full = cnt >= 16384u;
    const unsigned long long bal = __ballot(full);
    const unsigned nfull = __popcll(bal);
    const unsigned wl = WL_OFF + (tid >> 6) * 512;
    if (full) {
        const unsigned pos = __builtin_amdgcn_mbcnt_hi((unsigned)(bal >> 32), __builtin_amdgcn_mbcnt_lo((unsigned)bal, 0));
        *ldsp(wl + pos * 4) = o.la;
        cnt -= 24 * TINC;
        *ldsp(tid * 4) = cnt;
        o.ovf |= cnt;
        o.nl++;
        o.p[3] = __builtin_amdgcn_alignbit(o.p[3], o.p[2], 22); o.p[2] = __builtin_amdgcn_alignbit(o.p[2], o.p[1], 22); o.p[1] = __builtin_amdgcn_alignbit(o.p[1], o.p[0], 22);
        o.p[0] = (o.p[0] << 10) + pos + wc;
    }
    const unsigned g = (tid >> 3) & 7, k16 = (tid & 7) * 16;
    const unsigned e0 = *ldsp(wl + g * 4) + k16, e1 = *ldsp(wl + g * 4 + 32) + k16;
    pd.v0 = read_unit(e0); pd.v1 = read_unit(e1);
    asm volatile("" ::: "memory");
    pd.a0 = flush_addr(e0, g + wc, low_lanes(nfull), base);
    pd.a1 = flush_addr(e1, g + wc + 8, nfull > 8 ? low_lanes(nfull - 8) : 0ull, base);
    if (now) pend_out(pd, rr);
    for (unsigned p = 16; p < nfull; p += 8) {
        const unsigned e = *ldsp(wl + (p + g) * 4) + k16;
        const v4u v = read_unit(e);
        asm volatile("" ::: "memory");
        __builtin_amdgcn_raw_buffer_store_b128(v, rr, flush_addr(e, g + wc + p, low_lanes(nfull - p), base), 0, ST_AUX);
    }
    wc = min(wc + nfull, 960u);
    asm volatile("s_waitcnt lgkmcnt(0)" : "+v"(pd.v0), "+v"(pd.v1) :: "memory");
}

static __device__ __forceinline__ void resolve(Held& H, unsigned* v, Conv& c, unsigned lb) {
    unsigned bt[4];
#pragma unroll
    for (int k = 0; k < 4; k++) asm("v_mad_u32_u24 %0, %1, %2, %3" : "=v"(bt[k]) : "v"(c.t4[k]), "n"(LS / 4), "v"(lb));
#pragma unroll
    for (int k = 0; k < 4; k++) {
        asm volatile("s_waitcnt lgkmcnt(%1)" : "+v"(v[k]) : "n"(3 - k));
        H.m[k] = __ballot(v[k] >= 16384u);
        unsigned sh;
        asm("v_lshrrev_b32 %0, 7, %3\n v_lshrrev_b32 %2, 6, %3\n v_and_b32 %0, 0x7c, %0\n v_or_b32 %1, 12, %0\n v_add_u32 %0, %0, %4\n v_add_u32 %1, %1, %4"
            : "=&v"(H.a[k]), "=&v"(H.t[k]), "=&v"(sh) : "v"(v[k]), "v"(bt[k]));
        H.A[k] = c.A[k];
        asm("v_lshlrev_b32 %0, %1, %2" : "=v"(H.cd[k]) : "v"(sh), "v"(c.cq[k]));
    }
#pragma unroll
    for (int k = 0; k < 4; k++) asm volatile("" : "+v"(H.a[k]), "+v"(H.t[k]), "+v"(H.cd[k]) : "s"(H.m[k]));
}
static __device__ __forceinline__ void put(const Held& H, int k) {
    asm volatile("s_not_b64 exec, %[m]\n ds_write_b32 %[a], %[A]\n ds_or_b32 %[t], %[c]\n s_mov_b64 exec, -1\n"
                 :: [m] "s"(H.m[k]), [a] "v"(H.a[k]), [A] "v"(H.A[k]), [t] "v"(H.t[k]), [c] "v"(H.cd[k]) : "memory", "scc");
}
static __device__ __forceinline__ void take_unit(v2u& x, v2u& y, unsigned e, unsigned long long act, unsigned zero) {
    asm volatile("s_mov_b64 exec, %3\n ds_read2_b32 %0, %2 offset1:1\n ds_read2_b32 %1, %2 offset0:2 offset1:3\n ds_write_b32 %2, %4 offset:12\n s_mov_b64 exec, -1"
                 : "=&v"(x), "=&v"(y) : "v"(e), "s"(act), "v"(zero) : "memory");
}

template <int V> static __device__ __forceinline__ void bin_body(unsigned* L, const int* __restrict__ idx_in, const int* __restrict__ idx_out,
                                                    const unsigned short* __restrict__ angle, const unsigned short* __restrict__ ang_freq,
                                                    const unsigned short* __restrict__ m, const unsigned short* __restrict__ w_down,
                                                    unsigned* __restrict__ mdp, int E, int T,
                                                    char* __restrict__ rec0, unsigned* __restrict__ tab, unsigned* __restrict__ flags,
                                                    int NR, int per, Lay ly, unsigned* __restrict__ wgmode, const unsigned short* __restrict__ dist) {
    const int tid = threadIdx.x;
    const unsigned run = blockIdx.x;
    char* rec = rec_align(rec0);
    unsigned tc0 = touch_code(tid);
    unsigned fbits;
    asm volatile("buffer_load_ushort %0, off, %1, 0" : "=v"(fbits) : "s"(mk_rsrc(ang_freq, 2)));
    float freq;
    const v4i_rs ro = mk_rsrc(idx_out, (unsigned)T * 4), ri = mk_rsrc(idx_in, (unsigned)T * 4), ra = mk_rsrc(angle, (unsigned)T * 2);
    const __amdgpu_buffer_rsrc_t rr = __builtin_amdgcn_make_buffer_rsrc((void*)rec, (short)0,
        (int)(ly.dumu * 16),
        0x00020000);
    const unsigned vo16 = ((tid >> P_LP) << P_LS) + (tid & ((1 << P_LP) - 1)) * 16, vo8 = vo16 >> 1;
    auto chunk = [&](unsigned c) {
        const unsigned nb = P_LS - 4 - P_LP, lo = 8 - P_LP;
        unsigned j = c & ((1 << nb) - 1);
        if (P_JM == 1) j = ((j & 7) << lo) | ((j >> 3) & ((1 << lo) - 1)) | (j >> (3 + lo) << (3 + lo));
        unsigned g = c >> nb;
        if (P_SPL == 1) g = (g >> 1) + (g & 1) * (NR * 4);
        if (P_SPL == 2) g = (g >> 2) + (g & 3) * (NR * 2);
        if (P_SPL == 3) g = (g >> 3) + (g & 7) * NR;
        return (g << (nb + 6)) + (j << (P_LP - 4));
    };
#define NSH 4
    const unsigned shard = (run >> 3) & 3, member = (run & 7) | ((run >> 5) << 3);
    const bool leader = member == 0;
    const v4i_rs rw = mk_rsrc(wgmode + WM_CTR, (256 + 256) * 4);
    const int wave0 = __builtin_amdgcn_readfirstlane(tid >> 6) == 0;
    const unsigned ctr_o = shard * 128, box_o = (256 + shard * (256 / NSH) + member) * 4;
    if (wave0) asm volatile("buffer_store_dword %0, %1, %2, 0 offen sc1" :: "v"(0u), "v"(tid == 0 ? (leader ? ctr_o : box_o) : OOB), "s"(rw) : "memory");
    unsigned mail = NR < NSTAT ? 24u : 0u;
    if (leader && NR >= NSTAT) {
        const v4i_rs rp = mk_rsrc(rec, ly.dumu * 16);
        const unsigned pbase = __builtin_amdgcn_readfirstlane(logbase(tid >> 6, run, P_XO) + (((ly.F0 + (tid >> 6) * 24) * LMUL) & LMASK)) * 16;
        unsigned x = 0, xcc;
        asm volatile("buffer_atomic_add %0, %1, %2, %3 offen sc0" : "+v"(x) : "v"((tid & 63) < 2 * CALN ? (tid & 1) * 8192 + ((tid & 63) >> 1) * PSTR : OOB), "s"(rp), "s"(pbase));
        asm volatile("s_getreg_b32 %0, hwreg(HW_REG_XCC_ID)" : "=s"(xcc));
        unsigned mn[2] = {~0u, ~0u};
        unsigned long long ta, tb;
        asm volatile("s_waitcnt vmcnt(0)\n s_memtime %1" : "+v"(x), "=s"(ta));
#pragma unroll
        for (int i = 0; i < 2 * CALN; i++) {
            const int sd = (CORD ? i ^ (i >> 1) : i) & 1;
            asm volatile("v_mov_b32 %0, 0\n buffer_atomic_add %0, %5, %3, %4 offen sc0\n s_waitcnt vmcnt(0)\n s_memtime %1\n s_waitcnt lgkmcnt(0)"
                         : "+v"(x), "=s"(tb), "+s"(ta) : "s"(rp), "s"(pbase + sd * 8192 + (i >> 1) * PSTR), "v"((tid & 63) ? OOB : 0u));
            mn[sd] = min(mn[sd], (unsigned)(tb - ta));
            ta = tb;
        }
        *ldsp(WL_OFF + (tid >> 6) * 4) = (mn[1] < mn[0]) ^ ((pbase >> 21) & 1);
        __syncthreads();
        const unsigned votes = (unsigned)__ballot(*ldsp(WL_OFF + (tid & 15) * 4) != 0) & 0xFFFF, nv = __popc(votes);
        mail = 8 | (xcc & 3) | (nv >= 8 ? 4 : 0) | (nv > VTHR && nv < 16 - VTHR ? 16 : 0);
        if (wave0) asm volatile("buffer_store_dword %0, %1, %2, 0 offen sc1" :: "v"(mail), "v"((256 + shard * 64 + tid) * 4), "s"(rw) : "memory");
        __syncthreads();
    }
    v2u mv[4];
    v4u dv;
    const unsigned dof = tid - 640u < 320u ? (run * 320 + tid - 640) * 16 : OOB;
    const __amdgpu_buffer_rsrc_t rdc = __builtin_amdgcn_make_buffer_rsrc((void*)(mdp + 64 * LMAX), (short)0, 64 * LMAX, 0x00020000);
    if (P_DC) asm volatile("buffer_load_dwordx4 %0, %1, %2, 0 offen " NTH : "=v"(dv) : "v"(dof), "s"(mk_rsrc(dist, (unsigned)E * 2)));
    const unsigned gp = run * 80 + (tid >> 5) * 4;
    {
        const v4i_rs rm = mk_rsrc(m, (unsigned)E * 8);
        const unsigned o = tid < 640 ? (((gp & ~255u) | ((gp >> 2) & 63)) * 32 + (tid & 31)) * 8 : OOB;
#pragma unroll
        for (int j = 0; j < 4; j++) asm volatile("buffer_load_dwordx2 %0, %1, %2, %3 offen " NTH : "=v"(mv[j]) : "v"(o), "s"(rm), "s"(j * 64 * 32 * 8));
    }
    Grp tg; bool tv[4];
    const unsigned tbase = (unsigned)NR * (P1_GRID * CH) + run * per;
    {
        const bool in = tid * 4 < per;
#pragma unroll
        for (int k = 0; k < 4; k++) tv[k] = in && tbase + tid * 4 + k < (unsigned)T;
        const unsigned o16 = in ? tid * 16 : OOB, o8 = in ? tid * 8 : OOB;
        asm volatile("buffer_load_dwordx4 %0, %1, %2, %3 offen nt" : "=v"(tg.io) : "v"(o16), "s"(ro), "s"(tbase * 4));
        asm volatile("buffer_load_dwordx4 %0, %1, %2, %3 offen nt" : "=v"(tg.ii) : "v"(o16), "s"(ri), "s"(tbase * 4));
        asm volatile("buffer_load_dwordx2 %0, %1, %2, %3 offen nt" : "=v"(tg.an) : "v"(o8), "s"(ra), "s"(tbase * 2));
    }
    const unsigned NFULL = (unsigned)NR * P1_GRID;
    Grp g0, cur[2];
    load_grp(g0, ro, ri, ra, NR > 0 ? chunk(run) : 0x3FFFFFu, vo16, vo8);
    load_grp(cur[0], ro, ri, ra, NR > 1 ? chunk(run + P1_GRID) : 0x3FFFFFu, vo16, vo8);
    Own own;
    own.la = LB + tid * LS; own.nl = 0; own.p[0] = own.p[1] = own.p[2] = own.p[3] = 0;
    unsigned wc = ly.F0;
    unsigned base = OOB;
    auto sided = [&]() {
        const unsigned lb0 = logbase(tid >> 6, run, P_XO), sw = (((run + mail) >> 2) ^ (mail & 16 ? tid >> 6 : lb0 >> 17)) & 1;
        base = ((lb0 | (sw << 9)) + (tid & 7)) * 16;
    };
    auto await = [&]() {
        for (int i = 0; i < 4096 && !mail; i++) {
            unsigned x = 0;
            asm volatile("buffer_atomic_add %0, %1, %2, 0 offen sc0\n s_waitcnt vmcnt(0)" : "+v"(x) : "v"((tid & 63) ? OOB : box_o), "s"(rw) : "memory");
            mail = __builtin_amdgcn_readfirstlane(x);
        }
        if (mail) sided(); else { mail = 1; own.ovf |= 16384u; }
    };
    if (mail) sided();
    own.ovf = (unsigned)(size_t)(lds_u32*)L << 14;
    *ldsp(tid * 4) = 64;
#pragma unroll
    for (int i = 0; i < 8; i++) *ldsp(LB + tid * LS + i * 16 + 12) = 0;
    { const v2u z = {0, 0}; *(lds_v2u*)(size_t)(WL_OFF + tid * 8) = z; }
    if (tid < 32) *ldsp(MS_OFF + tid * 4) = 0;
    if (P_GAP) { if (run == 0) wgmode[tid] = 0; }
    else if (run == 0 && tid < 64) wgmode[(tid >> 1) * 32 + (tid & 1)] = 0;
    if (run == 1 && tid < 64) {
        v4u v = {pad_rec(tid), pad_rec(tid + 64), pad_rec(tid + 128), CZERO};
        *(v4u*)(rec + ((size_t)ly.dumu + tid) * 16) = v;
    }
    Conv cv[2];
    unsigned seen = 0;
    auto convert = [&](Grp& gp, Conv& o) {
#pragma unroll
        for (int k = 0; k < 4; k++) {
            unsigned io = k == 0 ? gp.io.x : k == 1 ? gp.io.y : k == 2 ? gp.io.z : gp.io.w;
            unsigned ii = k == 0 ? gp.ii.x : k == 1 ? gp.ii.y : k == 2 ? gp.ii.z : gp.ii.w;
            unsigned aw = (k & 2) ? gp.an.y : gp.an.x;
            float ang = (k & 1) ? hi_bf(aw) : lo_bf(aw);
            conv_rec(io, ii, ang, freq, o.A[k], o.t4[k], o.cq[k], seen);
        }
#pragma unroll
        for (int k = 0; k < 4; k++) asm volatile("" : "+v"(o.A[k]), "+v"(o.t4[k]), "+v"(o.cq[k]));
    };
    const unsigned tinc = TINC, lb7 = LB;
    Held H;
    {
        asm volatile("s_waitcnt vmcnt(9)" : "+v"(mv[0]), "+v"(mv[1]), "+v"(mv[2]), "+v"(mv[3]));
        if (P_DC) asm volatile("" : "+v"(dv));
        if (P_DC == 1 && tid >= 640) __builtin_amdgcn_raw_buffer_store_b128(dv, rdc, dof, 0, 0);
        if (tid < 640) {
            unsigned mx[4] = {0, 0, 0, 0};
            float sq[4] = {0.f, 0.f, 0.f, 0.f};
            prep_md(mv, (tid & 31) * LMAX + gp, w_down, mdp, 32 * LMAX, mx, sq);
#pragma unroll
            for (int d = 0; d < 4; d++) {
                const unsigned v = wave_scan_max(mx[d]);
                const float s = wave_scan_addf(sq[d]);
                if ((tid & 63) == 63) { *ldsp(MS_OFF + 128 + (tid >> 6) * 32 + d * 4) = v; *ldsp(MS_OFF + 128 + (tid >> 6) * 32 + 16 + d * 4) = __float_as_uint(s); }
            }
        }
        asm volatile("s_waitcnt vmcnt(3)" : "+v"(tg.io), "+v"(tg.ii), "+v"(tg.an), "+v"(fbits),
                     "+v"(g0.io), "+v"(g0.ii), "+v"(g0.an));
        asm volatile("" :: "v"(tc0));
        freq = lo_bf(fbits) * 0.15915494309189535f;
        if (T & 1) {
            const unsigned rel = (unsigned)T - 1 - (tbase + tid * 4);
            if (tv[0] && rel < 4) { const unsigned av = angle[T - 1]; if (rel == 0) tg.an.x = av; else tg.an.y = av; }
        }
        __syncthreads();
        unsigned long long vm[4];
#pragma unroll
        for (int k = 0; k < 4; k++) vm[k] = __ballot(tv[k]);
        if (vm[0]) {
            unsigned v[4];
            convert(tg, cv[1]);
            tickets_masked(v, cv[1], tinc, vm);
            place(H, v, cv[1], vm, lb7);
            if ((H.m[0] & vm[0]) | (H.m[1] & vm[1]) | (H.m[2] & vm[2]) | (H.m[3] & vm[3])) own.ovf |= 16384u;
        }
        convert(g0, cv[0]);
#pragma unroll
        for (int k = 0; k < 4; k++) { H.m[k] = 0; H.a[k] = 0; H.t[k] = 0; H.A[k] = 0; H.cd[k] = 0; }
        asm volatile("s_waitcnt vmcnt(0)" ::: "memory");
        if (P_EA && !P_EP) load_grp(cur[1], ro, ri, ra, NR > 2 ? chunk(run + 2 * P1_GRID) : 0x3FFFFFu, vo16, vo8, P_EA);
    }
    Held Q[2];
    if (P_NEW) {
#pragma unroll
        for (int k = 0; k < 8; k++) { Held& q = Q[k >> 2]; q.m[k & 3] = 0; q.a[k & 3] = 0; q.t[k & 3] = 0; q.A[k & 3] = 0; q.cd[k & 3] = 0; }
        if (NR > 0) { unsigned v[4]; tickets(v, cv[0], tinc); resolve(Q[0], v, cv[0], lb7); __syncthreads(); }
    }
    const unsigned long long vm[4] = {~0ull, ~0ull, ~0ull, ~0ull};
    unsigned ca = run + P1_GRID, cb = run + 2 * P1_GRID;
    Pend pd; pd.a0 = pd.a1 = OOB; pd.v0 = pd.v1 = v4u{0, 0, 0, 0};
    unsigned got = 0;
    int st = leader ? 1 : 0;
    if (NR == 0) { asm volatile("s_waitcnt lgkmcnt(0)" ::: "memory"); __syncthreads(); }
    const unsigned wl = WL_OFF + (tid >> 6) * 512, g8 = (tid >> 3) & 7, k16 = (tid & 7) * 16, zero = 0;
    for (int rd = 0; P_NEW && NR > 0; rd += 2) {
        bool done = false;
#pragma unroll
        for (int r = 0; r < 2; r++) {
            unsigned cnt, nfull; v2u e01; unsigned e2;
            auto rdcnt = [&]() { asm volatile("ds_read_b32 %0, %1" : "=v"(cnt) : "v"(tid * 4) : "memory"); };
            auto list = [&]() {
                asm volatile("s_waitcnt lgkmcnt(0)" : "+v"(cnt));
                const bool full = cnt >= 16384u;
                const unsigned long long bal = __ballot(full);
                nfull = __popcll(bal);
                if (full) {
                    const unsigned pos = __builtin_amdgcn_mbcnt_hi((unsigned)(bal >> 32), __builtin_amdgcn_mbcnt_lo((unsigned)bal, 0));
                    *ldsp(wl + pos * 4) = own.la;
                    cnt -= 24 * TINC;
                    *ldsp(tid * 4) = cnt;
                    own.ovf |= cnt;
                    own.nl++;
                    own.p[3] = __builtin_amdgcn_alignbit(own.p[3], own.p[2], 22); own.p[2] = __builtin_amdgcn_alignbit(own.p[2], own.p[1], 22); own.p[1] = __builtin_amdgcn_alignbit(own.p[1], own.p[0], 22);
                    own.p[0] = (own.p[0] << 10) + pos + wc;
                }
                asm volatile("ds_read2_b32 %0, %2 offset1:8\n ds_read_b32 %1, %2 offset:64" : "=&v"(e01), "=&v"(e2) : "v"(wl + g8 * 4) : "memory");
            };
            auto conv = [&](int k) {
                Conv& o = cv[0];
                const Grp& gp = cur[r];
                unsigned io = k == 0 ? gp.io.x : k == 1 ? gp.io.y : k == 2 ? gp.io.z : gp.io.w;
                unsigned ii = k == 0 ? gp.ii.x : k == 1 ? gp.ii.y : k == 2 ? gp.ii.z : gp.ii.w;
                unsigned aw = (k & 2) ? gp.an.y : gp.an.x;
                float ang = (k & 1) ? hi_bf(aw) : lo_bf(aw);
                asm volatile("" : "+v"(ang));
                conv_rec2(io, ii, ang, freq, o.A[k], o.t4[k], o.cq[k], seen);
                asm volatile("" : "+v"(o.A[k]), "+v"(o.t4[k]), "+v"(o.cq[k]), "+v"(seen));
            };
            auto vmw = [&]() { asm volatile("s_waitcnt vmcnt(%4)" : "+v"(cur[r].io), "+v"(cur[r].ii), "+v"(cur[r].an), "+v"(got) : "n"(2 + N_EA)); };
            if (P_X == 0) { held_out(Q[r ^ 1]); put(Q[r], 0); rdcnt(); vmw(); conv(0); list(); put(Q[r], 1); conv(1); put(Q[r], 2); conv(2); put(Q[r], 3); conv(3); }
            if (P_X == 1) { rdcnt(); list(); held_out(Q[r ^ 1]); put(Q[r], 0); vmw(); conv(0); put(Q[r], 1); conv(1); put(Q[r], 2); conv(2); put(Q[r], 3); conv(3); }
            if (P_X == 2) { held_out(Q[r ^ 1]); put(Q[r], 0); put(Q[r], 1); put(Q[r], 2); put(Q[r], 3); rdcnt(); vmw(); conv(0); conv(1); list(); conv(2); conv(3); }
            if (P_X == 3) { held_out(Q[r ^ 1]); rdcnt(); vmw(); conv(0); list(); put(Q[r], 0); conv(1); put(Q[r], 1); conv(2); put(Q[r], 2); conv(3); put(Q[r], 3); }
            if (wave0) {
                const unsigned g = __builtin_amdgcn_readfirstlane(got);
                unsigned nx = cb + P1_GRID;
                if (rd + r + 3 >= NSTAT) nx = st == 2 ? NSTAT * P1_GRID + g * NSH + shard : NFULL;
                if (st == 0 && g != 0) { st = 1; if (!mail) { mail = g; sided(); } }
                if (st == 0 && rd + r + 4 == NSTAT) {
                    for (int i = 0; i < 128 && st == 0; i++) {
                        unsigned x = 0;
                        asm volatile("buffer_atomic_add %0, %1, %2, 0 offen sc0\n s_waitcnt vmcnt(0)" : "+v"(x) : "v"(tid == 0 ? box_o : OOB), "s"(rw) : "memory");
                        if (__builtin_amdgcn_readfirstlane(x) != 0) st = 1;
                    }
                }
                if (st == 1 && rd + r + 4 >= NSTAT) st = 2;
                nx = cb < NFULL ? min(nx, NFULL) : NFULL;
                if ((tid & 63) == 0) { const v2u w2 = {nx, mail}; *(lds_v2u*)(size_t)(MS_OFF + 104) = w2; }
                got = st == 2 ? 1u : 0u;
                if (st != 1 && nx < NFULL)
                    asm volatile("buffer_atomic_add %0, %1, %2, 0 offen sc0" : "+v"(got) : "v"(tid == 0 ? (st == 2 ? ctr_o : box_o) : OOB), "s"(rw) : "memory");
                if (leader) asm volatile("buffer_store_dword %0, %1, %2, 0 offen sc1" :: "v"(mail), "v"(tid < 256 / NSH ? (256 + shard * (256 / NSH) + tid) * 4 : OOB), "s"(rw) : "memory");
            }
            load_grp(cur[r ^ 1], ro, ri, ra, cb < NFULL ? chunk(cb) : 0x3FFFFFu, vo16, vo8, 7 ^ P_EA);
            if (!mail) {
                mail = __builtin_amdgcn_readfirstlane(*ldsp(MS_OFF + 108));
                if (mail) sided(); else if (nfull) await();
            }
            const unsigned long long act0 = __ballot(g8 < nfull), act1 = __ballot(g8 + 8 < nfull);
            const unsigned vo = (__umul24(g8 + wc, LMUL * 16) & (LMASK * 16u)) | base;
            unsigned a0 = g8 < nfull ? vo : OOB, a1 = g8 + 8 < nfull ? vo : OOB;
            asm volatile("s_waitcnt lgkmcnt(0)" : "+v"(e01), "+v"(e2), "+v"(a0), "+v"(a1) :: "memory");
            e01.x += k16; e01.y += k16;
            asm volatile("" : "+v"(e01));
            __syncthreads();
            v2u x0, y0, x1, y1; unsigned nx, v[4];
            take_unit(x0, y0, e01.x, act0, zero);
            take_unit(x1, y1, e01.y, act1, zero);
            asm volatile("ds_read_b32 %0, %1" : "=v"(nx) : "v"(MS_OFF + 104) : "memory");
            if (nfull > 16) {
                for (unsigned p = 16; p < nfull; p += 8) {
                    if (p > 16) e2 = *ldsp(wl + (p + g8) * 4);
                    v2u x, y;
                    take_unit(x, y, e2 + k16, __ballot(g8 + p < nfull), zero);
                    asm volatile("s_waitcnt lgkmcnt(0)" : "+v"(x), "+v"(y));
                    const v4u u = {x.x, x.y, y.x, y.y};
                    __builtin_amdgcn_raw_buffer_store_b128(u, rr, g8 + p < nfull ? vo : OOB, p << (15 + 4), ST_AUX);
                }
            }
            auto lines_out = [&]() {
                const v4u u0 = {x0.x, x0.y, y0.x, y0.y}, u1 = {x1.x, x1.y, y1.x, y1.y};
                __builtin_amdgcn_raw_buffer_store_b128(u0, rr, a0, 0, ST_AUX);
                __builtin_amdgcn_raw_buffer_store_b128(u1, rr, a1, 8 << (15 + 4), ST_AUX);
                asm volatile("" ::: "memory");
            };
            wc = min(wc + nfull, 960u);
            if (ca < NFULL) tickets(v, cv[0], tinc);
            else asm volatile("s_waitcnt lgkmcnt(0)" : "=v"(v[0]), "=v"(v[1]), "=v"(v[2]), "=v"(v[3]));
            asm volatile("s_waitcnt lgkmcnt(4)" : "+v"(x0), "+v"(y0), "+v"(x1), "+v"(y1), "+v"(nx));
            lines_out();
            if (ca >= NFULL) {
                __syncthreads();
                held_out(Q[r]);
                done = true; break;
            }
            const unsigned cn = __builtin_amdgcn_readfirstlane(nx);
            if (P_EA) load_grp(cur[r], ro, ri, ra, cn < NFULL ? chunk(cn) : 0x3FFFFFu, vo16, vo8, P_EA);
            resolve(Q[r ^ 1], v, cv[0], lb7);
            __syncthreads();
            ca = cb;
            cb = ca < NFULL ? cn : NFULL;
        }
        if (done) break;
    }
    for (int rd = 0; !P_NEW && NR > 0; rd += 2) {
        bool done = false;
#pragma unroll
        for (int r = 0; r < 2; r++) {
            unsigned v[4];
            if (P_EP == 2) load_grp(cur[r ^ 1], ro, ri, ra, cb < NFULL ? chunk(cb) : 0x3FFFFFu, vo16, vo8, P_EA);
            tickets(v, cv[r & 1], tinc);
            held_out(H);
            if (P_EP == 1) load_grp(cur[r ^ 1], ro, ri, ra, cb < NFULL ? chunk(cb) : 0x3FFFFFu, vo16, vo8, P_EA);
            if (P_DF == 1) pend_out(pd, rr);
            asm volatile("s_waitcnt vmcnt(%4)" : "+v"(cur[r].io), "+v"(cur[r].ii), "+v"(cur[r].an), "+v"(got) : "n"(2 + N_EA));
            convert(cur[r], cv[(r + 1) & 1]);
            if (wave0) {
                const unsigned g = __builtin_amdgcn_readfirstlane(got);
                unsigned nx = cb + P1_GRID;
                if (rd + r + 3 >= NSTAT) nx = st == 2 ? NSTAT * P1_GRID + g * NSH + shard : NFULL;
                if (st == 0 && g != 0) { st = 1; if (!mail) { mail = g; sided(); } }
                if (st == 0 && rd + r + 4 == NSTAT) {
                    for (int i = 0; i < 128 && st == 0; i++) {
                        unsigned x = 0;
                        asm volatile("buffer_atomic_add %0, %1, %2, 0 offen sc0\n s_waitcnt vmcnt(0)" : "+v"(x) : "v"(tid == 0 ? box_o : OOB), "s"(rw) : "memory");
                        if (__builtin_amdgcn_readfirstlane(x) != 0) st = 1;
                    }
                }
                if (st == 1 && rd + r + 4 >= NSTAT) st = 2;
                nx = cb < NFULL ? min(nx, NFULL) : NFULL;
                if ((tid & 63) == 0) { const v2u w2 = {nx, mail}; *(lds_v2u*)(size_t)(MS_OFF + 104) = w2; }
                got = st == 2 ? 1u : 0u;
                if (st != 1 && nx < NFULL)
                    asm volatile("buffer_atomic_add %0, %1, %2, 0 offen sc0" : "+v"(got) : "v"(tid == 0 ? (st == 2 ? ctr_o : box_o) : OOB), "s"(rw) : "memory");
                if (leader) asm volatile("buffer_store_dword %0, %1, %2, 0 offen sc1" :: "v"(mail), "v"(tid < 256 / NSH ? (256 + shard * (256 / NSH) + tid) * 4 : OOB), "s"(rw) : "memory");
            }
            load_grp(cur[r ^ 1], ro, ri, ra, cb < NFULL ? chunk(cb) : 0x3FFFFFu, vo16, vo8, 7 ^ P_EA);
            if (P_DF == 2) { asm volatile("" ::: "memory"); pend_out(pd, rr); asm volatile("" ::: "memory"); }
            place(H, v, cv[r & 1], vm, lb7);
            if (P_DF == 3) pend_out(pd, rr);
            asm volatile("s_waitcnt lgkmcnt(0)" ::: "memory");
            __syncthreads();
            const unsigned cnt = *ldsp(tid * 4);
            const unsigned nx = *ldsp(MS_OFF + 104);
            if (!mail) {
                mail = __builtin_amdgcn_readfirstlane(*ldsp(MS_OFF + 108));
                if (mail) sided(); else if (__ballot(cnt >= 16384u)) await();
            }
            flush(own, pd, wc, cnt, tid, base, rr, P_DF == 0);
            if (P_EA && !P_EP) {
                const unsigned cn = __builtin_amdgcn_readfirstlane(nx);
                load_grp(cur[r], ro, ri, ra, cn < NFULL ? chunk(cn) : 0x3FFFFFu, vo16, vo8, P_EA);
            }
            __syncthreads();
            if (ca >= NFULL) { done = true; break; }
            ca = cb;
            cb = ca < NFULL ? __builtin_amdgcn_readfirstlane(nx) : NFULL;
        }
        if (done) break;
    }
    asm volatile("" : "+v"(got));
    if (P_DF) pend_out(pd, rr);
    unsigned tc1 = touch_code(tid);
    if (P_DC == 2 && tid >= 640) __builtin_amdgcn_raw_buffer_store_b128(dv, rdc, dof, 0, 16);
    if (!mail) await();
    if (!P_NEW) held_out(H);
    asm volatile("s_waitcnt lgkmcnt(0)" ::: "memory");
    {
        v2u fl;
        if (tid < 64) {
            const unsigned sa = MS_OFF + 128 + (tid & 15) * 32 + (tid >> 4) * 4;
            unsigned mx = (tid & 15) < 10 ? *ldsp(sa) : 0u;
            float sq = (tid & 15) < 10 ? __uint_as_float(*ldsp(sa + 16)) : 0.f;
#pragma unroll
            for (int i = 0; i < 4; i++) {
                mx = max(mx, (unsigned)(i == 0 ? __builtin_amdgcn_update_dpp(0, (int)mx, 0x111, 0xF, 0xF, false) : i == 1 ? __builtin_amdgcn_update_dpp(0, (int)mx, 0x112, 0xF, 0xF, false)
                                        : i == 2 ? __builtin_amdgcn_update_dpp(0, (int)mx, 0x114, 0xF, 0xF, false) : __builtin_amdgcn_update_dpp(0, (int)mx, 0x118, 0xF, 0xF, false)));
                const int si = __float_as_int(sq);
                sq += __int_as_float(i == 0 ? __builtin_amdgcn_update_dpp(0, si, 0x111, 0xF, 0xF, false) : i == 1 ? __builtin_amdgcn_update_dpp(0, si, 0x112, 0xF, 0xF, false)
                                     : i == 2 ? __builtin_amdgcn_update_dpp(0, si, 0x114, 0xF, 0xF, false) : __builtin_amdgcn_update_dpp(0, si, 0x118, 0xF, 0xF, false));
            }
            mx = min(mx ? (mx >> 16) + 2 : 0u, 0x7F80u);
            fl.x = (mx << 16) | (unsigned)__builtin_amdgcn_update_dpp(0, (int)mx, 0x142, 0xA, 0xF, false);
            fl.y = (__float_as_uint(sq) & 0xFFFF0000u) | ((unsigned)__builtin_amdgcn_update_dpp(0, __float_as_int(sq), 0x142, 0xA, 0xF, false) >> 16);
        }
        if (P_GAP && tid < 20) *ldsp(MS_OFF + 128 + tid * 4) = 0;
        if (own.ovf >= 16384u || own.nl > NPOS || wc > ly.cap || (seen & 0x30000000u)) *ldsp(MS_OFF + 96) = 1;
        const unsigned tt = ((tid & 31) << 5) | (tid >> 5);
        const unsigned d = *ldsp(tt * 4) - 64;
        unsigned P = (d * 3) >> 11;
        if (P >= 24) { *ldsp(MS_OFF + 96) = 1; P = 0; }
        const unsigned nu = (P + 2) / 3;
        const unsigned inc = wave_scan_add(nu);
        if ((tid & 63) == 63) *ldsp(MS_OFF + (tid >> 6) * 4) = inc;
        __syncthreads();
        unsigned wb = (tid & 63) < (tid >> 6) ? *ldsp(MS_OFF + (tid & 15) * 4) : 0;
        wb = __builtin_amdgcn_readlane(row_scan_add(wb), 15);
        unsigned fo = wb + inc - nu;
        if (fo + nu > ly.fcap) *ldsp(MS_OFF + 96) = 1;
        const unsigned fbase = pairidx(run) * (ly.F0 * 256);
        auto piece = [&](unsigned n) { return n * 1024 + (((((run + mail) >> 2) ^ (mail & 16 ? n : (fbase + n * 1024) >> 17)) & 1) << 9); };
        {
            const unsigned n = (fo * 33223) >> 24;
            fo -= n * 505;
            if (P_GAP && (fo + nu >= 505 || tid == P1_THREADS - 1)) *ldsp(MS_OFF + 128 + n * 4) = fo + nu;
            fo = (fo + piece(n)) & 0xFFFF;
        }
        *ldsp(WL_OFF + tt * 4) = (nu << 8) | (fo << 16);
        *ldsp(tid * 4) = (fo << 8) | P;
        __syncthreads();
        {
            const v4u te = {own.p[0], own.p[1], own.p[2], (own.p[3] & 15) | (min(own.nl, 15u) << 4) | ((base >> 1) & 4096) | *ldsp(WL_OFF + tid * 4)};
            __builtin_amdgcn_raw_buffer_store_b128(te, __builtin_amdgcn_make_buffer_rsrc((void*)(tab + run * NT * 4), (short)0, NT * 16, 0x00020000), tid * 16, 0, 16);
        }
        if (tid < 64) {
            if (*ldsp(MS_OFF + 96)) fl.x = 0x7F807F80u;
            if ((tid & 31) == 31) __hip_atomic_store((unsigned long long*)(flags + ((tid >> 5) * P1_GRID + run) * 2), ((unsigned long long)fl.y << 32) | fl.x, __ATOMIC_RELAXED, __HIP_MEMORY_SCOPE_AGENT);
        }
        const unsigned k = tid & 7;
        const unsigned pa = (run & 15) * 24 + k * 3;
        const __amdgpu_buffer_rsrc_t rf = __builtin_amdgcn_make_buffer_rsrc((void*)(rec + (size_t)fbase * 16), (short)0, (int)(ly.F0 * 4096), 0x00020000);
        if (P_GAP && tid >= 768) {
            const unsigned n = (tid - 768) >> 3, x = *ldsp(MS_OFF + 128 + min(n, 19u) * 4) + k;
            const v4u v = {0, 0, 0, 0};
            const bool on = n < 20 && x > k && (x - k >= 505 ? k < 7 : ((x ^ (x - k - 1)) & 8) == 0);
            __builtin_amdgcn_raw_buffer_store_b128(v, rf, on ? (x < 512 ? piece(n) + x : piece(n + 1) + x - 512) * 16 : OOB, 0, 16);
        }
#pragma unroll 4
        for (int it = 0; it < 8; it++) {
            const unsigned u = (tid & ~63) + it * 8 + ((tid >> 3) & 7), t = ((u & 31) << 5) | (u >> 5);
            const unsigned w = *ldsp(u * 4), Pt = w & 255;
            v4u v = read_unit(LB + t * LS + k * 16);
            if (k * 3 + 1 >= Pt) { v.y = pad_rec(pa + 1); v.w |= 512u << 11; }
            if (k * 3 + 2 >= Pt) { v.z = pad_rec(pa + 2); v.w |= 512u << 22; }
            __builtin_amdgcn_raw_buffer_store_b128(v, rf, k * 3 < Pt ? ((w >> 8) + k) * 16 : OOB, 0, 16);
        }
        asm volatile("s_waitcnt vmcnt(8)" :: "v"(tc1));
    }
    WAIT_GRP(0, cur[0]); WAIT_GRP(0, cur[1]);
}

#define BIN_ARGS const int* __restrict__ idx_in, const int* __restrict__ idx_out, const unsigned short* __restrict__ angle, const unsigned short* __restrict__ ang_freq, \
    const unsigned short* __restrict__ m, const unsigned short* __restrict__ w_down, unsigned* __restrict__ mdp, int E, int T, \
    char* __restrict__ rec0, unsigned* __restrict__ tab, unsigned* __restrict__ flags, int NR, int per, Lay ly, unsigned* __restrict__ wgmode, const unsigned short* __restrict__ dist
#define BIN_PASS idx_in, idx_out, angle, ang_freq, m, w_down, mdp, E, T, rec0, tab, flags, NR, per, ly, wgmode, dist
extern "C" __global__ __launch_bounds__(P1_THREADS) void k_bin(BIN_ARGS) {
    __shared__ __attribute__((aligned(16))) unsigned L[BIN_LDS / 4];
    bin_body<VDEF>(L, BIN_PASS);
}

struct AccArgs {
    const char* rec; const unsigned* tab; const unsigned* flags; const unsigned* mdp; float* part; unsigned* praw; unsigned* wgmode;
    const int* idx_in; const int* idx_out; const unsigned short* angle; const unsigned short* ang_freq; const unsigned short* m; const unsigned short* w_down;
    const unsigned short* dist; const unsigned short* centers; const unsigned short* w_rbf; const unsigned short* w_sbf; const unsigned short* w_up;
    unsigned short* out;
    int EP; unsigned O; int T; float c4; int E; int unused;
    Lay ly;
};
typedef char AccArgs_has_no_padding[sizeof(AccArgs) == 19 * 8 + 10 * 4 ? 1 : -1];

typedef const __attribute__((address_space(4))) unsigned* cptr;
static __device__ __forceinline__ void edge_gh(float d, const unsigned short* centers, const unsigned short* w_sbf, const unsigned short* w_rbf, float* g, float* h) {
#pragma unroll
    for (int k = 0; k < 4; k++) g[k] = h[k] = 0.f;
#pragma unroll
    for (int r = 0; r < 8; r++) {
        const unsigned cu = ((cptr)centers)[r >> 1];
        const float df = d - ((r & 1) ? hi_bf(cu) : lo_bf(cu));
        const float rb = __builtin_amdgcn_exp2f(df * df * -14.426950408889634f);
#pragma unroll
        for (int k = 0; k < 2; k++) {
            const unsigned u = ((cptr)w_sbf)[r * 2 + k], v = ((cptr)w_rbf)[r * 2 + k];
            g[2 * k] += rb * lo_bf(u); g[2 * k + 1] += rb * hi_bf(u); h[2 * k] += rb * lo_bf(v); h[2 * k + 1] += rb * hi_bf(v);
        }
    }
}
static __device__ __forceinline__ v2u edge_out(const float* agg, const float* g, const float* h, unsigned m0, unsigned m1, const unsigned* wu) {
    float a[4];
#pragma unroll
    for (int k = 0; k < 4; k++) a[k] = agg[k] * g[k];
    const float mm[4] = {lo_bf(m0), hi_bf(m0), lo_bf(m1), hi_bf(m1)};
    float o[4];
#pragma unroll
    for (int dd = 0; dd < 4; dd++) {
        float s = 0.f;
#pragma unroll
        for (int k = 0; k < 4; k++) { const unsigned u = wu[k * 2 + (dd >> 1)]; s += a[k] * ((dd & 1) ? hi_bf(u) : lo_bf(u)); }
        o[dd] = mm[dd] + s * h[dd];
    }
    v2u r;
    asm("v_cvt_pk_bf16_f32 %0, %1, %2" : "=v"(r.x) : "v"(o[0]), "v"(o[1]));
    asm("v_cvt_pk_bf16_f32 %0, %1, %2" : "=v"(r.y) : "v"(o[2]), "v"(o[3]));
    return r;
}

typedef const __attribute__((address_space(4))) AccArgs* kaptr;
static __device__ __forceinline__ void acc_fallback(unsigned* lds, unsigned mode, const __attribute__((address_space(4))) AccArgs& a_, int tid, int b, int qt, int cp) {
    const char* rec = rec_align(a_.rec); const unsigned* flags = a_.flags; const unsigned* mdp = a_.mdp;
    const int EP = a_.EP, T = a_.T;
    const Lay ly = {a_.ly.F0, a_.ly.cap, a_.ly.dumu, a_.ly.fcap};
    const int lane = tid & 63;
    const unsigned O = a_.O;
    float* pout0 = a_.part + (size_t)(qt * 4 + cp * 2) * EP + (size_t)b * O;
    const unsigned G = tid >> 3, k = tid & 7;
    {
        unsigned* praw = a_.praw + (size_t)(qt * 2 + cp) * EP + (size_t)b * O;
        for (unsigned i = tid; i < O; i += P2_THREADS) praw[i] = 0;
    }
    if (mode == 1) {
        unsigned* acc = lds + OBASE;
        const float Xm = 2139095040.0f / (float)(NWS * (NPOS * 24 + 24)) - 2.0f;
#pragma unroll 1
        for (int c = 0; c < 2; c++) {
            __syncthreads();
            if (tid == 0) lds[0] = 0;
            __syncthreads();
            if (tid < NWS) { unsigned v = wave_scan_max((flags[(cp * P1_GRID + tid) * 2] >> (16 * c)) & 0xFFFF); if (lane == 63) atomicMax(&lds[0], v); }
            __syncthreads();
            unsigned mbits = __builtin_amdgcn_readfirstlane(lds[0]);
            if (mbits == 0) mbits = 0x3f80;
            const float M = __uint_as_float(mbits << 16);
            const float S = Xm / (511.0f * M), inv = M / Xm;
            float racc[20];
#pragma unroll
            for (int r = 0; r < 20; r++) racc[r] = 0.f;
#pragma unroll 1
            for (int s = 0; s < 8; s++) {
                __syncthreads();
                const unsigned* src = mdp + (size_t)cp * (32 * LMAX) + (size_t)(qt * 8 + s) * LMAX;
                for (unsigned i = tid; i < LMAX; i += P2_THREADS) {
                    unsigned w = src[i];
                    lds[(i & ~255u) | ((i & 3) << 6) | ((i >> 2) & 63)] = __float_as_uint((c ? __uint_as_float(w) : lo_bf(w)) * S);
                }
                for (int i = tid; i < OBASE; i += P2_THREADS) acc[i] = 0;
                __syncthreads();
                const unsigned t = b * 32 + qt * 8 + s;
                for (unsigned r = G; r < NWS; r += 128) {
                    v4u te = ((const v4u*)a_.tab)[r * NT + t];
                    const unsigned tw = te.w, nl = (tw >> 4) & 15, nu = (tw >> 8) & 15;
                    for (unsigned l = 0; l <= nl; l++) {
                        unsigned u;
                        if (l < nl) {
                            u = (((te.x & 1023) * LMUL) & LMASK) + logbase(t >> 6, r, P_XO) + ((tw >> 3) & 512) + k;
                            te.x = __builtin_amdgcn_alignbit(te.y, te.x, 10); te.y = __builtin_amdgcn_alignbit(te.z, te.y, 10); te.z = __builtin_amdgcn_alignbit(te.w, te.z, 10);
                        }
                        else { if (k >= nu) break; u = pairidx(r) * (ly.F0 * 256) + (tw >> 16) + k; }
                        v4u q = *(const v4u*)(rec + (size_t)u * 16);
                        unsigned a[3] = {q.x, q.y, q.z};
#pragma unroll
                        for (int j = 0; j < 3; j++) {
                            const int cq = (int)((q.w >> (j == 0 ? 1 : j == 1 ? 11 : 22)) & 1023) - 512;
                            const int x = __float2int_rn(__uint_as_float(lds[(a[j] >> 2) & 0x7FFF]) * (float)cq);
                            if ((a[j] >> 17) - IDXB < OBASE) atomicAdd(&acc[(a[j] >> 17) - IDXB], (unsigned)x);
                        }
                    }
                }
                __syncthreads();
#pragma unroll
                for (int r = 0; r < 20; r++) racc[r] += (float)(int)acc[r * P2_THREADS + tid] * inv;
            }
#pragma unroll
            for (int r = 0; r < 20; r++) if ((unsigned)(r * P2_THREADS + tid) < O) pout0[(size_t)c * EP + r * P2_THREADS + tid] = racc[r];
        }
    }
    if (mode == 2) {
        float* facc = (float*)lds;
        const float freq = bf2f(a_.ang_freq[0]);
        float wd[2][4];
#pragma unroll
        for (int kk = 0; kk < 4; kk++) { wd[0][kk] = bf2f(a_.w_down[kk * 4 + cp * 2]); wd[1][kk] = bf2f(a_.w_down[kk * 4 + cp * 2 + 1]); }
        __syncthreads();
        for (int i = tid; i < 2 * OBASE; i += P2_THREADS) facc[i] = 0.f;
        __syncthreads();
        for (int t = tid; t < T; t += P2_THREADS) {
            const unsigned io = a_.idx_out[t], ii = a_.idx_in[t];
            if (((io >> 7) & 31) == (unsigned)b && ((ii >> 3) & 3) == (unsigned)qt) {
                const v2u mv = ((const v2u*)a_.m)[ii];
                const float a[4] = {lo_bf(mv.x), hi_bf(mv.x), lo_bf(mv.y), hi_bf(mv.y)};
                const float cs = __cosf(freq * bf2f(a_.angle[t]));
                const unsigned ol = (io & 127u) | ((io >> 5) & ~127u);
#pragma unroll
                for (int c = 0; c < 2; c++)
                    atomicAdd(&facc[c * OBASE + ol], (a[0] * wd[c][0] + a[1] * wd[c][1] + a[2] * wd[c][2] + a[3] * wd[c][3]) * cs);
            }
        }
        __syncthreads();
        for (unsigned idx = tid; idx < O; idx += P2_THREADS) { pout0[idx] = facc[idx]; pout0[(size_t)EP + idx] = facc[OBASE + idx]; }
    }
}

#define SWAPX 2
#define RAUX ""
#define J0 (wave >> 1)
#define IDXMASK "0x8fffffff"
#define STR2(x) #x
#define STR(x) STR2(x)
#define PLAN_IT 160

extern "C" __global__ __launch_bounds__(P2_THREADS) void k_acc(AccArgs a_) {
    constexpr int AV = 136;
    const Lay ly = a_.ly;
    __shared__ __attribute__((aligned(16))) unsigned lds[2 * OBASE];
    const int tid = threadIdx.x;
    const int wave = __builtin_amdgcn_readfirstlane(tid >> 6), lane = tid & 63;
    const int xcd = blockIdx.x & 7, jj = blockIdx.x >> 3;
    const int cp = jj & 1;
    const int qt = (AV & 3) == 0 ? xcd >> 1 : (AV & 3) == 1 ? (jj >> 1) & 3 : (xcd & 1) * 2 + ((jj >> 1) & 1);
    const int b = (AV & 3) == 0 ? (xcd & 1) * 16 + (jj >> 1) : (AV & 3) == 1 ? xcd * 4 + (jj >> 3) : (xcd >> 1) * 8 + (jj >> 2);
    const unsigned O = a_.O;
    unsigned stv = 0;
    unsigned tc2 = AV & 256 ? 0 : touch_code2(AV & 128 ? (tid < 64 ? tid * 8 + (jj & 7) : 512) : tid);
    unsigned* praw = a_.praw + (size_t)(qt * 2 + cp) * a_.EP + (size_t)b * O;
    const unsigned* mds = a_.mdp + (size_t)cp * (32 * LMAX) + (size_t)(qt * 8) * LMAX;
    unsigned mode = (unsigned)(size_t)lds ? 2u : 0u;
    unsigned exs = 0, bad = 0;
    v4u mm[2], dd;
    const int q = qt * 2 + cp;
    unsigned t16 = tid * 16;
    if (mode == 0) {
        const unsigned w = wave * 16 + ((lane & 15) ^ (cp ? SWAPX : 0)), t0 = b * 32 + qt * 8 + (lane >> 4) * 2;
        v4i_rs rs = mk_rsrc(rec_align(a_.rec), ly.dumu + 64);
        rs.y |= 16 << 16;
        const v4i_rs rm = mk_rsrc(mds, 8 * LMAX * 4);
        const v4i_rs rp0 = mk_rsrc(praw, O * 4);
        const v4i_rs rt = mk_rsrc(a_.tab, (NT * NWS * 4 + FL_SIZE) * 4);
        const v4i_rs rmm = mk_rsrc(AV & 16 ? (const void*)a_.mdp : (const void*)a_.m, a_.E * 8), rd = AV & 32 ? mk_rsrc(a_.mdp + 64 * LMAX, 64 * LMAX) : mk_rsrc(a_.dist, a_.E * 2);
        asm volatile(
            "s_setprio 3\n"
            "buffer_load_dwordx4 v[32:35], %[tof], %[rt], 0 offen\n"
            "buffer_load_dwordx4 v[36:39], %[tof], %[rt], 0 offen offset:16\n"
            "v_and_b32 v10, 1023, %[tid16]\n"
            "s_load_dword s80, %[w0], 0\n s_load_dword s81, %[w1], 0\n s_load_dword s93, %[w2], 0\n s_load_dword s97, %[w3], 0\n"
            "s_movk_i32 s74, 16384\n s_mov_b32 s75, 32768\n s_mov_b32 s79, 49152\n s_mov_b32 s92, 65536\n"
            "v_bfrev_b32 v12, 1\n"
            "v_add_u32 v11, %[pb], v10\n"
            "v_bfrev_b32 v13, 1\n"
            "v_bfrev_b32 v14, 1\n"
            "v_bfrev_b32 v15, 1\n"
            "ds_write_b128 v11, v[12:15] offset:0\n"
            "ds_write_b128 v11, v[12:15] offset:1024\n"
            "ds_write_b128 v11, v[12:15] offset:2048\n"
            "ds_write_b128 v11, v[12:15] offset:3072\n"
            "ds_write_b128 v11, v[12:15] offset:4096\n"
            "v_bfrev_b32 v80, 1\n"
            "v_mov_b32 v84, 0xb9000000\n"
            "v_mov_b32 v88, 0\n"
            "v_add_u32 v89, 81920, %[tid16]\n"
            "v_add_u32 v90, 65536, v89\n"
            "v_lshrrev_b32 v16, 4, v10\n"
            "v_add_u32 v83, %[dumu], v16\n"
            "v_and_b32 v17, 7, v16\n"
            "v_lshl_or_b32 v82, v17, 28, v17\n"
            "v_lshrrev_b32 v18, 3, v16\n"
            "v_lshl_add_u32 v17, v17, 3, v18\n"
            "v_lshlrev_b32 v17, 2, v17\n"
            "v_add_u32 v17, %[pb], v17\n"
            "s_getpc_b64 s[70:71]\n"
            "Lpc%=:\n"
            "s_add_u32 s72, s70, Lchg2%=-Lpc%=\n s_addc_u32 s73, s71, 0\n"
            "s_add_u32 s66, s70, Lchg3%=-Lpc%=\n s_addc_u32 s67, s71, 0\n"
            "s_add_u32 s70, s70, Lchg1%=-Lpc%=\n s_addc_u32 s71, s71, 0\n"
            "s_waitcnt vmcnt(0)\n"
            "buffer_load_dwordx4 v[92:95], v10, %[rt], %[fof] offen\n"
            "buffer_load_dwordx4 v[96:99], v10, %[rt], %[fof] offen offset:1024\n"
            "s_mov_b32 s94, 0\n"
            "buffer_load_dwordx4 v[40:43], %[tid16], %[rm], s94 offen\n"
            "s_add_u32 s96, s94, 16384\n"
            "buffer_load_dwordx4 v[44:47], %[tid16], %[rm], s96 offen\n"
            "s_add_u32 s96, s94, 32768\n"
            "buffer_load_dwordx4 v[48:51], %[tid16], %[rm], s96 offen\n"
            "s_add_u32 s96, s94, 49152\n"
            "buffer_load_dwordx4 v[52:55], %[tid16], %[rm], s96 offen\n"
            "s_add_u32 s96, s94, 65536\n"
            "buffer_load_dwordx4 v[56:59], %[tid16], %[rm], s96 offen\n"
            "v_bfe_u32 v20, v35, 4, 4\n v_bfe_u32 v21, v39, 4, 4\n v_bfe_u32 v22, v35, 8, 4\n v_bfe_u32 v23, v39, 8, 4\n"
            "v_min_u32 v24, 1, v22\n v_min_u32 v25, 1, v23\n v_add_u32 v24, v24, v20\n v_add_u32 v25, v25, v21\n"
            "v_lshl_or_b32 v24, v25, 16, v24\n"
            "v_lshrrev_b32 v8, 16, v35\n v_lshrrev_b32 v9, 16, v39\n"
            "v_add_u32_dpp v25, v24, v24 row_shr:1 row_mask:0xf bank_mask:0xf bound_ctrl:1\n"
            "v_add_u32 v8, %[fb], v8\n v_add_u32 v9, %[fb], v9\n"
            "v_add_u32_dpp v25, v25, v25 row_shr:2 row_mask:0xf bank_mask:0xf bound_ctrl:1\n"
            "v_sub_u32 v26, 8, v22\n v_sub_u32 v27, 8, v23\n"
            "v_add_u32_dpp v25, v25, v25 row_shr:4 row_mask:0xf bank_mask:0xf bound_ctrl:1\n"
            "v_lshl_or_b32 v8, v26, 28, v8\n v_lshl_or_b32 v9, v27, 28, v9\n"
            "v_add_u32_dpp v25, v25, v25 row_shr:8 row_mask:0xf bank_mask:0xf bound_ctrl:1\n"
            "s_nop 1\n"
            "v_readlane_b32 s84, v25, 15\n"
            "v_readlane_b32 s85, v25, 31\n"
            "v_readlane_b32 s86, v25, 47\n"
            "v_readlane_b32 s87, v25, 63\n"
            "v_sub_u32 v25, v25, v24\n"
            "s_mov_b32 s96, 0\n s_mov_b64 s[76:77], 0\n"
            "s_add_u32 s84, s84, 0x70007\n"
            "s_bfe_u32 s88, s84, 0xd0003\n s_lshr_b32 s89, s84, 19\n"
            "s_max_u32 s88, s88, 8\n s_max_u32 s89, s89, 8\n"
            "s_add_u32 s90, s96, s88\n"
            "s_pack_ll_b32_b16 s91, s96, s90\n"
            "s_add_u32 s96, s90, s89\n"
            "s_lshl_b32 s89, s89, 8\n s_or_b32 s88, s88, s89\n s_and_b32 s88, s88, 0xffff\n"
            "s_or_b32 s76, s76, s88\n"
            "s_lshl_b32 s91, s91, 3\n"
            "v_mov_b32 v26, s91\n"
            "s_add_u32 s85, s85, 0x70007\n"
            "s_bfe_u32 s88, s85, 0xd0003\n s_lshr_b32 s89, s85, 19\n"
            "s_max_u32 s88, s88, 8\n s_max_u32 s89, s89, 8\n"
            "s_add_u32 s90, s96, s88\n"
            "s_pack_ll_b32_b16 s91, s96, s90\n"
            "s_add_u32 s96, s90, s89\n"
            "s_lshl_b32 s89, s89, 8\n s_or_b32 s88, s88, s89\n s_and_b32 s88, s88, 0xffff\n"
            "s_lshl_b32 s88, s88, 16\n"
            "s_or_b32 s76, s76, s88\n"
            "s_lshl_b32 s91, s91, 3\n"
            "s_mov_b32 exec_lo, 0xffff0000\n s_mov_b32 exec_hi, 0xffffffff\n"
            "v_mov_b32 v26, s91\n"
            "s_add_u32 s86, s86, 0x70007\n"
            "s_bfe_u32 s88, s86, 0xd0003\n s_lshr_b32 s89, s86, 19\n"
            "s_max_u32 s88, s88, 8\n s_max_u32 s89, s89, 8\n"
            "s_add_u32 s90, s96, s88\n"
            "s_pack_ll_b32_b16 s91, s96, s90\n"
            "s_add_u32 s96, s90, s89\n"
            "s_lshl_b32 s89, s89, 8\n s_or_b32 s88, s88, s89\n s_and_b32 s88, s88, 0xffff\n"
            "s_or_b32 s77, s77, s88\n"
            "s_lshl_b32 s91, s91, 3\n"
            "s_mov_b32 exec_lo, 0x0\n s_mov_b32 exec_hi, 0xffffffff\n"
            "v_mov_b32 v26, s91\n"
            "s_add_u32 s87, s87, 0x70007\n"
            "s_bfe_u32 s88, s87, 0xd0003\n s_lshr_b32 s89, s87, 19\n"
            "s_max_u32 s88, s88, 8\n s_max_u32 s89, s89, 8\n"
            "s_add_u32 s90, s96, s88\n"
            "s_pack_ll_b32_b16 s91, s96, s90\n"
            "s_add_u32 s96, s90, s89\n"
            "s_lshl_b32 s89, s89, 8\n s_or_b32 s88, s88, s89\n s_and_b32 s88, s88, 0xffff\n"
            "s_lshl_b32 s88, s88, 16\n"
            "s_or_b32 s77, s77, s88\n"
            "s_lshl_b32 s91, s91, 3\n"
            "s_mov_b32 exec_lo, 0x0\n s_mov_b32 exec_hi, 0xffff0000\n"
            "v_mov_b32 v26, s91\n"
            "s_mov_b64 exec, -1\n"
            "s_mov_b32 %[bad], 0\n"
            "s_cmp_gt_u32 s96, " STR(PLAN_IT) "\n"
            "s_cbranch_scc0 1f\n"
            "s_mov_b32 %[bad], 1\n s_mov_b32 s76, 0x08080808\n s_mov_b32 s77, 0x08080808\n"
            "s_branch Lnoplan%=\n"
            "1:\n"
            "v_add_u32 v25, v25, v26\n"
            "v_and_b32 v26, 0xffff, v25\n v_lshrrev_b32 v27, 16, v25\n"
            "v_lshlrev_b32 v26, 2, v26\n v_lshlrev_b32 v27, 2, v27\n"
            "v_add_u32 v26, %[pb], v26\n v_add_u32 v27, %[pb], v27\n"
            "v_lshl_add_u32 v28, v20, 2, v26\n v_lshl_add_u32 v29, v21, 2, v27\n"
            "v_cmp_ne_u32 vcc, 0, v22\n v_cmp_ne_u32 s[88:89], 0, v23\n"
            "v_lshrrev_b32 v30, 3, v35\n v_and_b32 v30, 0x200, v30\n v_or_b32 v30, %[xb], v30\n"
            "s_mov_b64 exec, vcc\n ds_write_b32 v28, v8\n s_mov_b64 exec, s[88:89]\n ds_write_b32 v29, v9\n s_mov_b64 exec, -1\n"
            "v_min_u32 v24, v20, v21\n v_mov_b32 v31, %[lmask]\n s_movk_i32 s91, 1023\n"
            "v_cmp_gt_u32 vcc, 4, v24\n"
            "s_cmp_eq_u64 vcc, 0\n s_cbranch_scc1 4f\n"
            "v_cmp_lt_u32 vcc, 0, v20\n v_cmp_lt_u32 s[88:89], 0, v21\n"
            "v_bfe_u32 v28, v32, 0, 10\n"
            "v_bfe_u32 v29, v36, 0, 10\n"
            "s_or_b64 s[84:85], vcc, s[88:89]\n"
            "s_cbranch_scc0 Lnoplan%=\n"
            "v_mul_u32_u24 v28, %[lmul], v28\n"
            "v_mul_u32_u24 v29, %[lmul], v29\n"
            "v_and_or_b32 v28, v28, v31, v30\n"
            "v_and_or_b32 v29, v29, v31, v30\n"
            "s_mov_b64 exec, vcc\n ds_write_b32 v26, v28 offset:0\n s_mov_b64 exec, s[88:89]\n ds_write_b32 v27, v29 offset:0\n s_mov_b64 exec, -1\n"
            "v_cmp_lt_u32 vcc, 1, v20\n v_cmp_lt_u32 s[88:89], 1, v21\n"
            "v_bfe_u32 v28, v32, 10, 10\n"
            "v_bfe_u32 v29, v36, 10, 10\n"
            "s_or_b64 s[84:85], vcc, s[88:89]\n"
            "s_cbranch_scc0 Lnoplan%=\n"
            "v_mul_u32_u24 v28, %[lmul], v28\n"
            "v_mul_u32_u24 v29, %[lmul], v29\n"
            "v_and_or_b32 v28, v28, v31, v30\n"
            "v_and_or_b32 v29, v29, v31, v30\n"
            "s_mov_b64 exec, vcc\n ds_write_b32 v26, v28 offset:4\n s_mov_b64 exec, s[88:89]\n ds_write_b32 v27, v29 offset:4\n s_mov_b64 exec, -1\n"
            "v_cmp_lt_u32 vcc, 2, v20\n v_cmp_lt_u32 s[88:89], 2, v21\n"
            "v_bfe_u32 v28, v32, 20, 10\n"
            "v_bfe_u32 v29, v36, 20, 10\n"
            "s_or_b64 s[84:85], vcc, s[88:89]\n"
            "s_cbranch_scc0 Lnoplan%=\n"
            "v_mul_u32_u24 v28, %[lmul], v28\n"
            "v_mul_u32_u24 v29, %[lmul], v29\n"
            "v_and_or_b32 v28, v28, v31, v30\n"
            "v_and_or_b32 v29, v29, v31, v30\n"
            "s_mov_b64 exec, vcc\n ds_write_b32 v26, v28 offset:8\n s_mov_b64 exec, s[88:89]\n ds_write_b32 v27, v29 offset:8\n s_mov_b64 exec, -1\n"
            "v_cmp_lt_u32 vcc, 3, v20\n v_cmp_lt_u32 s[88:89], 3, v21\n"
            "v_alignbit_b32 v28, v33, v32, 30\n"
            "v_and_b32 v28, s91, v28\n"
            "v_alignbit_b32 v29, v37, v36, 30\n"
            "v_and_b32 v29, s91, v29\n"
            "s_or_b64 s[84:85], vcc, s[88:89]\n"
            "s_cbranch_scc0 Lnoplan%=\n"
            "v_mul_u32_u24 v28, %[lmul], v28\n"
            "v_mul_u32_u24 v29, %[lmul], v29\n"
            "v_and_or_b32 v28, v28, v31, v30\n"
            "v_and_or_b32 v29, v29, v31, v30\n"
            "s_mov_b64 exec, vcc\n ds_write_b32 v26, v28 offset:12\n s_mov_b64 exec, s[88:89]\n ds_write_b32 v27, v29 offset:12\n s_mov_b64 exec, -1\n"
            "s_branch 5f\n"
            "4:\n"
            "v_bfe_u32 v12, v32, 0, 10\n"
            "v_bfe_u32 v13, v32, 10, 10\n"
            "v_bfe_u32 v14, v36, 0, 10\n"
            "v_bfe_u32 v15, v36, 10, 10\n"
            "v_mul_u32_u24 v12, %[lmul], v12\n"
            "v_mul_u32_u24 v13, %[lmul], v13\n"
            "v_mul_u32_u24 v14, %[lmul], v14\n"
            "v_mul_u32_u24 v15, %[lmul], v15\n"
            "v_and_or_b32 v12, v12, v31, v30\n"
            "v_and_or_b32 v13, v13, v31, v30\n"
            "v_and_or_b32 v14, v14, v31, v30\n"
            "v_and_or_b32 v15, v15, v31, v30\n"
            "ds_write2_b32 v26, v12, v13 offset0:0 offset1:1\n ds_write2_b32 v27, v14, v15 offset0:0 offset1:1\n"
            "v_bfe_u32 v12, v32, 20, 10\n"
            "v_alignbit_b32 v13, v33, v32, 30\n"
            "v_and_b32 v13, s91, v13\n"
            "v_bfe_u32 v14, v36, 20, 10\n"
            "v_alignbit_b32 v15, v37, v36, 30\n"
            "v_and_b32 v15, s91, v15\n"
            "v_mul_u32_u24 v12, %[lmul], v12\n"
            "v_mul_u32_u24 v13, %[lmul], v13\n"
            "v_mul_u32_u24 v14, %[lmul], v14\n"
            "v_mul_u32_u24 v15, %[lmul], v15\n"
            "v_and_or_b32 v12, v12, v31, v30\n"
            "v_and_or_b32 v13, v13, v31, v30\n"
            "v_and_or_b32 v14, v14, v31, v30\n"
            "v_and_or_b32 v15, v15, v31, v30\n"
            "ds_write2_b32 v26, v12, v13 offset0:2 offset1:3\n ds_write2_b32 v27, v14, v15 offset0:2 offset1:3\n"
            "5:\n"
            "v_cmp_lt_u32 vcc, 4, v20\n v_cmp_lt_u32 s[88:89], 4, v21\n"
            "v_bfe_u32 v28, v33, 8, 10\n"
            "v_bfe_u32 v29, v37, 8, 10\n"
            "s_or_b64 s[84:85], vcc, s[88:89]\n"
            "s_cbranch_scc0 Lnoplan%=\n"
            "v_mul_u32_u24 v28, %[lmul], v28\n"
            "v_mul_u32_u24 v29, %[lmul], v29\n"
            "v_and_or_b32 v28, v28, v31, v30\n"
            "v_and_or_b32 v29, v29, v31, v30\n"
            "s_mov_b64 exec, vcc\n ds_write_b32 v26, v28 offset:16\n s_mov_b64 exec, s[88:89]\n ds_write_b32 v27, v29 offset:16\n s_mov_b64 exec, -1\n"
            "v_cmp_lt_u32 vcc, 5, v20\n v_cmp_lt_u32 s[88:89], 5, v21\n"
            "v_bfe_u32 v28, v33, 18, 10\n"
            "v_bfe_u32 v29, v37, 18, 10\n"
            "s_or_b64 s[84:85], vcc, s[88:89]\n"
            "s_cbranch_scc0 Lnoplan%=\n"
            "v_mul_u32_u24 v28, %[lmul], v28\n"
            "v_mul_u32_u24 v29, %[lmul], v29\n"
            "v_and_or_b32 v28, v28, v31, v30\n"
            "v_and_or_b32 v29, v29, v31, v30\n"
            "s_mov_b64 exec, vcc\n ds_write_b32 v26, v28 offset:20\n s_mov_b64 exec, s[88:89]\n ds_write_b32 v27, v29 offset:20\n s_mov_b64 exec, -1\n"
            "v_cmp_lt_u32 vcc, 6, v20\n v_cmp_lt_u32 s[88:89], 6, v21\n"
            "v_alignbit_b32 v28, v34, v33, 28\n"
            "v_and_b32 v28, s91, v28\n"
            "v_alignbit_b32 v29, v38, v37, 28\n"
            "v_and_b32 v29, s91, v29\n"
            "s_or_b64 s[84:85], vcc, s[88:89]\n"
            "s_cbranch_scc0 Lnoplan%=\n"
            "v_mul_u32_u24 v28, %[lmul], v28\n"
            "v_mul_u32_u24 v29, %[lmul], v29\n"
            "v_and_or_b32 v28, v28, v31, v30\n"
            "v_and_or_b32 v29, v29, v31, v30\n"
            "s_mov_b64 exec, vcc\n ds_write_b32 v26, v28 offset:24\n s_mov_b64 exec, s[88:89]\n ds_write_b32 v27, v29 offset:24\n s_mov_b64 exec, -1\n"
            "v_cmp_lt_u32 vcc, 7, v20\n v_cmp_lt_u32 s[88:89], 7, v21\n"
            "v_bfe_u32 v28, v34, 6, 10\n"
            "v_bfe_u32 v29, v38, 6, 10\n"
            "s_or_b64 s[84:85], vcc, s[88:89]\n"
            "s_cbranch_scc0 Lnoplan%=\n"
            "v_mul_u32_u24 v28, %[lmul], v28\n"
            "v_mul_u32_u24 v29, %[lmul], v29\n"
            "v_and_or_b32 v28, v28, v31, v30\n"
            "v_and_or_b32 v29, v29, v31, v30\n"
            "s_mov_b64 exec, vcc\n ds_write_b32 v26, v28 offset:28\n s_mov_b64 exec, s[88:89]\n ds_write_b32 v27, v29 offset:28\n s_mov_b64 exec, -1\n"
            "v_cmp_lt_u32 vcc, 8, v20\n v_cmp_lt_u32 s[88:89], 8, v21\n"
            "v_bfe_u32 v28, v34, 16, 10\n"
            "v_bfe_u32 v29, v38, 16, 10\n"
            "s_or_b64 s[84:85], vcc, s[88:89]\n"
            "s_cbranch_scc0 Lnoplan%=\n"
            "v_mul_u32_u24 v28, %[lmul], v28\n"
            "v_mul_u32_u24 v29, %[lmul], v29\n"
            "v_and_or_b32 v28, v28, v31, v30\n"
            "v_and_or_b32 v29, v29, v31, v30\n"
            "s_mov_b64 exec, vcc\n ds_write_b32 v26, v28 offset:32\n s_mov_b64 exec, s[88:89]\n ds_write_b32 v27, v29 offset:32\n s_mov_b64 exec, -1\n"
            "v_cmp_lt_u32 vcc, 9, v20\n v_cmp_lt_u32 s[88:89], 9, v21\n"
            "v_alignbit_b32 v28, v35, v34, 26\n"
            "v_and_b32 v28, s91, v28\n"
            "v_alignbit_b32 v29, v39, v38, 26\n"
            "v_and_b32 v29, s91, v29\n"
            "s_or_b64 s[84:85], vcc, s[88:89]\n"
            "s_cbranch_scc0 Lnoplan%=\n"
            "v_mul_u32_u24 v28, %[lmul], v28\n"
            "v_mul_u32_u24 v29, %[lmul], v29\n"
            "v_and_or_b32 v28, v28, v31, v30\n"
            "v_and_or_b32 v29, v29, v31, v30\n"
            "s_mov_b64 exec, vcc\n ds_write_b32 v26, v28 offset:36\n s_mov_b64 exec, s[88:89]\n ds_write_b32 v27, v29 offset:36\n s_mov_b64 exec, -1\n"
            "Lnoplan%=:\n"
            "ds_read_b32 v60, v17 offset:0\n"
            "ds_read_b32 v61, v17 offset:256\n"
            "ds_read_b32 v62, v17 offset:512\n"
            "ds_read_b32 v63, v17 offset:768\n"
            "ds_read_b32 v64, v17 offset:1024\n"
            "ds_read_b32 v65, v17 offset:1280\n"
            "ds_read_b32 v66, v17 offset:1536\n"
            "ds_read_b32 v67, v17 offset:1792\n"
            "ds_read_b32 v68, v17 offset:2048\n"
            "ds_read_b32 v69, v17 offset:2304\n"
            "ds_read_b32 v70, v17 offset:2560\n"
            "ds_read_b32 v71, v17 offset:2816\n"
            "ds_read_b32 v72, v17 offset:3072\n"
            "ds_read_b32 v73, v17 offset:3328\n"
            "ds_read_b32 v74, v17 offset:3584\n"
            "ds_read_b32 v75, v17 offset:3840\n"
            "ds_read_b32 v76, v17 offset:4096\n"
            "ds_read_b32 v77, v17 offset:4352\n"
            "ds_read_b32 v78, v17 offset:4608\n"
            "ds_read_b32 v79, v17 offset:4864\n"
            "v_mov_b32 v100, 0\n"
            "v_mov_b32 v101, 0\n"
            "v_mov_b32 v102, 0\n"
            "v_mov_b32 v103, 0\n"
            "v_mov_b32 v107, v11\n"
            "s_mov_b32 s82, 0\n"
            "s_waitcnt lgkmcnt(15)\n"
            "v_mov_b32 v81, v60\n"
            "s_nop 1\n"
            "v_add_u32_dpp v111, v81, v82 row_newbcast:0 row_mask:0xf bank_mask:0x3\n"
            "v_add_u32_dpp v111, v81, v82 row_newbcast:8 row_mask:0xf bank_mask:0xc\n"
            "v_and_b32 v111, " IDXMASK ", v111\n"
            "v_min_u32 v111, v111, v83\n"
            "buffer_load_dwordx4 v[8:11], v111, %[rs], 0 idxen " RAUX "\n"
            "v_add_u32_dpp v111, v81, v82 row_newbcast:1 row_mask:0xf bank_mask:0x3\n"
            "v_add_u32_dpp v111, v81, v82 row_newbcast:9 row_mask:0xf bank_mask:0xc\n"
            "v_and_b32 v111, " IDXMASK ", v111\n"
            "v_min_u32 v111, v111, v83\n"
            "buffer_load_dwordx4 v[12:15], v111, %[rs], 0 idxen " RAUX "\n"
            "v_add_u32_dpp v111, v81, v82 row_newbcast:2 row_mask:0xf bank_mask:0x3\n"
            "v_add_u32_dpp v111, v81, v82 row_newbcast:10 row_mask:0xf bank_mask:0xc\n"
            "v_and_b32 v111, " IDXMASK ", v111\n"
            "v_min_u32 v111, v111, v83\n"
            "buffer_load_dwordx4 v[16:19], v111, %[rs], 0 idxen " RAUX "\n"
            "s_setprio 0\n"
            "ds_write_b128 v107, v[100:103] offset:0\n"
            "ds_write_b128 v107, v[100:103] offset:1024\n"
            "ds_write_b128 v107, v[100:103] offset:2048\n"
            "ds_write_b128 v107, v[100:103] offset:3072\n"
            "ds_write_b128 v107, v[100:103] offset:4096\n"
            "s_waitcnt vmcnt(8)\n"
            "v_pk_max_u16 v92, v92, v94\n v_pk_max_u16 v96, v96, v98\n"
            "v_lshlrev_b32 v104, 16, v93\n v_lshlrev_b32 v105, 16, v95\n v_lshlrev_b32 v106, 16, v97\n v_lshlrev_b32 v108, 16, v99\n"
            "v_pk_max_u16 v92, v92, v96\n"
            "v_add_f32 v104, v104, v105\n v_add_f32 v106, v106, v108\n v_add_f32 v93, v93, v95\n v_add_f32 v97, v97, v99\n"
            "v_and_b32 v94, 0xffff, v92\n v_lshrrev_b32 v92, 16, v92\n"
            "v_add_f32 v104, v104, v106\n v_add_f32 v93, v93, v97\n"
            "s_nop 0\n"
            "v_max_u32_dpp v94, v94, v94 row_shr:1 row_mask:0xf bank_mask:0xf bound_ctrl:1\n"
            "v_max_u32_dpp v92, v92, v92 row_shr:1 row_mask:0xf bank_mask:0xf bound_ctrl:1\n"
            "v_add_f32_dpp v104, v104, v104 row_shr:1 row_mask:0xf bank_mask:0xf bound_ctrl:1\n"
            "v_add_f32_dpp v93, v93, v93 row_shr:1 row_mask:0xf bank_mask:0xf bound_ctrl:1\n"
            "v_max_u32_dpp v94, v94, v94 row_shr:2 row_mask:0xf bank_mask:0xf bound_ctrl:1\n"
            "v_max_u32_dpp v92, v92, v92 row_shr:2 row_mask:0xf bank_mask:0xf bound_ctrl:1\n"
            "v_add_f32_dpp v104, v104, v104 row_shr:2 row_mask:0xf bank_mask:0xf bound_ctrl:1\n"
            "v_add_f32_dpp v93, v93, v93 row_shr:2 row_mask:0xf bank_mask:0xf bound_ctrl:1\n"
            "v_max_u32_dpp v94, v94, v94 row_shr:4 row_mask:0xf bank_mask:0xf bound_ctrl:1\n"
            "v_max_u32_dpp v92, v92, v92 row_shr:4 row_mask:0xf bank_mask:0xf bound_ctrl:1\n"
            "v_add_f32_dpp v104, v104, v104 row_shr:4 row_mask:0xf bank_mask:0xf bound_ctrl:1\n"
            "v_add_f32_dpp v93, v93, v93 row_shr:4 row_mask:0xf bank_mask:0xf bound_ctrl:1\n"
            "v_max_u32_dpp v94, v94, v94 row_shr:8 row_mask:0xf bank_mask:0xf bound_ctrl:1\n"
            "v_max_u32_dpp v92, v92, v92 row_shr:8 row_mask:0xf bank_mask:0xf bound_ctrl:1\n"
            "v_add_f32_dpp v104, v104, v104 row_shr:8 row_mask:0xf bank_mask:0xf bound_ctrl:1\n"
            "v_add_f32_dpp v93, v93, v93 row_shr:8 row_mask:0xf bank_mask:0xf bound_ctrl:1\n"
            "v_max_u32_dpp v94, v94, v94 row_bcast:15 row_mask:0xa bank_mask:0xf\n"
            "v_max_u32_dpp v92, v92, v92 row_bcast:15 row_mask:0xa bank_mask:0xf\n"
            "v_add_f32_dpp v104, v104, v104 row_bcast:15 row_mask:0xa bank_mask:0xf\n"
            "v_add_f32_dpp v93, v93, v93 row_bcast:15 row_mask:0xa bank_mask:0xf\n"
            "v_max_u32_dpp v94, v94, v94 row_bcast:31 row_mask:0xc bank_mask:0xf\n"
            "v_max_u32_dpp v92, v92, v92 row_bcast:31 row_mask:0xc bank_mask:0xf\n"
            "v_add_f32_dpp v104, v104, v104 row_bcast:31 row_mask:0xc bank_mask:0xf\n"
            "v_add_f32_dpp v93, v93, v93 row_bcast:31 row_mask:0xc bank_mask:0xf\n"
            "s_nop 1\n"
            "v_readlane_b32 s84, v94, 63\n v_readlane_b32 s85, v92, 63\n v_readlane_b32 s86, v104, 63\n v_readlane_b32 s87, v93, 63\n"
            "s_mov_b32 %[mode], 0\n"
            "s_max_u32 s96, s84, s85\n s_cmp_ge_u32 s96, 0x7f00\n s_cselect_b32 %[mode], 2, 0\n"
            "s_cmp_eq_u32 s84, 0\n s_cselect_b32 s84, 0x3f80, s84\n s_cselect_b32 s86, 0x7f000000, s86\n"
            "s_cmp_eq_u32 s85, 0\n s_cselect_b32 s85, 0x3f80, s85\n s_cselect_b32 s87, 0x7f000000, s87\n"
            "s_min_u32 s96, s84, s85\n s_cmp_lt_u32 s96, 0x800\n s_cselect_b32 %[mode], 2, %[mode]\n"
            "s_cmp_lg_u32 %[mode], 0\n"
            "s_cbranch_scc1 Lend%=\n"
            "s_lshl_b32 s84, s84, 16\n s_lshl_b32 s85, s85, 16\n"
            "v_mov_b32 v104, s84\n v_mov_b32 v105, s85\n"
            "v_mul_f32 v106, 0x3e82d0e5, v104\n v_mul_f32 v108, 0x3e82d0e5, v105\n"
            "v_mul_f32 v104, v104, v104\n v_mul_f32 v105, v105, v105\n"
            "v_add_u32 v106, 0x7fffff, v106\n v_add_u32 v108, 0x7fffff, v108\n"
            "v_mul_f32 v104, %[c4], v104\n v_mul_f32 v105, %[c4], v105\n"
            "v_and_b32 v106, 0x7f800000, v106\n v_and_b32 v108, 0x7f800000, v108\n"
            "v_cmp_lt_f32 vcc, s86, v104\n v_cmp_lt_f32 s[88:89], s87, v105\n"
            "v_add_u32 v86, 0xc00000, v106\n v_add_u32 v87, 0xc00000, v108\n"
            "v_lshrrev_b32 v106, 23, v106\n v_lshrrev_b32 v108, 15, v108\n"
            "v_or_b32 %[exs], v106, v108\n"
            "s_or_b64 vcc, vcc, s[88:89]\n"
            "s_cselect_b32 %[mode], 1, 0\n"
            "s_cbranch_scc1 Lend%=\n"
            "s_waitcnt vmcnt(3)\n"
            "s_mov_b32 m0, %[wb]\n"
            "s_nop 0\n"
            "ds_write_addtid_b32 v40 offset:0\n"
            "ds_write_addtid_b32 v41 offset:256\n"
            "ds_write_addtid_b32 v42 offset:512\n"
            "ds_write_addtid_b32 v43 offset:768\n"
            "ds_write_addtid_b32 v44 offset:16384\n"
            "ds_write_addtid_b32 v45 offset:16640\n"
            "ds_write_addtid_b32 v46 offset:16896\n"
            "ds_write_addtid_b32 v47 offset:17152\n"
            "ds_write_addtid_b32 v48 offset:32768\n"
            "ds_write_addtid_b32 v49 offset:33024\n"
            "ds_write_addtid_b32 v50 offset:33280\n"
            "ds_write_addtid_b32 v51 offset:33536\n"
            "ds_write_addtid_b32 v52 offset:49152\n"
            "ds_write_addtid_b32 v53 offset:49408\n"
            "ds_write_addtid_b32 v54 offset:49664\n"
            "ds_write_addtid_b32 v55 offset:49920\n"
            "s_add_u32 m0, %[wb], 16384\n"
            "s_nop 0\n"
            "ds_write_addtid_b32 v56 offset:49152\n"
            "ds_write_addtid_b32 v57 offset:49408\n"
            "ds_write_addtid_b32 v58 offset:49664\n"
            "ds_write_addtid_b32 v59 offset:49920\n"
            "s_mov_b32 s92, 0\n"
            "s_and_b32 s75, s76, 255\n"
            "s_movk_i32 s74, 1000\n"
            "s_sub_i32 s79, s75, %[j0]\n"
            "s_max_i32 s79, s79, 8\n"
            "s_cmp_eq_u32 s92, 7\n s_cselect_b32 s79, 8, s79\n"
            "s_setprio 3\n"
            "v_add_u32_dpp v111, v81, v82 row_newbcast:3 row_mask:0xf bank_mask:0x3\n"
            "v_add_u32_dpp v111, v81, v82 row_newbcast:11 row_mask:0xf bank_mask:0xc\n"
            "v_and_b32 v111, " IDXMASK ", v111\n"
            "v_min_u32 v111, v111, v83\n"
            "buffer_load_dwordx4 v[20:23], v111, %[rs], 0 idxen " RAUX "\n"
            "v_add_u32_dpp v111, v81, v82 row_newbcast:4 row_mask:0xf bank_mask:0x3\n"
            "v_add_u32_dpp v111, v81, v82 row_newbcast:12 row_mask:0xf bank_mask:0xc\n"
            "v_and_b32 v111, " IDXMASK ", v111\n"
            "v_min_u32 v111, v111, v83\n"
            "buffer_load_dwordx4 v[24:27], v111, %[rs], 0 idxen " RAUX "\n"
            "v_add_u32_dpp v111, v81, v82 row_newbcast:5 row_mask:0xf bank_mask:0x3\n"
            "v_add_u32_dpp v111, v81, v82 row_newbcast:13 row_mask:0xf bank_mask:0xc\n"
            "v_and_b32 v111, " IDXMASK ", v111\n"
            "v_min_u32 v111, v111, v83\n"
            "buffer_load_dwordx4 v[28:31], v111, %[rs], 0 idxen " RAUX "\n"
            "v_add_u32_dpp v111, v81, v82 row_newbcast:6 row_mask:0xf bank_mask:0x3\n"
            "v_add_u32_dpp v111, v81, v82 row_newbcast:14 row_mask:0xf bank_mask:0xc\n"
            "v_and_b32 v111, " IDXMASK ", v111\n"
            "v_min_u32 v111, v111, v83\n"
            "buffer_load_dwordx4 v[32:35], v111, %[rs], 0 idxen " RAUX "\n"
            "v_add_u32_dpp v111, v81, v82 row_newbcast:7 row_mask:0xf bank_mask:0x3\n"
            "v_add_u32_dpp v111, v81, v82 row_newbcast:15 row_mask:0xf bank_mask:0xc\n"
            "v_and_b32 v111, " IDXMASK ", v111\n"
            "v_min_u32 v111, v111, v83\n"
            "buffer_load_dwordx4 v[36:39], v111, %[rs], 0 idxen " RAUX "\n"
            "s_min_u32 s82, s82, 19\n"
            "s_add_u32 s82, s82, 1\n"
            "s_set_gpr_idx_on s82, gpr_idx(SRC0)\n"
            "s_nop 0\n"
            "v_mov_b32 v81, v60\n"
            "s_set_gpr_idx_off\n"
            "s_waitcnt lgkmcnt(0)\n"
            "s_barrier\n"
            "s_waitcnt vmcnt(7)\n"
            "v_and_b32 v104, 0x1fffc, v8\n"
            "v_and_b32 v105, 0x1fffc, v9\n"
            "v_and_b32 v106, 0x1fffc, v10\n"
            "ds_read_b32 v93, v104\n"
            "ds_read_b32 v95, v105\n"
            "ds_read_b32 v97, v106\n"
            "s_waitcnt lgkmcnt(0)\n"
            "Ls0_%=:\n"
            "s_cmp_gt_i32 s75, s74\n"
            "s_cbranch_scc1 1f\n"
            "s_waitcnt vmcnt(6)\n"
            "1:\n"
            "s_waitcnt vmcnt(11)\n"
            "s_cmp_eq_u32 s75, s79\n"
            "s_cbranch_scc1 Le0_%=\n"
            "Lc0_%=:\n"
            "v_and_b32 v104, 0x1fffc, v12\n"
            "v_and_b32 v105, 0x1fffc, v13\n"
            "v_and_b32 v106, 0x1fffc, v14\n"
            "ds_read_b32 v99, v104\n"
            "ds_read_b32 v101, v105\n"
            "ds_read_b32 v103, v106\n"
            "v_add_u32_dpp v111, v81, v82 row_newbcast:0 row_mask:0xf bank_mask:0x3\n"
            "v_add_u32_dpp v111, v81, v82 row_newbcast:8 row_mask:0xf bank_mask:0xc\n"
            "v_and_b32 v108, 0x7fe, v11\n"
            "v_and_b32 v109, 0x1ff800, v11\n"
            "v_lshrrev_b32 v110, 22, v11\n"
            "v_lshrrev_b32 v112, 15, v8\n"
            "v_lshrrev_b32 v113, 15, v9\n"
            "v_lshrrev_b32 v114, 15, v10\n"
            "v_and_b32 v111, " IDXMASK ", v111\n"
            "v_min_u32 v111, v111, v83\n"
            "buffer_load_dwordx4 v[8:11], v111, %[rs], 0 idxen " RAUX "\n"
            "v_fmamk_f32 v108, v108, 0x7e800000, v84\n"
            "v_fmamk_f32 v109, v109, 0x79800000, v84\n"
            "v_fmamk_f32 v110, v110, 0x7f000000, v84\n"
            "v_or_b32 v112, 3, v112\n"
            "v_or_b32 v113, 3, v113\n"
            "v_or_b32 v114, 3, v114\n"
            "s_sub_u32 s75, s75, 1\n"
            "s_waitcnt lgkmcnt(6)\n"
            "v_lshlrev_b32 v92, 16, v93\n"
            "v_lshlrev_b32 v94, 16, v95\n"
            "v_lshlrev_b32 v96, 16, v97\n"
            "v_pk_fma_f32 v[92:93], v[92:93], v[108:109], v[86:87] op_sel:[0,0,0] op_sel_hi:[1,0,1]\n"
            "v_pk_fma_f32 v[94:95], v[94:95], v[108:109], v[86:87] op_sel:[0,1,0] op_sel_hi:[1,1,1]\n"
            "v_pk_fma_f32 v[96:97], v[96:97], v[110:111], v[86:87] op_sel:[0,0,0] op_sel_hi:[1,0,1]\n"
            "v_lshl_add_u32 v92, v93, 16, v92\n"
            "v_lshl_add_u32 v94, v95, 16, v94\n"
            "v_lshl_add_u32 v96, v97, 16, v96\n"
            "v_sub_u32 v92, v92, v86\n"
            "v_mad_i32_i16 v88, v93, v112, v88\n"
            "ds_add_u32 v112, v92 offset:65533\n"
            "v_sub_u32 v94, v94, v86\n"
            "v_mad_i32_i16 v88, v95, v113, v88\n"
            "ds_add_u32 v113, v94 offset:65533\n"
            "v_sub_u32 v96, v96, v86\n"
            "v_mad_i32_i16 v88, v97, v114, v88\n"
            "ds_add_u32 v114, v96 offset:65533\n"
            "Ls1_%=:\n"
            "s_cmp_gt_i32 s75, s74\n"
            "s_cbranch_scc1 1f\n"
            "s_waitcnt vmcnt(6)\n"
            "1:\n"
            "s_waitcnt vmcnt(11)\n"
            "s_cmp_eq_u32 s75, s79\n"
            "s_cbranch_scc1 Le1_%=\n"
            "Lc1_%=:\n"
            "v_and_b32 v104, 0x1fffc, v16\n"
            "v_and_b32 v105, 0x1fffc, v17\n"
            "v_and_b32 v106, 0x1fffc, v18\n"
            "ds_read_b32 v93, v104\n"
            "ds_read_b32 v95, v105\n"
            "ds_read_b32 v97, v106\n"
            "v_add_u32_dpp v111, v81, v82 row_newbcast:1 row_mask:0xf bank_mask:0x3\n"
            "v_add_u32_dpp v111, v81, v82 row_newbcast:9 row_mask:0xf bank_mask:0xc\n"
            "v_and_b32 v108, 0x7fe, v15\n"
            "v_and_b32 v109, 0x1ff800, v15\n"
            "v_lshrrev_b32 v110, 22, v15\n"
            "v_lshrrev_b32 v112, 15, v12\n"
            "v_lshrrev_b32 v113, 15, v13\n"
            "v_lshrrev_b32 v114, 15, v14\n"
            "v_and_b32 v111, " IDXMASK ", v111\n"
            "v_min_u32 v111, v111, v83\n"
            "buffer_load_dwordx4 v[12:15], v111, %[rs], 0 idxen " RAUX "\n"
            "v_fmamk_f32 v108, v108, 0x7e800000, v84\n"
            "v_fmamk_f32 v109, v109, 0x79800000, v84\n"
            "v_fmamk_f32 v110, v110, 0x7f000000, v84\n"
            "v_or_b32 v112, 3, v112\n"
            "v_or_b32 v113, 3, v113\n"
            "v_or_b32 v114, 3, v114\n"
            "s_sub_u32 s75, s75, 1\n"
            "s_waitcnt lgkmcnt(6)\n"
            "v_lshlrev_b32 v98, 16, v99\n"
            "v_lshlrev_b32 v100, 16, v101\n"
            "v_lshlrev_b32 v102, 16, v103\n"
            "v_pk_fma_f32 v[98:99], v[98:99], v[108:109], v[86:87] op_sel:[0,0,0] op_sel_hi:[1,0,1]\n"
            "v_pk_fma_f32 v[100:101], v[100:101], v[108:109], v[86:87] op_sel:[0,1,0] op_sel_hi:[1,1,1]\n"
            "v_pk_fma_f32 v[102:103], v[102:103], v[110:111], v[86:87] op_sel:[0,0,0] op_sel_hi:[1,0,1]\n"
            "v_lshl_add_u32 v98, v99, 16, v98\n"
            "v_lshl_add_u32 v100, v101, 16, v100\n"
            "v_lshl_add_u32 v102, v103, 16, v102\n"
            "v_sub_u32 v98, v98, v86\n"
            "v_mad_i32_i16 v88, v99, v112, v88\n"
            "ds_add_u32 v112, v98 offset:65533\n"
            "v_sub_u32 v100, v100, v86\n"
            "v_mad_i32_i16 v88, v101, v113, v88\n"
            "ds_add_u32 v113, v100 offset:65533\n"
            "v_sub_u32 v102, v102, v86\n"
            "v_mad_i32_i16 v88, v103, v114, v88\n"
            "ds_add_u32 v114, v102 offset:65533\n"
            "Ls2_%=:\n"
            "s_cmp_gt_i32 s75, s74\n"
            "s_cbranch_scc1 1f\n"
            "s_waitcnt vmcnt(6)\n"
            "1:\n"
            "s_waitcnt vmcnt(11)\n"
            "s_cmp_eq_u32 s75, s79\n"
            "s_cbranch_scc1 Le2_%=\n"
            "Lc2_%=:\n"
            "v_and_b32 v104, 0x1fffc, v20\n"
            "v_and_b32 v105, 0x1fffc, v21\n"
            "v_and_b32 v106, 0x1fffc, v22\n"
            "ds_read_b32 v99, v104\n"
            "ds_read_b32 v101, v105\n"
            "ds_read_b32 v103, v106\n"
            "v_add_u32_dpp v111, v81, v82 row_newbcast:2 row_mask:0xf bank_mask:0x3\n"
            "v_add_u32_dpp v111, v81, v82 row_newbcast:10 row_mask:0xf bank_mask:0xc\n"
            "v_and_b32 v108, 0x7fe, v19\n"
            "v_and_b32 v109, 0x1ff800, v19\n"
            "v_lshrrev_b32 v110, 22, v19\n"
            "v_lshrrev_b32 v112, 15, v16\n"
            "v_lshrrev_b32 v113, 15, v17\n"
            "v_lshrrev_b32 v114, 15, v18\n"
            "v_and_b32 v111, " IDXMASK ", v111\n"
            "v_min_u32 v111, v111, v83\n"
            "buffer_load_dwordx4 v[16:19], v111, %[rs], 0 idxen " RAUX "\n"
            "v_fmamk_f32 v108, v108, 0x7e800000, v84\n"
            "v_fmamk_f32 v109, v109, 0x79800000, v84\n"
            "v_fmamk_f32 v110, v110, 0x7f000000, v84\n"
            "v_or_b32 v112, 3, v112\n"
            "v_or_b32 v113, 3, v113\n"
            "v_or_b32 v114, 3, v114\n"
            "s_sub_u32 s75, s75, 1\n"
            "s_waitcnt lgkmcnt(6)\n"
            "v_lshlrev_b32 v92, 16, v93\n"
            "v_lshlrev_b32 v94, 16, v95\n"
            "v_lshlrev_b32 v96, 16, v97\n"
            "v_pk_fma_f32 v[92:93], v[92:93], v[108:109], v[86:87] op_sel:[0,0,0] op_sel_hi:[1,0,1]\n"
            "v_pk_fma_f32 v[94:95], v[94:95], v[108:109], v[86:87] op_sel:[0,1,0] op_sel_hi:[1,1,1]\n"
            "v_pk_fma_f32 v[96:97], v[96:97], v[110:111], v[86:87] op_sel:[0,0,0] op_sel_hi:[1,0,1]\n"
            "v_lshl_add_u32 v92, v93, 16, v92\n"
            "v_lshl_add_u32 v94, v95, 16, v94\n"
            "v_lshl_add_u32 v96, v97, 16, v96\n"
            "v_sub_u32 v92, v92, v86\n"
            "v_mad_i32_i16 v88, v93, v112, v88\n"
            "ds_add_u32 v112, v92 offset:65533\n"
            "v_sub_u32 v94, v94, v86\n"
            "v_mad_i32_i16 v88, v95, v113, v88\n"
            "ds_add_u32 v113, v94 offset:65533\n"
            "v_sub_u32 v96, v96, v86\n"
            "v_mad_i32_i16 v88, v97, v114, v88\n"
            "ds_add_u32 v114, v96 offset:65533\n"
            "Ls3_%=:\n"
            "s_cmp_gt_i32 s75, s74\n"
            "s_cbranch_scc1 1f\n"
            "s_waitcnt vmcnt(6)\n"
            "1:\n"
            "s_waitcnt vmcnt(11)\n"
            "s_cmp_eq_u32 s75, s79\n"
            "s_cbranch_scc1 Le3_%=\n"
            "Lc3_%=:\n"
            "v_and_b32 v104, 0x1fffc, v24\n"
            "v_and_b32 v105, 0x1fffc, v25\n"
            "v_and_b32 v106, 0x1fffc, v26\n"
            "ds_read_b32 v93, v104\n"
            "ds_read_b32 v95, v105\n"
            "ds_read_b32 v97, v106\n"
            "v_add_u32_dpp v111, v81, v82 row_newbcast:3 row_mask:0xf bank_mask:0x3\n"
            "v_add_u32_dpp v111, v81, v82 row_newbcast:11 row_mask:0xf bank_mask:0xc\n"
            "v_and_b32 v108, 0x7fe, v23\n"
            "v_and_b32 v109, 0x1ff800, v23\n"
            "v_lshrrev_b32 v110, 22, v23\n"
            "v_lshrrev_b32 v112, 15, v20\n"
            "v_lshrrev_b32 v113, 15, v21\n"
            "v_lshrrev_b32 v114, 15, v22\n"
            "v_and_b32 v111, " IDXMASK ", v111\n"
            "v_min_u32 v111, v111, v83\n"
            "buffer_load_dwordx4 v[20:23], v111, %[rs], 0 idxen " RAUX "\n"
            "v_fmamk_f32 v108, v108, 0x7e800000, v84\n"
            "v_fmamk_f32 v109, v109, 0x79800000, v84\n"
            "v_fmamk_f32 v110, v110, 0x7f000000, v84\n"
            "v_or_b32 v112, 3, v112\n"
            "v_or_b32 v113, 3, v113\n"
            "v_or_b32 v114, 3, v114\n"
            "s_sub_u32 s75, s75, 1\n"
            "s_waitcnt lgkmcnt(6)\n"
            "v_lshlrev_b32 v98, 16, v99\n"
            "v_lshlrev_b32 v100, 16, v101\n"
            "v_lshlrev_b32 v102, 16, v103\n"
            "v_pk_fma_f32 v[98:99], v[98:99], v[108:109], v[86:87] op_sel:[0,0,0] op_sel_hi:[1,0,1]\n"
            "v_pk_fma_f32 v[100:101], v[100:101], v[108:109], v[86:87] op_sel:[0,1,0] op_sel_hi:[1,1,1]\n"
            "v_pk_fma_f32 v[102:103], v[102:103], v[110:111], v[86:87] op_sel:[0,0,0] op_sel_hi:[1,0,1]\n"
            "v_lshl_add_u32 v98, v99, 16, v98\n"
            "v_lshl_add_u32 v100, v101, 16, v100\n"
            "v_lshl_add_u32 v102, v103, 16, v102\n"
            "v_sub_u32 v98, v98, v86\n"
            "v_mad_i32_i16 v88, v99, v112, v88\n"
            "ds_add_u32 v112, v98 offset:65533\n"
            "v_sub_u32 v100, v100, v86\n"
            "v_mad_i32_i16 v88, v101, v113, v88\n"
            "ds_add_u32 v113, v100 offset:65533\n"
            "v_sub_u32 v102, v102, v86\n"
            "v_mad_i32_i16 v88, v103, v114, v88\n"
            "ds_add_u32 v114, v102 offset:65533\n"
            "Ls4_%=:\n"
            "s_cmp_gt_i32 s75, s74\n"
            "s_cbranch_scc1 1f\n"
            "s_waitcnt vmcnt(6)\n"
            "1:\n"
            "s_waitcnt vmcnt(11)\n"
            "s_cmp_eq_u32 s75, s79\n"
            "s_cbranch_scc1 Le4_%=\n"
            "Lc4_%=:\n"
            "v_and_b32 v104, 0x1fffc, v28\n"
            "v_and_b32 v105, 0x1fffc, v29\n"
            "v_and_b32 v106, 0x1fffc, v30\n"
            "ds_read_b32 v99, v104\n"
            "ds_read_b32 v101, v105\n"
            "ds_read_b32 v103, v106\n"
            "v_add_u32_dpp v111, v81, v82 row_newbcast:4 row_mask:0xf bank_mask:0x3\n"
            "v_add_u32_dpp v111, v81, v82 row_newbcast:12 row_mask:0xf bank_mask:0xc\n"
            "v_and_b32 v108, 0x7fe, v27\n"
            "v_and_b32 v109, 0x1ff800, v27\n"
            "v_lshrrev_b32 v110, 22, v27\n"
            "v_lshrrev_b32 v112, 15, v24\n"
            "v_lshrrev_b32 v113, 15, v25\n"
            "v_lshrrev_b32 v114, 15, v26\n"
            "v_and_b32 v111, " IDXMASK ", v111\n"
            "v_min_u32 v111, v111, v83\n"
            "buffer_load_dwordx4 v[24:27], v111, %[rs], 0 idxen " RAUX "\n"
            "v_fmamk_f32 v108, v108, 0x7e800000, v84\n"
            "v_fmamk_f32 v109, v109, 0x79800000, v84\n"
            "v_fmamk_f32 v110, v110, 0x7f000000, v84\n"
            "v_or_b32 v112, 3, v112\n"
            "v_or_b32 v113, 3, v113\n"
            "v_or_b32 v114, 3, v114\n"
            "s_sub_u32 s75, s75, 1\n"
            "s_waitcnt lgkmcnt(6)\n"
            "v_lshlrev_b32 v92, 16, v93\n"
            "v_lshlrev_b32 v94, 16, v95\n"
            "v_lshlrev_b32 v96, 16, v97\n"
            "v_pk_fma_f32 v[92:93], v[92:93], v[108:109], v[86:87] op_sel:[0,0,0] op_sel_hi:[1,0,1]\n"
            "v_pk_fma_f32 v[94:95], v[94:95], v[108:109], v[86:87] op_sel:[0,1,0] op_sel_hi:[1,1,1]\n"
            "v_pk_fma_f32 v[96:97], v[96:97], v[110:111], v[86:87] op_sel:[0,0,0] op_sel_hi:[1,0,1]\n"
            "v_lshl_add_u32 v92, v93, 16, v92\n"
            "v_lshl_add_u32 v94, v95, 16, v94\n"
            "v_lshl_add_u32 v96, v97, 16, v96\n"
            "v_sub_u32 v92, v92, v86\n"
            "v_mad_i32_i16 v88, v93, v112, v88\n"
            "ds_add_u32 v112, v92 offset:65533\n"
            "v_sub_u32 v94, v94, v86\n"
            "v_mad_i32_i16 v88, v95, v113, v88\n"
            "ds_add_u32 v113, v94 offset:65533\n"
            "v_sub_u32 v96, v96, v86\n"
            "v_mad_i32_i16 v88, v97, v114, v88\n"
            "ds_add_u32 v114, v96 offset:65533\n"
            "Ls5_%=:\n"
            "s_cmp_gt_i32 s75, s74\n"
            "s_cbranch_scc1 1f\n"
            "s_waitcnt vmcnt(6)\n"
            "1:\n"
            "s_waitcnt vmcnt(11)\n"
            "s_cmp_eq_u32 s75, s79\n"
            "s_cbranch_scc1 Le5_%=\n"
            "Lc5_%=:\n"
            "v_and_b32 v104, 0x1fffc, v32\n"
            "v_and_b32 v105, 0x1fffc, v33\n"
            "v_and_b32 v106, 0x1fffc, v34\n"
            "ds_read_b32 v93, v104\n"
            "ds_read_b32 v95, v105\n"
            "ds_read_b32 v97, v106\n"
            "v_add_u32_dpp v111, v81, v82 row_newbcast:5 row_mask:0xf bank_mask:0x3\n"
            "v_add_u32_dpp v111, v81, v82 row_newbcast:13 row_mask:0xf bank_mask:0xc\n"
            "v_and_b32 v108, 0x7fe, v31\n"
            "v_and_b32 v109, 0x1ff800, v31\n"
            "v_lshrrev_b32 v110, 22, v31\n"
            "v_lshrrev_b32 v112, 15, v28\n"
            "v_lshrrev_b32 v113, 15, v29\n"
            "v_lshrrev_b32 v114, 15, v30\n"
            "v_and_b32 v111, " IDXMASK ", v111\n"
            "v_min_u32 v111, v111, v83\n"
            "buffer_load_dwordx4 v[28:31], v111, %[rs], 0 idxen " RAUX "\n"
            "v_fmamk_f32 v108, v108, 0x7e800000, v84\n"
            "v_fmamk_f32 v109, v109, 0x79800000, v84\n"
            "v_fmamk_f32 v110, v110, 0x7f000000, v84\n"
            "v_or_b32 v112, 3, v112\n"
            "v_or_b32 v113, 3, v113\n"
            "v_or_b32 v114, 3, v114\n"
            "s_sub_u32 s75, s75, 1\n"
            "s_waitcnt lgkmcnt(6)\n"
            "v_lshlrev_b32 v98, 16, v99\n"
            "v_lshlrev_b32 v100, 16, v101\n"
            "v_lshlrev_b32 v102, 16, v103\n"
            "v_pk_fma_f32 v[98:99], v[98:99], v[108:109], v[86:87] op_sel:[0,0,0] op_sel_hi:[1,0,1]\n"
            "v_pk_fma_f32 v[100:101], v[100:101], v[108:109], v[86:87] op_sel:[0,1,0] op_sel_hi:[1,1,1]\n"
            "v_pk_fma_f32 v[102:103], v[102:103], v[110:111], v[86:87] op_sel:[0,0,0] op_sel_hi:[1,0,1]\n"
            "v_lshl_add_u32 v98, v99, 16, v98\n"
            "v_lshl_add_u32 v100, v101, 16, v100\n"
            "v_lshl_add_u32 v102, v103, 16, v102\n"
            "v_sub_u32 v98, v98, v86\n"
            "v_mad_i32_i16 v88, v99, v112, v88\n"
            "ds_add_u32 v112, v98 offset:65533\n"
            "v_sub_u32 v100, v100, v86\n"
            "v_mad_i32_i16 v88, v101, v113, v88\n"
            "ds_add_u32 v113, v100 offset:65533\n"
            "v_sub_u32 v102, v102, v86\n"
            "v_mad_i32_i16 v88, v103, v114, v88\n"
            "ds_add_u32 v114, v102 offset:65533\n"
            "Ls6_%=:\n"
            "s_cmp_gt_i32 s75, s74\n"
            "s_cbranch_scc1 1f\n"
            "s_waitcnt vmcnt(6)\n"
            "1:\n"
            "s_waitcnt vmcnt(11)\n"
            "s_cmp_eq_u32 s75, s79\n"
            "s_cbranch_scc1 Le6_%=\n"
            "Lc6_%=:\n"
            "v_and_b32 v104, 0x1fffc, v36\n"
            "v_and_b32 v105, 0x1fffc, v37\n"
            "v_and_b32 v106, 0x1fffc, v38\n"
            "ds_read_b32 v99, v104\n"
            "ds_read_b32 v101, v105\n"
            "ds_read_b32 v103, v106\n"
            "v_add_u32_dpp v111, v81, v82 row_newbcast:6 row_mask:0xf bank_mask:0x3\n"
            "v_add_u32_dpp v111, v81, v82 row_newbcast:14 row_mask:0xf bank_mask:0xc\n"
            "v_and_b32 v108, 0x7fe, v35\n"
            "v_and_b32 v109, 0x1ff800, v35\n"
            "v_lshrrev_b32 v110, 22, v35\n"
            "v_lshrrev_b32 v112, 15, v32\n"
            "v_lshrrev_b32 v113, 15, v33\n"
            "v_lshrrev_b32 v114, 15, v34\n"
            "v_and_b32 v111, " IDXMASK ", v111\n"
            "v_min_u32 v111, v111, v83\n"
            "buffer_load_dwordx4 v[32:35], v111, %[rs], 0 idxen " RAUX "\n"
            "v_fmamk_f32 v108, v108, 0x7e800000, v84\n"
            "v_fmamk_f32 v109, v109, 0x79800000, v84\n"
            "v_fmamk_f32 v110, v110, 0x7f000000, v84\n"
            "v_or_b32 v112, 3, v112\n"
            "v_or_b32 v113, 3, v113\n"
            "v_or_b32 v114, 3, v114\n"
            "s_sub_u32 s75, s75, 1\n"
            "s_waitcnt lgkmcnt(6)\n"
            "v_lshlrev_b32 v92, 16, v93\n"
            "v_lshlrev_b32 v94, 16, v95\n"
            "v_lshlrev_b32 v96, 16, v97\n"
            "v_pk_fma_f32 v[92:93], v[92:93], v[108:109], v[86:87] op_sel:[0,0,0] op_sel_hi:[1,0,1]\n"
            "v_pk_fma_f32 v[94:95], v[94:95], v[108:109], v[86:87] op_sel:[0,1,0] op_sel_hi:[1,1,1]\n"
            "v_pk_fma_f32 v[96:97], v[96:97], v[110:111], v[86:87] op_sel:[0,0,0] op_sel_hi:[1,0,1]\n"
            "v_lshl_add_u32 v92, v93, 16, v92\n"
            "v_lshl_add_u32 v94, v95, 16, v94\n"
            "v_lshl_add_u32 v96, v97, 16, v96\n"
            "v_sub_u32 v92, v92, v86\n"
            "v_mad_i32_i16 v88, v93, v112, v88\n"
            "ds_add_u32 v112, v92 offset:65533\n"
            "v_sub_u32 v94, v94, v86\n"
            "v_mad_i32_i16 v88, v95, v113, v88\n"
            "ds_add_u32 v113, v94 offset:65533\n"
            "v_sub_u32 v96, v96, v86\n"
            "v_mad_i32_i16 v88, v97, v114, v88\n"
            "ds_add_u32 v114, v96 offset:65533\n"
            "Ls7_%=:\n"
            "s_cmp_gt_i32 s75, s74\n"
            "s_cbranch_scc1 1f\n"
            "s_waitcnt vmcnt(6)\n"
            "1:\n"
            "s_waitcnt vmcnt(11)\n"
            "s_cmp_eq_u32 s75, s79\n"
            "s_cbranch_scc1 Le7_%=\n"
            "Lc7_%=:\n"
            "v_and_b32 v104, 0x1fffc, v8\n"
            "v_and_b32 v105, 0x1fffc, v9\n"
            "v_and_b32 v106, 0x1fffc, v10\n"
            "ds_read_b32 v93, v104\n"
            "ds_read_b32 v95, v105\n"
            "ds_read_b32 v97, v106\n"
            "v_add_u32_dpp v111, v81, v82 row_newbcast:7 row_mask:0xf bank_mask:0x3\n"
            "v_add_u32_dpp v111, v81, v82 row_newbcast:15 row_mask:0xf bank_mask:0xc\n"
            "v_and_b32 v108, 0x7fe, v39\n"
            "v_and_b32 v109, 0x1ff800, v39\n"
            "v_lshrrev_b32 v110, 22, v39\n"
            "v_lshrrev_b32 v112, 15, v36\n"
            "v_lshrrev_b32 v113, 15, v37\n"
            "v_lshrrev_b32 v114, 15, v38\n"
            "v_and_b32 v111, " IDXMASK ", v111\n"
            "v_min_u32 v111, v111, v83\n"
            "buffer_load_dwordx4 v[36:39], v111, %[rs], 0 idxen " RAUX "\n"
            "s_min_u32 s82, s82, 19\n"
            "s_add_u32 s82, s82, 1\n"
            "s_set_gpr_idx_on s82, gpr_idx(SRC0)\n"
            "s_nop 0\n"
            "v_mov_b32 v81, v60\n"
            "s_set_gpr_idx_off\n"
            "v_fmamk_f32 v108, v108, 0x7e800000, v84\n"
            "v_fmamk_f32 v109, v109, 0x79800000, v84\n"
            "v_fmamk_f32 v110, v110, 0x7f000000, v84\n"
            "v_or_b32 v112, 3, v112\n"
            "v_or_b32 v113, 3, v113\n"
            "v_or_b32 v114, 3, v114\n"
            "s_sub_u32 s75, s75, 1\n"
            "s_waitcnt lgkmcnt(6)\n"
            "v_lshlrev_b32 v98, 16, v99\n"
            "v_lshlrev_b32 v100, 16, v101\n"
            "v_lshlrev_b32 v102, 16, v103\n"
            "v_pk_fma_f32 v[98:99], v[98:99], v[108:109], v[86:87] op_sel:[0,0,0] op_sel_hi:[1,0,1]\n"
            "v_pk_fma_f32 v[100:101], v[100:101], v[108:109], v[86:87] op_sel:[0,1,0] op_sel_hi:[1,1,1]\n"
            "v_pk_fma_f32 v[102:103], v[102:103], v[110:111], v[86:87] op_sel:[0,0,0] op_sel_hi:[1,0,1]\n"
            "v_lshl_add_u32 v98, v99, 16, v98\n"
            "v_lshl_add_u32 v100, v101, 16, v100\n"
            "v_lshl_add_u32 v102, v103, 16, v102\n"
            "v_sub_u32 v98, v98, v86\n"
            "v_mad_i32_i16 v88, v99, v112, v88\n"
            "ds_add_u32 v112, v98 offset:65533\n"
            "v_sub_u32 v100, v100, v86\n"
            "v_mad_i32_i16 v88, v101, v113, v88\n"
            "ds_add_u32 v113, v100 offset:65533\n"
            "v_sub_u32 v102, v102, v86\n"
            "v_mad_i32_i16 v88, v103, v114, v88\n"
            "ds_add_u32 v114, v102 offset:65533\n"
            "s_branch Ls0_%=\n"
            "Le0_%=:\n"
            "s_cmp_lt_u32 s75, 8\n"
            "s_cbranch_scc1 1f\n"
            "s_swappc_b64 s[80:81], s[66:67]\n"
            "s_branch Lc0_%=\n"
            "1:\n"
            "s_cmp_lg_u32 s75, 7\n"
            "s_cbranch_scc1 1f\n"
            "s_setprio 2\n"
            "s_mov_b32 s79, 3\n"
            "s_branch Lc0_%=\n"
            "1:\n"
            "s_cmp_lg_u32 s75, 3\n"
            "s_cbranch_scc1 1f\n"
            "s_setprio 1\n"
            "s_mov_b32 s79, 1\n"
            "s_branch Lc0_%=\n"
            "1:\n"
            "s_setprio 0\n"
            "v_add_u32_dpp v111, v81, v82 row_newbcast:0 row_mask:0xf bank_mask:0x3\n"
            "v_add_u32_dpp v111, v81, v82 row_newbcast:8 row_mask:0xf bank_mask:0xc\n"
            "v_and_b32 v108, 0x7fe, v11\n"
            "v_and_b32 v109, 0x1ff800, v11\n"
            "v_lshrrev_b32 v110, 22, v11\n"
            "v_lshrrev_b32 v112, 15, v8\n"
            "v_lshrrev_b32 v113, 15, v9\n"
            "v_lshrrev_b32 v114, 15, v10\n"
            "v_and_b32 v111, " IDXMASK ", v111\n"
            "v_min_u32 v111, v111, v83\n"
            "buffer_load_dwordx4 v[8:11], v111, %[rs], 0 idxen " RAUX "\n"
            "v_fmamk_f32 v108, v108, 0x7e800000, v84\n"
            "v_fmamk_f32 v109, v109, 0x79800000, v84\n"
            "v_fmamk_f32 v110, v110, 0x7f000000, v84\n"
            "v_or_b32 v112, 3, v112\n"
            "v_or_b32 v113, 3, v113\n"
            "v_or_b32 v114, 3, v114\n"
            "s_waitcnt lgkmcnt(3)\n"
            "s_swappc_b64 s[80:81], s[70:71]\n"
            "v_lshlrev_b32 v92, 16, v93\n"
            "v_lshlrev_b32 v94, 16, v95\n"
            "v_lshlrev_b32 v96, 16, v97\n"
            "v_pk_fma_f32 v[92:93], v[92:93], v[108:109], v[86:87] op_sel:[0,0,0] op_sel_hi:[1,0,1]\n"
            "v_pk_fma_f32 v[94:95], v[94:95], v[108:109], v[86:87] op_sel:[0,1,0] op_sel_hi:[1,1,1]\n"
            "v_pk_fma_f32 v[96:97], v[96:97], v[110:111], v[86:87] op_sel:[0,0,0] op_sel_hi:[1,0,1]\n"
            "v_lshl_add_u32 v92, v93, 16, v92\n"
            "v_lshl_add_u32 v94, v95, 16, v94\n"
            "v_lshl_add_u32 v96, v97, 16, v96\n"
            "v_sub_u32 v92, v92, v86\n"
            "v_mad_i32_i16 v88, v93, v112, v88\n"
            "ds_add_u32 v112, v92 offset:65533\n"
            "v_sub_u32 v94, v94, v86\n"
            "v_mad_i32_i16 v88, v95, v113, v88\n"
            "ds_add_u32 v113, v94 offset:65533\n"
            "v_sub_u32 v96, v96, v86\n"
            "v_mad_i32_i16 v88, v97, v114, v88\n"
            "ds_add_u32 v114, v96 offset:65533\n"
            "v_and_b32 v104, 0x1fffc, v12\n"
            "v_and_b32 v105, 0x1fffc, v13\n"
            "v_and_b32 v106, 0x1fffc, v14\n"
            "s_swappc_b64 s[80:81], s[72:73]\n"
            "ds_read_b32 v99, v104\n"
            "ds_read_b32 v101, v105\n"
            "ds_read_b32 v103, v106\n"
            "s_waitcnt lgkmcnt(0)\n"
            "s_branch Ls1_%=\n"
            "Le1_%=:\n"
            "s_cmp_lt_u32 s75, 8\n"
            "s_cbranch_scc1 1f\n"
            "s_swappc_b64 s[80:81], s[66:67]\n"
            "s_branch Lc1_%=\n"
            "1:\n"
            "s_cmp_lg_u32 s75, 7\n"
            "s_cbranch_scc1 1f\n"
            "s_setprio 2\n"
            "s_mov_b32 s79, 3\n"
            "s_branch Lc1_%=\n"
            "1:\n"
            "s_cmp_lg_u32 s75, 3\n"
            "s_cbranch_scc1 1f\n"
            "s_setprio 1\n"
            "s_mov_b32 s79, 1\n"
            "s_branch Lc1_%=\n"
            "1:\n"
            "s_setprio 0\n"
            "v_add_u32_dpp v111, v81, v82 row_newbcast:1 row_mask:0xf bank_mask:0x3\n"
            "v_add_u32_dpp v111, v81, v82 row_newbcast:9 row_mask:0xf bank_mask:0xc\n"
            "v_and_b32 v108, 0x7fe, v15\n"
            "v_and_b32 v109, 0x1ff800, v15\n"
            "v_lshrrev_b32 v110, 22, v15\n"
            "v_lshrrev_b32 v112, 15, v12\n"
            "v_lshrrev_b32 v113, 15, v13\n"
            "v_lshrrev_b32 v114, 15, v14\n"
            "v_and_b32 v111, " IDXMASK ", v111\n"
            "v_min_u32 v111, v111, v83\n"
            "buffer_load_dwordx4 v[12:15], v111, %[rs], 0 idxen " RAUX "\n"
            "v_fmamk_f32 v108, v108, 0x7e800000, v84\n"
            "v_fmamk_f32 v109, v109, 0x79800000, v84\n"
            "v_fmamk_f32 v110, v110, 0x7f000000, v84\n"
            "v_or_b32 v112, 3, v112\n"
            "v_or_b32 v113, 3, v113\n"
            "v_or_b32 v114, 3, v114\n"
            "s_waitcnt lgkmcnt(3)\n"
            "s_swappc_b64 s[80:81], s[70:71]\n"
            "v_lshlrev_b32 v98, 16, v99\n"
            "v_lshlrev_b32 v100, 16, v101\n"
            "v_lshlrev_b32 v102, 16, v103\n"
            "v_pk_fma_f32 v[98:99], v[98:99], v[108:109], v[86:87] op_sel:[0,0,0] op_sel_hi:[1,0,1]\n"
            "v_pk_fma_f32 v[100:101], v[100:101], v[108:109], v[86:87] op_sel:[0,1,0] op_sel_hi:[1,1,1]\n"
            "v_pk_fma_f32 v[102:103], v[102:103], v[110:111], v[86:87] op_sel:[0,0,0] op_sel_hi:[1,0,1]\n"
            "v_lshl_add_u32 v98, v99, 16, v98\n"
            "v_lshl_add_u32 v100, v101, 16, v100\n"
            "v_lshl_add_u32 v102, v103, 16, v102\n"
            "v_sub_u32 v98, v98, v86\n"
            "v_mad_i32_i16 v88, v99, v112, v88\n"
            "ds_add_u32 v112, v98 offset:65533\n"
            "v_sub_u32 v100, v100, v86\n"
            "v_mad_i32_i16 v88, v101, v113, v88\n"
            "ds_add_u32 v113, v100 offset:65533\n"
            "v_sub_u32 v102, v102, v86\n"
            "v_mad_i32_i16 v88, v103, v114, v88\n"
            "ds_add_u32 v114, v102 offset:65533\n"
            "v_and_b32 v104, 0x1fffc, v16\n"
            "v_and_b32 v105, 0x1fffc, v17\n"
            "v_and_b32 v106, 0x1fffc, v18\n"
            "s_swappc_b64 s[80:81], s[72:73]\n"
            "ds_read_b32 v93, v104\n"
            "ds_read_b32 v95, v105\n"
            "ds_read_b32 v97, v106\n"
            "s_waitcnt lgkmcnt(0)\n"
            "s_branch Ls2_%=\n"
            "Le2_%=:\n"
            "s_cmp_lt_u32 s75, 8\n"
            "s_cbranch_scc1 1f\n"
            "s_swappc_b64 s[80:81], s[66:67]\n"
            "s_branch Lc2_%=\n"
            "1:\n"
            "s_cmp_lg_u32 s75, 7\n"
            "s_cbranch_scc1 1f\n"
            "s_setprio 2\n"
            "s_mov_b32 s79, 3\n"
            "s_branch Lc2_%=\n"
            "1:\n"
            "s_cmp_lg_u32 s75, 3\n"
            "s_cbranch_scc1 1f\n"
            "s_setprio 1\n"
            "s_mov_b32 s79, 1\n"
            "s_branch Lc2_%=\n"
            "1:\n"
            "s_setprio 0\n"
            "v_add_u32_dpp v111, v81, v82 row_newbcast:2 row_mask:0xf bank_mask:0x3\n"
            "v_add_u32_dpp v111, v81, v82 row_newbcast:10 row_mask:0xf bank_mask:0xc\n"
            "v_and_b32 v108, 0x7fe, v19\n"
            "v_and_b32 v109, 0x1ff800, v19\n"
            "v_lshrrev_b32 v110, 22, v19\n"
            "v_lshrrev_b32 v112, 15, v16\n"
            "v_lshrrev_b32 v113, 15, v17\n"
            "v_lshrrev_b32 v114, 15, v18\n"
            "v_and_b32 v111, " IDXMASK ", v111\n"
            "v_min_u32 v111, v111, v83\n"
            "buffer_load_dwordx4 v[16:19], v111, %[rs], 0 idxen " RAUX "\n"
            "v_fmamk_f32 v108, v108, 0x7e800000, v84\n"
            "v_fmamk_f32 v109, v109, 0x79800000, v84\n"
            "v_fmamk_f32 v110, v110, 0x7f000000, v84\n"
            "v_or_b32 v112, 3, v112\n"
            "v_or_b32 v113, 3, v113\n"
            "v_or_b32 v114, 3, v114\n"
            "s_waitcnt lgkmcnt(3)\n"
            "s_swappc_b64 s[80:81], s[70:71]\n"
            "v_lshlrev_b32 v92, 16, v93\n"
            "v_lshlrev_b32 v94, 16, v95\n"
            "v_lshlrev_b32 v96, 16, v97\n"
            "v_pk_fma_f32 v[92:93], v[92:93], v[108:109], v[86:87] op_sel:[0,0,0] op_sel_hi:[1,0,1]\n"
            "v_pk_fma_f32 v[94:95], v[94:95], v[108:109], v[86:87] op_sel:[0,1,0] op_sel_hi:[1,1,1]\n"
            "v_pk_fma_f32 v[96:97], v[96:97], v[110:111], v[86:87] op_sel:[0,0,0] op_sel_hi:[1,0,1]\n"
            "v_lshl_add_u32 v92, v93, 16, v92\n"
            "v_lshl_add_u32 v94, v95, 16, v94\n"
            "v_lshl_add_u32 v96, v97, 16, v96\n"
            "v_sub_u32 v92, v92, v86\n"
            "v_mad_i32_i16 v88, v93, v112, v88\n"
            "ds_add_u32 v112, v92 offset:65533\n"
            "v_sub_u32 v94, v94, v86\n"
            "v_mad_i32_i16 v88, v95, v113, v88\n"
            "ds_add_u32 v113, v94 offset:65533\n"
            "v_sub_u32 v96, v96, v86\n"
            "v_mad_i32_i16 v88, v97, v114, v88\n"
            "ds_add_u32 v114, v96 offset:65533\n"
            "v_and_b32 v104, 0x1fffc, v20\n"
            "v_and_b32 v105, 0x1fffc, v21\n"
            "v_and_b32 v106, 0x1fffc, v22\n"
            "s_swappc_b64 s[80:81], s[72:73]\n"
            "ds_read_b32 v99, v104\n"
            "ds_read_b32 v101, v105\n"
            "ds_read_b32 v103, v106\n"
            "s_waitcnt lgkmcnt(0)\n"
            "s_branch Ls3_%=\n"
            "Le3_%=:\n"
            "s_cmp_lt_u32 s75, 8\n"
            "s_cbranch_scc1 1f\n"
            "s_swappc_b64 s[80:81], s[66:67]\n"
            "s_branch Lc3_%=\n"
            "1:\n"
            "s_cmp_lg_u32 s75, 7\n"
            "s_cbranch_scc1 1f\n"
            "s_setprio 2\n"
            "s_mov_b32 s79, 3\n"
            "s_branch Lc3_%=\n"
            "1:\n"
            "s_cmp_lg_u32 s75, 3\n"
            "s_cbranch_scc1 1f\n"
            "s_setprio 1\n"
            "s_mov_b32 s79, 1\n"
            "s_branch Lc3_%=\n"
            "1:\n"
            "s_setprio 0\n"
            "v_add_u32_dpp v111, v81, v82 row_newbcast:3 row_mask:0xf bank_mask:0x3\n"
            "v_add_u32_dpp v111, v81, v82 row_newbcast:11 row_mask:0xf bank_mask:0xc\n"
            "v_and_b32 v108, 0x7fe, v23\n"
            "v_and_b32 v109, 0x1ff800, v23\n"
            "v_lshrrev_b32 v110, 22, v23\n"
            "v_lshrrev_b32 v112, 15, v20\n"
            "v_lshrrev_b32 v113, 15, v21\n"
            "v_lshrrev_b32 v114, 15, v22\n"
            "v_and_b32 v111, " IDXMASK ", v111\n"
            "v_min_u32 v111, v111, v83\n"
            "buffer_load_dwordx4 v[20:23], v111, %[rs], 0 idxen " RAUX "\n"
            "v_fmamk_f32 v108, v108, 0x7e800000, v84\n"
            "v_fmamk_f32 v109, v109, 0x79800000, v84\n"
            "v_fmamk_f32 v110, v110, 0x7f000000, v84\n"
            "v_or_b32 v112, 3, v112\n"
            "v_or_b32 v113, 3, v113\n"
            "v_or_b32 v114, 3, v114\n"
            "s_waitcnt lgkmcnt(3)\n"
            "s_swappc_b64 s[80:81], s[70:71]\n"
            "v_lshlrev_b32 v98, 16, v99\n"
            "v_lshlrev_b32 v100, 16, v101\n"
            "v_lshlrev_b32 v102, 16, v103\n"
            "v_pk_fma_f32 v[98:99], v[98:99], v[108:109], v[86:87] op_sel:[0,0,0] op_sel_hi:[1,0,1]\n"
            "v_pk_fma_f32 v[100:101], v[100:101], v[108:109], v[86:87] op_sel:[0,1,0] op_sel_hi:[1,1,1]\n"
            "v_pk_fma_f32 v[102:103], v[102:103], v[110:111], v[86:87] op_sel:[0,0,0] op_sel_hi:[1,0,1]\n"
            "v_lshl_add_u32 v98, v99, 16, v98\n"
            "v_lshl_add_u32 v100, v101, 16, v100\n"
            "v_lshl_add_u32 v102, v103, 16, v102\n"
            "v_sub_u32 v98, v98, v86\n"
            "v_mad_i32_i16 v88, v99, v112, v88\n"
            "ds_add_u32 v112, v98 offset:65533\n"
            "v_sub_u32 v100, v100, v86\n"
            "v_mad_i32_i16 v88, v101, v113, v88\n"
            "ds_add_u32 v113, v100 offset:65533\n"
            "v_sub_u32 v102, v102, v86\n"
            "v_mad_i32_i16 v88, v103, v114, v88\n"
            "ds_add_u32 v114, v102 offset:65533\n"
            "v_and_b32 v104, 0x1fffc, v24\n"
            "v_and_b32 v105, 0x1fffc, v25\n"
            "v_and_b32 v106, 0x1fffc, v26\n"
            "s_swappc_b64 s[80:81], s[72:73]\n"
            "ds_read_b32 v93, v104\n"
            "ds_read_b32 v95, v105\n"
            "ds_read_b32 v97, v106\n"
            "s_waitcnt lgkmcnt(0)\n"
            "s_branch Ls4_%=\n"
            "Le4_%=:\n"
            "s_cmp_lt_u32 s75, 8\n"
            "s_cbranch_scc1 1f\n"
            "s_swappc_b64 s[80:81], s[66:67]\n"
            "s_branch Lc4_%=\n"
            "1:\n"
            "s_cmp_lg_u32 s75, 7\n"
            "s_cbranch_scc1 1f\n"
            "s_setprio 2\n"
            "s_mov_b32 s79, 3\n"
            "s_branch Lc4_%=\n"
            "1:\n"
            "s_cmp_lg_u32 s75, 3\n"
            "s_cbranch_scc1 1f\n"
            "s_setprio 1\n"
            "s_mov_b32 s79, 1\n"
            "s_branch Lc4_%=\n"
            "1:\n"
            "s_setprio 0\n"
            "v_add_u32_dpp v111, v81, v82 row_newbcast:4 row_mask:0xf bank_mask:0x3\n"
            "v_add_u32_dpp v111, v81, v82 row_newbcast:12 row_mask:0xf bank_mask:0xc\n"
            "v_and_b32 v108, 0x7fe, v27\n"
            "v_and_b32 v109, 0x1ff800, v27\n"
            "v_lshrrev_b32 v110, 22, v27\n"
            "v_lshrrev_b32 v112, 15, v24\n"
            "v_lshrrev_b32 v113, 15, v25\n"
            "v_lshrrev_b32 v114, 15, v26\n"
            "v_and_b32 v111, " IDXMASK ", v111\n"
            "v_min_u32 v111, v111, v83\n"
            "buffer_load_dwordx4 v[24:27], v111, %[rs], 0 idxen " RAUX "\n"
            "v_fmamk_f32 v108, v108, 0x7e800000, v84\n"
            "v_fmamk_f32 v109, v109, 0x79800000, v84\n"
            "v_fmamk_f32 v110, v110, 0x7f000000, v84\n"
            "v_or_b32 v112, 3, v112\n"
            "v_or_b32 v113, 3, v113\n"
            "v_or_b32 v114, 3, v114\n"
            "s_waitcnt lgkmcnt(3)\n"
            "s_swappc_b64 s[80:81], s[70:71]\n"
            "v_lshlrev_b32 v92, 16, v93\n"
            "v_lshlrev_b32 v94, 16, v95\n"
            "v_lshlrev_b32 v96, 16, v97\n"
            "v_pk_fma_f32 v[92:93], v[92:93], v[108:109], v[86:87] op_sel:[0,0,0] op_sel_hi:[1,0,1]\n"
            "v_pk_fma_f32 v[94:95], v[94:95], v[108:109], v[86:87] op_sel:[0,1,0] op_sel_hi:[1,1,1]\n"
            "v_pk_fma_f32 v[96:97], v[96:97], v[110:111], v[86:87] op_sel:[0,0,0] op_sel_hi:[1,0,1]\n"
            "v_lshl_add_u32 v92, v93, 16, v92\n"
            "v_lshl_add_u32 v94, v95, 16, v94\n"
            "v_lshl_add_u32 v96, v97, 16, v96\n"
            "v_sub_u32 v92, v92, v86\n"
            "v_mad_i32_i16 v88, v93, v112, v88\n"
            "ds_add_u32 v112, v92 offset:65533\n"
            "v_sub_u32 v94, v94, v86\n"
            "v_mad_i32_i16 v88, v95, v113, v88\n"
            "ds_add_u32 v113, v94 offset:65533\n"
            "v_sub_u32 v96, v96, v86\n"
            "v_mad_i32_i16 v88, v97, v114, v88\n"
            "ds_add_u32 v114, v96 offset:65533\n"
            "v_and_b32 v104, 0x1fffc, v28\n"
            "v_and_b32 v105, 0x1fffc, v29\n"
            "v_and_b32 v106, 0x1fffc, v30\n"
            "s_swappc_b64 s[80:81], s[72:73]\n"
            "ds_read_b32 v99, v104\n"
            "ds_read_b32 v101, v105\n"
            "ds_read_b32 v103, v106\n"
            "s_waitcnt lgkmcnt(0)\n"
            "s_branch Ls5_%=\n"
            "Le5_%=:\n"
            "s_cmp_lt_u32 s75, 8\n"
            "s_cbranch_scc1 1f\n"
            "s_swappc_b64 s[80:81], s[66:67]\n"
            "s_branch Lc5_%=\n"
            "1:\n"
            "s_cmp_lg_u32 s75, 7\n"
            "s_cbranch_scc1 1f\n"
            "s_setprio 2\n"
            "s_mov_b32 s79, 3\n"
            "s_branch Lc5_%=\n"
            "1:\n"
            "s_cmp_lg_u32 s75, 3\n"
            "s_cbranch_scc1 1f\n"
            "s_setprio 1\n"
            "s_mov_b32 s79, 1\n"
            "s_branch Lc5_%=\n"
            "1:\n"
            "s_setprio 0\n"
            "v_add_u32_dpp v111, v81, v82 row_newbcast:5 row_mask:0xf bank_mask:0x3\n"
            "v_add_u32_dpp v111, v81, v82 row_newbcast:13 row_mask:0xf bank_mask:0xc\n"
            "v_and_b32 v108, 0x7fe, v31\n"
            "v_and_b32 v109, 0x1ff800, v31\n"
            "v_lshrrev_b32 v110, 22, v31\n"
            "v_lshrrev_b32 v112, 15, v28\n"
            "v_lshrrev_b32 v113, 15, v29\n"
            "v_lshrrev_b32 v114, 15, v30\n"
            "v_and_b32 v111, " IDXMASK ", v111\n"
            "v_min_u32 v111, v111, v83\n"
            "buffer_load_dwordx4 v[28:31], v111, %[rs], 0 idxen " RAUX "\n"
            "v_fmamk_f32 v108, v108, 0x7e800000, v84\n"
            "v_fmamk_f32 v109, v109, 0x79800000, v84\n"
            "v_fmamk_f32 v110, v110, 0x7f000000, v84\n"
            "v_or_b32 v112, 3, v112\n"
            "v_or_b32 v113, 3, v113\n"
            "v_or_b32 v114, 3, v114\n"
            "s_waitcnt lgkmcnt(3)\n"
            "s_swappc_b64 s[80:81], s[70:71]\n"
            "v_lshlrev_b32 v98, 16, v99\n"
            "v_lshlrev_b32 v100, 16, v101\n"
            "v_lshlrev_b32 v102, 16, v103\n"
            "v_pk_fma_f32 v[98:99], v[98:99], v[108:109], v[86:87] op_sel:[0,0,0] op_sel_hi:[1,0,1]\n"
            "v_pk_fma_f32 v[100:101], v[100:101], v[108:109], v[86:87] op_sel:[0,1,0] op_sel_hi:[1,1,1]\n"
            "v_pk_fma_f32 v[102:103], v[102:103], v[110:111], v[86:87] op_sel:[0,0,0] op_sel_hi:[1,0,1]\n"
            "v_lshl_add_u32 v98, v99, 16, v98\n"
            "v_lshl_add_u32 v100, v101, 16, v100\n"
            "v_lshl_add_u32 v102, v103, 16, v102\n"
            "v_sub_u32 v98, v98, v86\n"
            "v_mad_i32_i16 v88, v99, v112, v88\n"
            "ds_add_u32 v112, v98 offset:65533\n"
            "v_sub_u32 v100, v100, v86\n"
            "v_mad_i32_i16 v88, v101, v113, v88\n"
            "ds_add_u32 v113, v100 offset:65533\n"
            "v_sub_u32 v102, v102, v86\n"
            "v_mad_i32_i16 v88, v103, v114, v88\n"
            "ds_add_u32 v114, v102 offset:65533\n"
            "v_and_b32 v104, 0x1fffc, v32\n"
            "v_and_b32 v105, 0x1fffc, v33\n"
            "v_and_b32 v106, 0x1fffc, v34\n"
            "s_swappc_b64 s[80:81], s[72:73]\n"
            "ds_read_b32 v93, v104\n"
            "ds_read_b32 v95, v105\n"
            "ds_read_b32 v97, v106\n"
            "s_waitcnt lgkmcnt(0)\n"
            "s_branch Ls6_%=\n"
            "Le6_%=:\n"
            "s_cmp_lt_u32 s75, 8\n"
            "s_cbranch_scc1 1f\n"
            "s_swappc_b64 s[80:81], s[66:67]\n"
            "s_branch Lc6_%=\n"
            "1:\n"
            "s_cmp_lg_u32 s75, 7\n"
            "s_cbranch_scc1 1f\n"
            "s_setprio 2\n"
            "s_mov_b32 s79, 3\n"
            "s_branch Lc6_%=\n"
            "1:\n"
            "s_cmp_lg_u32 s75, 3\n"
            "s_cbranch_scc1 1f\n"
            "s_setprio 1\n"
            "s_mov_b32 s79, 1\n"
            "s_branch Lc6_%=\n"
            "1:\n"
            "s_setprio 0\n"
            "v_add_u32_dpp v111, v81, v82 row_newbcast:6 row_mask:0xf bank_mask:0x3\n"
            "v_add_u32_dpp v111, v81, v82 row_newbcast:14 row_mask:0xf bank_mask:0xc\n"
            "v_and_b32 v108, 0x7fe, v35\n"
            "v_and_b32 v109, 0x1ff800, v35\n"
            "v_lshrrev_b32 v110, 22, v35\n"
            "v_lshrrev_b32 v112, 15, v32\n"
            "v_lshrrev_b32 v113, 15, v33\n"
            "v_lshrrev_b32 v114, 15, v34\n"
            "v_and_b32 v111, " IDXMASK ", v111\n"
            "v_min_u32 v111, v111, v83\n"
            "buffer_load_dwordx4 v[32:35], v111, %[rs], 0 idxen " RAUX "\n"
            "v_fmamk_f32 v108, v108, 0x7e800000, v84\n"
            "v_fmamk_f32 v109, v109, 0x79800000, v84\n"
            "v_fmamk_f32 v110, v110, 0x7f000000, v84\n"
            "v_or_b32 v112, 3, v112\n"
            "v_or_b32 v113, 3, v113\n"
            "v_or_b32 v114, 3, v114\n"
            "s_waitcnt lgkmcnt(3)\n"
            "s_swappc_b64 s[80:81], s[70:71]\n"
            "v_lshlrev_b32 v92, 16, v93\n"
            "v_lshlrev_b32 v94, 16, v95\n"
            "v_lshlrev_b32 v96, 16, v97\n"
            "v_pk_fma_f32 v[92:93], v[92:93], v[108:109], v[86:87] op_sel:[0,0,0] op_sel_hi:[1,0,1]\n"
            "v_pk_fma_f32 v[94:95], v[94:95], v[108:109], v[86:87] op_sel:[0,1,0] op_sel_hi:[1,1,1]\n"
            "v_pk_fma_f32 v[96:97], v[96:97], v[110:111], v[86:87] op_sel:[0,0,0] op_sel_hi:[1,0,1]\n"
            "v_lshl_add_u32 v92, v93, 16, v92\n"
            "v_lshl_add_u32 v94, v95, 16, v94\n"
            "v_lshl_add_u32 v96, v97, 16, v96\n"
            "v_sub_u32 v92, v92, v86\n"
            "v_mad_i32_i16 v88, v93, v112, v88\n"
            "ds_add_u32 v112, v92 offset:65533\n"
            "v_sub_u32 v94, v94, v86\n"
            "v_mad_i32_i16 v88, v95, v113, v88\n"
            "ds_add_u32 v113, v94 offset:65533\n"
            "v_sub_u32 v96, v96, v86\n"
            "v_mad_i32_i16 v88, v97, v114, v88\n"
            "ds_add_u32 v114, v96 offset:65533\n"
            "v_and_b32 v104, 0x1fffc, v36\n"
            "v_and_b32 v105, 0x1fffc, v37\n"
            "v_and_b32 v106, 0x1fffc, v38\n"
            "s_swappc_b64 s[80:81], s[72:73]\n"
            "ds_read_b32 v99, v104\n"
            "ds_read_b32 v101, v105\n"
            "ds_read_b32 v103, v106\n"
            "s_waitcnt lgkmcnt(0)\n"
            "s_branch Ls7_%=\n"
            "Le7_%=:\n"
            "s_cmp_lt_u32 s75, 8\n"
            "s_cbranch_scc1 1f\n"
            "s_swappc_b64 s[80:81], s[66:67]\n"
            "s_branch Lc7_%=\n"
            "1:\n"
            "s_cmp_lg_u32 s75, 7\n"
            "s_cbranch_scc1 1f\n"
            "s_setprio 2\n"
            "s_mov_b32 s79, 3\n"
            "s_branch Lc7_%=\n"
            "1:\n"
            "s_cmp_lg_u32 s75, 3\n"
            "s_cbranch_scc1 1f\n"
            "s_setprio 1\n"
            "s_mov_b32 s79, 1\n"
            "s_branch Lc7_%=\n"
            "1:\n"
            "s_setprio 0\n"
            "v_add_u32_dpp v111, v81, v82 row_newbcast:7 row_mask:0xf bank_mask:0x3\n"
            "v_add_u32_dpp v111, v81, v82 row_newbcast:15 row_mask:0xf bank_mask:0xc\n"
            "v_and_b32 v108, 0x7fe, v39\n"
            "v_and_b32 v109, 0x1ff800, v39\n"
            "v_lshrrev_b32 v110, 22, v39\n"
            "v_lshrrev_b32 v112, 15, v36\n"
            "v_lshrrev_b32 v113, 15, v37\n"
            "v_lshrrev_b32 v114, 15, v38\n"
            "v_and_b32 v111, " IDXMASK ", v111\n"
            "v_min_u32 v111, v111, v83\n"
            "buffer_load_dwordx4 v[36:39], v111, %[rs], 0 idxen " RAUX "\n"
            "s_min_u32 s82, s82, 19\n"
            "s_add_u32 s82, s82, 1\n"
            "s_set_gpr_idx_on s82, gpr_idx(SRC0)\n"
            "s_nop 0\n"
            "v_mov_b32 v81, v60\n"
            "s_set_gpr_idx_off\n"
            "v_fmamk_f32 v108, v108, 0x7e800000, v84\n"
            "v_fmamk_f32 v109, v109, 0x79800000, v84\n"
            "v_fmamk_f32 v110, v110, 0x7f000000, v84\n"
            "v_or_b32 v112, 3, v112\n"
            "v_or_b32 v113, 3, v113\n"
            "v_or_b32 v114, 3, v114\n"
            "s_waitcnt lgkmcnt(3)\n"
            "s_swappc_b64 s[80:81], s[70:71]\n"
            "v_lshlrev_b32 v98, 16, v99\n"
            "v_lshlrev_b32 v100, 16, v101\n"
            "v_lshlrev_b32 v102, 16, v103\n"
            "v_pk_fma_f32 v[98:99], v[98:99], v[108:109], v[86:87] op_sel:[0,0,0] op_sel_hi:[1,0,1]\n"
            "v_pk_fma_f32 v[100:101], v[100:101], v[108:109], v[86:87] op_sel:[0,1,0] op_sel_hi:[1,1,1]\n"
            "v_pk_fma_f32 v[102:103], v[102:103], v[110:111], v[86:87] op_sel:[0,0,0] op_sel_hi:[1,0,1]\n"
            "v_lshl_add_u32 v98, v99, 16, v98\n"
            "v_lshl_add_u32 v100, v101, 16, v100\n"
            "v_lshl_add_u32 v102, v103, 16, v102\n"
            "v_sub_u32 v98, v98, v86\n"
            "v_mad_i32_i16 v88, v99, v112, v88\n"
            "ds_add_u32 v112, v98 offset:65533\n"
            "v_sub_u32 v100, v100, v86\n"
            "v_mad_i32_i16 v88, v101, v113, v88\n"
            "ds_add_u32 v113, v100 offset:65533\n"
            "v_sub_u32 v102, v102, v86\n"
            "v_mad_i32_i16 v88, v103, v114, v88\n"
            "ds_add_u32 v114, v102 offset:65533\n"
            "v_and_b32 v104, 0x1fffc, v8\n"
            "v_and_b32 v105, 0x1fffc, v9\n"
            "v_and_b32 v106, 0x1fffc, v10\n"
            "s_swappc_b64 s[80:81], s[72:73]\n"
            "ds_read_b32 v93, v104\n"
            "ds_read_b32 v95, v105\n"
            "ds_read_b32 v97, v106\n"
            "s_waitcnt lgkmcnt(0)\n"
            "s_branch Ls0_%=\n"
            "Lchg1%=:\n"
            "s_add_u32 s92, s92, 1\n"
            "s_cmp_eq_u32 s92, 8\n"
            "s_cbranch_scc1 1f\n"
            "s_barrier\n"
            "s_mov_b32 m0, %[wb]\n"
            "s_nop 0\n"
            "ds_write_addtid_b32 v40 offset:0\n"
            "ds_write_addtid_b32 v41 offset:256\n"
            "ds_write_addtid_b32 v42 offset:512\n"
            "ds_write_addtid_b32 v43 offset:768\n"
            "ds_write_addtid_b32 v44 offset:16384\n"
            "ds_write_addtid_b32 v45 offset:16640\n"
            "ds_write_addtid_b32 v46 offset:16896\n"
            "ds_write_addtid_b32 v47 offset:17152\n"
            "ds_write_addtid_b32 v48 offset:32768\n"
            "ds_write_addtid_b32 v49 offset:33024\n"
            "ds_write_addtid_b32 v50 offset:33280\n"
            "ds_write_addtid_b32 v51 offset:33536\n"
            "ds_write_addtid_b32 v52 offset:49152\n"
            "ds_write_addtid_b32 v53 offset:49408\n"
            "ds_write_addtid_b32 v54 offset:49664\n"
            "ds_write_addtid_b32 v55 offset:49920\n"
            "s_add_u32 m0, %[wb], 16384\n"
            "s_nop 0\n"
            "ds_write_addtid_b32 v56 offset:49152\n"
            "ds_write_addtid_b32 v57 offset:49408\n"
            "ds_write_addtid_b32 v58 offset:49664\n"
            "ds_write_addtid_b32 v59 offset:49920\n"
            "1:\n"
            "s_setpc_b64 s[80:81]\n"
            "Lchg2%=:\n"
            "s_cmp_eq_u32 s92, 8\n"
            "s_cbranch_scc1 Lfin%=\n"
            "s_lshr_b64 s[76:77], s[76:77], 8\n"
            "s_and_b32 s75, s76, 255\n"
            "s_movk_i32 s74, 1000\n"
            "s_sub_i32 s79, s75, %[j0]\n"
            "s_max_i32 s79, s79, 8\n"
            "s_cmp_eq_u32 s92, 7\n s_cselect_b32 s79, 8, s79\n"
            "s_setprio 3\n"
            "s_waitcnt lgkmcnt(3)\n"
            "s_barrier\n"
            "s_setpc_b64 s[80:81]\n"
            "Lchg3%=:\n"
            "s_sub_u32 s74, s75, 7\n"
            "s_mov_b32 s79, 7\n"
            "s_cmp_eq_u32 s92, 7\n"
            "s_cbranch_scc1 1f\n"
            "s_add_u32 s94, s94, %[i4]\n"
            "buffer_load_dwordx4 v[40:43], %[tid16], %[rm], s94 offen\n"
            "s_add_u32 s96, s94, 16384\n"
            "buffer_load_dwordx4 v[44:47], %[tid16], %[rm], s96 offen\n"
            "s_add_u32 s96, s94, 32768\n"
            "buffer_load_dwordx4 v[48:51], %[tid16], %[rm], s96 offen\n"
            "s_add_u32 s96, s94, 49152\n"
            "buffer_load_dwordx4 v[52:55], %[tid16], %[rm], s96 offen\n"
            "s_add_u32 s96, s94, 65536\n"
            "buffer_load_dwordx4 v[56:59], %[tid16], %[rm], s96 offen\n"
            "s_branch 2f\n"
            "1:\n"
            "buffer_load_dword v49, v80, %[rd], 0 offen\n"
            "s_mov_b32 s74, 0\n"
            "buffer_load_dword v48, %[ed], %[rd], 0 offen " NTH "\n"
            "buffer_load_dword v50, %[ed], %[rd], %[e1] offen " NTH "\n"
            "v_lshlrev_b32 v52, 2, %[ed]\n"
            "s_lshl_b32 s96, %[e1], 2\n"
            "buffer_load_dwordx4 v[40:43], v52, %[rmm], 0 offen " NTH "\n"
            "buffer_load_dwordx4 v[44:47], v52, %[rmm], s96 offen " NTH "\n"
            "2:\n"
            "s_setpc_b64 s[80:81]\n"
            "Lfin%=:\n"
            "s_waitcnt lgkmcnt(0)\n"
            "s_barrier\n"
            "ds_read_b128 v[60:63], v89 offset:0\n"
            "ds_read_b128 v[64:67], v89 offset:16384\n"
            "ds_read_b128 v[68:71], v89 offset:32768\n"
            "ds_read_b128 v[72:75], v89 offset:49152\n"
            "ds_read_b128 v[76:79], v90\n"
            "v_subrev_u32 v107, 65533, v89\n"
            "v_mov_b32 v108, 0\n"
            "v_bfe_u32 v106, %[tid16], 9, 3\n"
            "v_cmp_eq_u32 vcc, %[q], v106\n"
            "v_bfrev_b32 v105, 1\n"
            "s_nop 0\n"
            "v_cndmask_b32 v105, %[tid16], v105, vcc\n"
            "s_mov_b32 s96, 16384\n s_mov_b32 s97, 32768\n s_mov_b32 s92, 49152\n s_mov_b32 s93, 65536\n"
            "s_waitcnt lgkmcnt(4)\n"
            "buffer_store_dwordx4 v[60:63], v105, %[rp0], 0 offen sc1\n"
            "s_waitcnt lgkmcnt(3)\n"
            "buffer_store_dwordx4 v[64:67], v105, %[rp0], s96 offen sc1\n"
            "s_waitcnt lgkmcnt(2)\n"
            "buffer_store_dwordx4 v[68:71], v105, %[rp0], s97 offen sc1\n"
            "s_waitcnt lgkmcnt(1)\n"
            "buffer_store_dwordx4 v[72:75], v105, %[rp0], s92 offen sc1\n"
            "s_waitcnt lgkmcnt(0)\n"
            "buffer_store_dwordx4 v[76:79], v105, %[rp0], s93 offen sc1\n"
            "v_add_u32 v112, 0x8000, v60\n"
            "v_add_u32 v106, 0, v107\n"
            "v_mad_i32_i16 v108, v112, v106, v108 op_sel:[1,0,0,0]\n"
            "v_add_u32 v112, 0x8000, v61\n"
            "v_add_u32 v106, 4, v107\n"
            "v_mad_i32_i16 v108, v112, v106, v108 op_sel:[1,0,0,0]\n"
            "v_add_u32 v112, 0x8000, v62\n"
            "v_add_u32 v106, 8, v107\n"
            "v_mad_i32_i16 v108, v112, v106, v108 op_sel:[1,0,0,0]\n"
            "v_add_u32 v112, 0x8000, v63\n"
            "v_add_u32 v106, 12, v107\n"
            "v_mad_i32_i16 v108, v112, v106, v108 op_sel:[1,0,0,0]\n"
            "v_add_u32 v107, 16384, v107\n"
            "v_add_u32 v112, 0x8000, v64\n"
            "v_add_u32 v106, 0, v107\n"
            "v_mad_i32_i16 v108, v112, v106, v108 op_sel:[1,0,0,0]\n"
            "v_add_u32 v112, 0x8000, v65\n"
            "v_add_u32 v106, 4, v107\n"
            "v_mad_i32_i16 v108, v112, v106, v108 op_sel:[1,0,0,0]\n"
            "v_add_u32 v112, 0x8000, v66\n"
            "v_add_u32 v106, 8, v107\n"
            "v_mad_i32_i16 v108, v112, v106, v108 op_sel:[1,0,0,0]\n"
            "v_add_u32 v112, 0x8000, v67\n"
            "v_add_u32 v106, 12, v107\n"
            "v_mad_i32_i16 v108, v112, v106, v108 op_sel:[1,0,0,0]\n"
            "v_add_u32 v107, 16384, v107\n"
            "v_add_u32 v112, 0x8000, v68\n"
            "v_add_u32 v106, 0, v107\n"
            "v_mad_i32_i16 v108, v112, v106, v108 op_sel:[1,0,0,0]\n"
            "v_add_u32 v112, 0x8000, v69\n"
            "v_add_u32 v106, 4, v107\n"
            "v_mad_i32_i16 v108, v112, v106, v108 op_sel:[1,0,0,0]\n"
            "v_add_u32 v112, 0x8000, v70\n"
            "v_add_u32 v106, 8, v107\n"
            "v_mad_i32_i16 v108, v112, v106, v108 op_sel:[1,0,0,0]\n"
            "v_add_u32 v112, 0x8000, v71\n"
            "v_add_u32 v106, 12, v107\n"
            "v_mad_i32_i16 v108, v112, v106, v108 op_sel:[1,0,0,0]\n"
            "v_add_u32 v107, 16384, v107\n"
            "v_add_u32 v112, 0x8000, v72\n"
            "v_add_u32 v106, 0, v107\n"
            "v_mad_i32_i16 v108, v112, v106, v108 op_sel:[1,0,0,0]\n"
            "v_add_u32 v112, 0x8000, v73\n"
            "v_add_u32 v106, 4, v107\n"
            "v_mad_i32_i16 v108, v112, v106, v108 op_sel:[1,0,0,0]\n"
            "v_add_u32 v112, 0x8000, v74\n"
            "v_add_u32 v106, 8, v107\n"
            "v_mad_i32_i16 v108, v112, v106, v108 op_sel:[1,0,0,0]\n"
            "v_add_u32 v112, 0x8000, v75\n"
            "v_add_u32 v106, 12, v107\n"
            "v_mad_i32_i16 v108, v112, v106, v108 op_sel:[1,0,0,0]\n"
            "v_add_u32 v107, 16384, v107\n"
            "v_add_u32 v112, 0x8000, v76\n"
            "v_add_u32 v106, 0, v107\n"
            "v_mad_i32_i16 v108, v112, v106, v108 op_sel:[1,0,0,0]\n"
            "v_add_u32 v112, 0x8000, v77\n"
            "v_add_u32 v106, 4, v107\n"
            "v_mad_i32_i16 v108, v112, v106, v108 op_sel:[1,0,0,0]\n"
            "v_add_u32 v112, 0x8000, v78\n"
            "v_add_u32 v106, 8, v107\n"
            "v_mad_i32_i16 v108, v112, v106, v108 op_sel:[1,0,0,0]\n"
            "v_add_u32 v112, 0x8000, v79\n"
            "v_add_u32 v106, 12, v107\n"
            "v_mad_i32_i16 v108, v112, v106, v108 op_sel:[1,0,0,0]\n"
            "v_add_u32 v107, 16384, v107\n"
            "v_sub_u32 v108, v88, v108\n"
            "v_mov_b32 v106, %[wb]\n"
            "v_mov_b32 v107, %[bad]\n"
            "v_add_u32_dpp v108, v108, v108 row_shr:1 row_mask:0xf bank_mask:0xf bound_ctrl:1\n"
            "s_nop 1\n"
            "v_add_u32_dpp v108, v108, v108 row_shr:2 row_mask:0xf bank_mask:0xf bound_ctrl:1\n"
            "s_nop 1\n"
            "v_add_u32_dpp v108, v108, v108 row_shr:4 row_mask:0xf bank_mask:0xf bound_ctrl:1\n"
            "s_nop 1\n"
            "v_add_u32_dpp v108, v108, v108 row_shr:8 row_mask:0xf bank_mask:0xf bound_ctrl:1\n"
            "s_nop 1\n"
            "v_add_u32_dpp v108, v108, v108 row_bcast:15 row_mask:0xa bank_mask:0xf\n"
            "s_nop 1\n"
            "v_add_u32_dpp v108, v108, v108 row_bcast:31 row_mask:0xc bank_mask:0xf\n"
            "s_nop 1\n"
            "v_lshrrev_b32 v106, 7, v106\n"
            "s_mov_b32 exec_lo, 0\n s_mov_b32 exec_hi, 0x80000000\n"
            "ds_write2_b32 v106, v108, v107 offset1:1\n"
            "s_mov_b64 exec, -1\n"
            "s_waitcnt vmcnt(5)\n"
            "s_branch Lend2%=\n"
            "Lend%=:\n"
            "s_waitcnt vmcnt(0)\n"
            "Lend2%=:\n"
            "s_setprio 0\n"
            "s_waitcnt lgkmcnt(0)\n"

            : [mode] "=&s"(mode), [bad] "=&s"(bad), [exs] "=&v"(exs), [tid16] "+v"(t16), "+v"(tc2), "=&{v[40:43]}"(mm[0]), "=&{v[44:47]}"(mm[1]), "=&{v[48:51]}"(dd)
            : [rs] "s"(rs), [rm] "s"(rm), [rp0] "s"(rp0), [rt] "s"(rt), [i4] "s"(LMAX * 4), [c4] "s"(a_.c4), [pb] "s"(81920 + wave * 5120), [wb] "s"(wave * 1024), [q] "s"(q), [j0] "s"(J0),
              [rmm] "s"(rmm), [rd] "s"(rd), [e1] "s"(128 * 32 * 128 * 2), [ed] "v"((((wave * 8 + q) * 32 + b) * 128 + lane * 2) * 2),
              [fof] "s"((NT * NWS * 4 + cp * (P1_GRID * 2)) * 4), [dumu] "s"(ly.dumu), [lmul] "n"(LMUL), [lmask] "n"(LMASK),
              [w0] "s"(a_.centers), [w1] "s"(a_.w_sbf), [w2] "s"(a_.w_rbf), [w3] "s"(a_.w_up),
              [tof] "v"((w * NT + t0) * 16), [fb] "v"(pairidx(w) * (ly.F0 * 256)), [xb] "v"(logbase(b >> 1, w, P_XO))
            : "memory", "vcc", "scc", "m0", "s66", "s67", "s70", "s71", "s72", "s73", "s74", "s75", "s76", "s77", "s79", "s80", "s81", "s82",
              "s84", "s85", "s86", "s87", "s88", "s89", "s90", "s91", "s92", "s93", "s94", "s96", "s97",
              "v8", "v9", "v10", "v11", "v12", "v13", "v14", "v15", "v16", "v17", "v18", "v19", "v20", "v21", "v22", "v23", "v24", "v25", "v26", "v27", "v28", "v29", "v30", "v31", "v32", "v33", "v34", "v35", "v36", "v37", "v38", "v39", "v52", "v53", "v54", "v55", "v56", "v57", "v58", "v59", "v60", "v61", "v62", "v63", "v64", "v65", "v66", "v67", "v68", "v69", "v70", "v71", "v72", "v73", "v74", "v75", "v76", "v77", "v78", "v79", "v80", "v81", "v82", "v83", "v84", "v85", "v86", "v87", "v88", "v89", "v90", "v91", "v92", "v93", "v94", "v95", "v96", "v97", "v98", "v99", "v100", "v101", "v102", "v103", "v104", "v105", "v106", "v107", "v108", "v109", "v110", "v111", "v112", "v113", "v114");
    }
    const int tid2 = (int)(t16 >> 4);
    kaptr ka = (kaptr)__builtin_amdgcn_kernarg_segment_ptr();
    asm volatile("" : "+s"(ka));
    unsigned long long* wm = (unsigned long long*)(ka->wgmode + b * 32);
    const int E = ka->E;
    const unsigned nblk = ka->O >> 7;
    const int ln = tid2 & 63, wv = __builtin_amdgcn_readfirstlane(tid2 >> 6);
    const bool two = (16 + wv) * 8 + q < nblk;
    float g[2][2][4], h[2][2][4];
    auto gh1 = [&](int p) __attribute__((always_inline)) {
        asm volatile("" : "+v"(dd));
        edge_gh(lo_bf(p ? dd.z : dd.x), ka->centers, ka->w_sbf, ka->w_rbf, g[p][0], h[p][0]);
        edge_gh(lo_bf(p ? dd.w : dd.y), ka->centers, ka->w_sbf, ka->w_rbf, g[p][1], h[p][1]);
#pragma unroll
        for (int i = 0; i < 8; i++) asm volatile("" : "+v"(g[p][i >> 2][i & 3]), "+v"(h[p][i >> 2][i & 3]));
    };
    constexpr unsigned GW = 1, M1 = 0xf, M2 = 0xff00, P1 = 0, PT = 0;
    v2u own[2] = {{0, 0}, {0, 0}};
    constexpr int LM = 0;
    if (mode == 0) {
        if (LM) {
            const v4i_rs rmm = mk_rsrc(ka->m, E * 8);
            const unsigned e8 = (((wv * 8 + q) * 32 + b) * 128 + ln * 2) * 8;
            asm volatile("buffer_load_dwordx4 %0, %2, %3, 0 offen " NTH "\n buffer_load_dwordx4 %1, %2, %3, %4 offen " NTH
                         : "=&v"(mm[0]), "=&v"(mm[1]) : "v"(e8), "s"(rmm), "s"(128 * 32 * 128 * 8) : "memory");
        }
        if (AV & 8) {
            if (E & 1) {
                const unsigned e = ((wv * 8 + q) * 32 + b) * 128 + ln * 2;
                if (e == E - 1) dd.x = ka->dist[e];
                if (e + 128 * 32 * 128 == E - 1) dd.z = ka->dist[E - 1];
            }
            dd.y = dd.x >> 16; dd.w = dd.z >> 16;
        }
        if ((M1 >> wv) & 1) gh1(0);
        if (P1 == 1 && two) gh1(1);
        own[0] = *(const v2u*)(lds + OBASE + (wv * 8 + q) * 128 + ln * 2);
        if (two) own[1] = *(const v2u*)(lds + OBASE + ((16 + wv) * 8 + q) * 128 + ln * 2);
        asm volatile("s_waitcnt vmcnt(%0)" :: "n"(LM ? 2 : 0) : "memory");
        __syncthreads();
        const v2u cs = *(const v2u*)(lds + (ln & 15) * 2);
        if ((__builtin_amdgcn_readlane(row_scan_add(cs.x), 15) != 0 || __ballot(cs.y != 0)) && !(AV & 64)) mode = 1;
    }
    if (mode != 0) {
        if (LM) asm volatile("s_waitcnt vmcnt(0)" : "+v"(mm[0]), "+v"(mm[1]) :: "memory");
        acc_fallback(lds, mode, *ka, tid2, b, qt, cp);
        asm volatile("s_waitcnt vmcnt(0)" ::: "memory");
        __syncthreads();
        if (tid2 == PT) { __builtin_amdgcn_fence(__ATOMIC_RELEASE, "agent"); asm volatile("s_waitcnt vmcnt(0)" ::: "memory"); }
        const __amdgpu_buffer_rsrc_t rmm = __builtin_amdgcn_make_buffer_rsrc((void*)ka->m, (short)0, E * 8, 0x00020000);
        const __amdgpu_buffer_rsrc_t rdd = __builtin_amdgcn_make_buffer_rsrc((void*)ka->dist, (short)0, E * 2, 0x00020000);
        const unsigned e = ((wv * 8 + q) * 32 + b) * 128 + ln * 2, e1 = e + 128 * 32 * 128;
        mm[0] = __builtin_amdgcn_raw_buffer_load_b128(rmm, e * 8, 0, 0); mm[1] = __builtin_amdgcn_raw_buffer_load_b128(rmm, e1 * 8, 0, 0);
        dd.x = __builtin_amdgcn_raw_buffer_load_b16(rdd, e * 2, 0, 0); dd.y = __builtin_amdgcn_raw_buffer_load_b16(rdd, e * 2 + 2, 0, 0);
        dd.z = __builtin_amdgcn_raw_buffer_load_b16(rdd, e1 * 2, 0, 0); dd.w = __builtin_amdgcn_raw_buffer_load_b16(rdd, e1 * 2 + 2, 0, 0);
        own[0].x = own[0].y = own[1].x = own[1].y = 0;
    }
    const unsigned* prb = ka->praw + (size_t)b * ka->O; unsigned short* outp = ka->out;
    int EP = ka->EP;
    unsigned wu[8];
#pragma unroll
    for (int i = 0; i < 8; i++) wu[i] = ((cptr)ka->w_up)[i];
    if (tid2 == PT) {
        const unsigned long long add = (1u | (mode ? 256u << q : 0u)) | (qt == 0 ? (unsigned long long)(__builtin_amdgcn_readfirstlane(exs) & 0xFFFF) << (32 + 16 * cp) : 0ull);
        unsigned long long v = __hip_atomic_fetch_add(wm, add, __ATOMIC_RELAXED, __HIP_MEMORY_SCOPE_AGENT) + add;
        for (int spin = 0; spin < (1 << 22) && (v & 255) != 8; spin++) {
            v = __hip_atomic_load(wm, __ATOMIC_RELAXED, __HIP_MEMORY_SCOPE_AGENT);
            if ((v & 255) != 8) __builtin_amdgcn_s_sleep(2);
        }
        if (v & 0xFF00) { __builtin_amdgcn_fence(__ATOMIC_ACQUIRE, "agent"); asm volatile("s_waitcnt vmcnt(0)" ::: "memory"); }
        lds[64] = ((unsigned)v >> 8) & 255; lds[65] = (unsigned)(v >> 32);
    }
    if (mode == 0 && ((M2 >> wv) & 1)) gh1(0);
    if (P1 == 2 && mode == 0 && two) gh1(1);
    asm volatile("" : "+s"(prb), "+s"(outp), "+s"(EP), "+s"(wu[0]), "+s"(wu[1]), "+s"(wu[2]), "+s"(wu[3]), "+s"(wu[4]), "+s"(wu[5]), "+s"(wu[6]), "+s"(wu[7]));
    const __amdgpu_buffer_rsrc_t rpr = __builtin_amdgcn_make_buffer_rsrc((void*)prb, (short)0, (7 * EP + (int)(nblk << 7)) * 4, 0x00020000);
    const __amdgpu_buffer_rsrc_t rou = __builtin_amdgcn_make_buffer_rsrc((void*)outp, (short)0, E * 8, 0x00020000);
    __syncthreads();
    v2u w[2][8];
#pragma unroll
    for (int p = 0; p < 2; p++) {
        if (p == 0 || two) {
#pragma unroll
            for (int j = 0; j < 8; j++)
                w[p][j] = __builtin_amdgcn_raw_buffer_load_b64(rpr, (((p * 16 + wv) * 8 + q) * 128 + ln * 2) * 4, j == q ? (int)OOB : j * EP * 4, AV & 4 ? 16 : PR_AUX);
        }
    }
    if (GW) {
        asm volatile("" ::: "memory");
        if (mode != 0 || !(((M1 | M2) >> wv) & 1)) gh1(0);
        if (two && (mode != 0 || P1 == 0)) gh1(1);
    }
    if (LM) asm volatile("s_waitcnt vmcnt(8)" : "+v"(mm[0]), "+v"(mm[1]) :: "memory");
    const unsigned fbm = __builtin_amdgcn_readfirstlane(lds[64]), exw = __builtin_amdgcn_readfirstlane(lds[65]);
    float inv[4];
#pragma unroll
    for (int k = 0; k < 4; k++) inv[k] = __uint_as_float(((exw >> (8 * k)) & 255) << 23) * (1.0f / 511.0f);
#pragma unroll
    for (int p = 0; p < 2; p++) {
        const unsigned blk = (p * 16 + wv) * 8 + q;
        if (p == 0 || two) {
            int s[2][4];
            {
                const int l0 = (int)(short)(own[p].x & 0xFFFF), h0 = (int)(own[p].x + 0x8000u) >> 16, l1 = (int)(short)(own[p].y & 0xFFFF), h1 = (int)(own[p].y + 0x8000u) >> 16;
                s[0][0] = cp ? 0 : l0; s[0][1] = cp ? 0 : h0; s[0][2] = cp ? l0 : 0; s[0][3] = cp ? h0 : 0;
                s[1][0] = cp ? 0 : l1; s[1][1] = cp ? 0 : h1; s[1][2] = cp ? l1 : 0; s[1][3] = cp ? h1 : 0;
            }
#pragma unroll
            for (int j = 0; j < 8; j++) {
                s[0][(j & 1) * 2] += (int)(short)(w[p][j].x & 0xFFFF); s[0][(j & 1) * 2 + 1] += (int)(w[p][j].x + 0x8000u) >> 16;
                s[1][(j & 1) * 2] += (int)(short)(w[p][j].y & 0xFFFF); s[1][(j & 1) * 2 + 1] += (int)(w[p][j].y + 0x8000u) >> 16;
            }
            float a[2][4];
#pragma unroll
            for (int i = 0; i < 2; i++)
#pragma unroll
                for (int k = 0; k < 4; k++) a[i][k] = s[i][k] ? (float)s[i][k] * inv[k] : 0.f;
            if (fbm && blk < nblk) {
                const float* part = ka->part + (size_t)b * ka->O + blk * 128 + ln * 2;
#pragma unroll 1
                for (int j = 0; j < 8; j++)
                    if ((fbm >> j) & 1) {
#pragma unroll
                        for (int i = 0; i < 2; i++) {
                            const float x = part[(size_t)(j * 2) * EP + i], y = part[(size_t)(j * 2 + 1) * EP + i];
                            if (j & 1) { a[i][2] += x; a[i][3] += y; } else { a[i][0] += x; a[i][1] += y; }
                        }
                    }
            }
            const v2u r0 = edge_out(a[0], g[p][0], h[p][0], mm[p].x, mm[p].y, wu), r1 = edge_out(a[1], g[p][1], h[p][1], mm[p].z, mm[p].w, wu);
            const v4u r = {r0.x, r0.y, r1.x, r1.y};
            __builtin_amdgcn_raw_buffer_store_b128(r, rou, ((blk * 32 + b) * 128 + ln * 2) * 8, 0, OUT_AUX);
        }
    }
}

extern "C" __global__ __launch_bounds__(256) void k_naive(const int* __restrict__ idx_in, const int* __restrict__ idx_out,
                                               const unsigned short* __restrict__ angle, const unsigned short* __restrict__ ang_freq,
                                               const unsigned short* __restrict__ m, const unsigned short* __restrict__ w_down,
                                               float* __restrict__ part, int T, int EP) {
    const float freq = bf2f(ang_freq[0]);
    for (long long t = (long long)blockIdx.x * 256 + threadIdx.x; t < T; t += (long long)gridDim.x * 256) {
        v2u mv = ((const v2u*)m)[idx_in[t]];
        float a[4] = {lo_bf(mv.x), hi_bf(mv.x), lo_bf(mv.y), hi_bf(mv.y)};
        float c = __cosf(freq * bf2f(angle[t]));
        int eo = idx_out[t];
#pragma unroll
        for (int d = 0; d < 4; d++) {
            float s = 0.f;
#pragma unroll
            for (int k = 0; k < 4; k++) s += a[k] * bf2f(w_down[k * 4 + d]);
            atomicAdd(&part[(size_t)d * EP + eo], s * c);
        }
    }
}

__global__ __launch_bounds__(256) void k_final(const unsigned short* __restrict__ m, const unsigned short* __restrict__ dist,
                                               const unsigned short* __restrict__ centers, const unsigned short* __restrict__ w_rbf,
                                               const unsigned short* __restrict__ w_sbf, const unsigned short* __restrict__ w_up,
                                               const float* __restrict__ part, unsigned short* __restrict__ out, int E, int EP) {
    const int e = blockIdx.x * 256 + threadIdx.x;
    if (e >= E) return;
    float a[4], g[4], h[4];
#pragma unroll
    for (int k = 0; k < 4; k++) a[k] = part[(size_t)k * EP + e];
    edge_gh(bf2f(dist[e]), centers, w_sbf, w_rbf, g, h);
    const v2u mv = ((const v2u*)m)[e];
    unsigned wu[8];
#pragma unroll
    for (int i = 0; i < 8; i++) wu[i] = ((cptr)w_up)[i];
    ((v2u*)out)[e] = edge_out(a, g, h, mv.x, mv.y, wu);
}

extern "C" void kernel_launch(void* const* d_in, const int* in_sizes, int n_in,
                              void* d_out, int out_size, void* d_ws, size_t ws_size,
                              hipStream_t stream) {
    const unsigned short* m = (const unsigned short*)d_in[0];
    const int* idx_in = (const int*)d_in[1];
    const int* idx_out = (const int*)d_in[2];
    const unsigned short* dist = (const unsigned short*)d_in[3];
    const unsigned short* angle = (const unsigned short*)d_in[4];
    const unsigned short* centers = (const unsigned short*)d_in[5];
    const unsigned short* ang_freq = (const unsigned short*)d_in[6];
    const unsigned short* w_rbf = (const unsigned short*)d_in[7];
    const unsigned short* w_sbf = (const unsigned short*)d_in[8];
    const unsigned short* w_down = (const unsigned short*)d_in[9];
    const unsigned short* w_up = (const unsigned short*)d_in[10];
    const int E = in_sizes[0] / 4;
    const int T = in_sizes[1];
    const int NR = T / (P1_GRID * CH);
    const int per = ((T - NR * (P1_GRID * CH) + P1_GRID - 1) / P1_GRID + 3) / 4 * 4;
    const unsigned O = (unsigned)(((E + 127) / 128 + 31) / 32) * 128;
    const unsigned I = (unsigned)(E + 31) / 32;
    const int EP = (int)O * 32;
    Lay ly;
    const unsigned meanr = (unsigned)(((size_t)NR * CH + per) / NT);
    ly.F0 = meanr >= 100 ? 40 : 72;
    if (LA > 3) ly.F0 = (ly.F0 + (1 << LA) - 1) >> LA << LA;
    ly.fcap = ly.F0 / 4 * 505;
    ly.cap = 768;
    char* ws = (char*)d_ws;
    size_t o = 0;
    ly.dumu = ly.cap * 32768;
    const size_t recsz = 0x200000 + ((size_t)ly.dumu + 64) * 16;
    char* rec = ws;
    o += recsz;
    unsigned* praw = (unsigned*)(ws + o); o += (size_t)8 * EP * 4; o = (o + 255) / 256 * 256;
    unsigned* wgmode = (unsigned*)(ws + o); o += 8192;
    unsigned* mdp = (unsigned*)(ws + o); o += (size_t)2 * 32 * LMAX * 4 + 64 * LMAX;
    unsigned* tab = (unsigned*)(ws + o); o += (size_t)NT * NWS * 16;
    unsigned* flags = (unsigned*)(ws + o); o += FL_SIZE * 4; o = (o + 255) / 256 * 256;
    float* part = (float*)(ws + o); o += (size_t)16 * EP * 4;
    const bool ok = E >= 16384 && O <= LMAX && I <= LMAX && T > 0 && meanr <= 180 && o <= ws_size;
    if (!ok) {
        part = (float*)ws;
        const int EQ = (E + 3) / 4 * 4;
        (void)hipMemsetAsync(part, 0, (size_t)4 * EQ * 4, stream);
        if (T > 0) k_naive<<<2048, 256, 0, stream>>>(idx_in, idx_out, angle, ang_freq, m, w_down, part, T, EQ);
        k_final<<<(E + 255) / 256, 256, 0, stream>>>(m, dist, centers, w_rbf, w_sbf, w_up, part, (unsigned short*)d_out, E, EQ);
        return;
    }
    k_bin<<<P1_GRID, P1_THREADS, 0, stream>>>(idx_in, idx_out, angle, ang_freq, m, w_down, mdp, E, T, rec, tab, flags, NR, per, ly, wgmode, dist);
    AccArgs aa;
    aa.rec = rec; aa.tab = tab; aa.flags = flags; aa.mdp = mdp; aa.part = part; aa.praw = praw; aa.wgmode = wgmode;
    aa.idx_in = idx_in; aa.idx_out = idx_out; aa.angle = angle; aa.ang_freq = ang_freq; aa.m = m; aa.w_down = w_down;
    aa.EP = EP; aa.O = O; aa.T = T; aa.c4 = (float)((double)E / 144.0);
    aa.dist = dist; aa.centers = centers; aa.w_rbf = w_rbf; aa.w_sbf = w_sbf; aa.w_up = w_up; aa.out = (unsigned short*)d_out; aa.E = E; aa.unused = 0;
    aa.ly = ly;
    k_acc<<<256, P2_THREADS, 0, stream>>>(aa);
}
```
